```python
import torch

def _softmax(x, axis=-1):
    return torch.softmax(x, dim=axis)

def _gelu(x):
    return torch.nn.functional.gelu(x, approximate='tanh')

def _silu(x):
    return torch.nn.functional.silu(x)

def _sigmoid(x):
    return torch.sigmoid(x)

def _layernorm(x, axis=-1, eps=1e-05):
    m = x.mean(dim=axis, keepdim=True)
    v = x.var(dim=axis, keepdim=True, unbiased=False)
    return (x - m) / torch.sqrt(v + eps)

def _scatter_add(tgt, idx, src, axis=0):
    return tgt.clone().index_add_(axis, idx, src)

def _segment_sum(src, seg, n):
    out = torch.zeros((n,) + src.shape[1:], dtype=src.dtype)
    return out.index_add_(0, seg, src)

def _pad(x, pw):
    flat = tuple((v for pair in reversed(pw) for v in pair))
    return torch.nn.functional.pad(x, flat)
kernel_name = 'adaLN_cond__sliding_block_cached__glu_gated_reglu__prompt'

def setup_inputs():

    _w = lambda a: (lambda t: t.to(torch.bfloat16) if t.is_floating_point() else t)(torch.as_tensor(a))

    def _A_setup():
        B, Sq, D = (1, 2048, 576)
        Hq, Hkv, Sk, Dh = (32, 16, 2048, 128)
        assert Hq % Hkv == 0, (Hq, Hkv)
        return {'x': _w((lambda *s: torch.randn(*s, dtype=torch.bfloat16) * 0.05)(B, Sq, D)), 'k_cache': _w((lambda *s: torch.randn(*s, dtype=torch.bfloat16) * 1.6)(B, Hkv, Sk, Dh)), 'v_cache': _w((lambda *s: torch.randn(*s, dtype=torch.bfloat16) * 0.8)(B, Hkv, Sk, Dh)), 'wq': _w((lambda *s: torch.randn(*s, dtype=torch.bfloat16) * 1.6)(D, Hq * Dh) * D ** (-0.5)), 'wo': _w((lambda *s: torch.randn(*s, dtype=torch.bfloat16) * 0.8)(Hq * Dh, D) * (Hq * Dh) ** (-0.5))}
    _A_in = _A_setup()

    def _B_setup():
        B, S, D, F = (1, 2048, 576, 4608)
        return {'x': _w((lambda *s: torch.randn(*s, dtype=torch.bfloat16) * 0.05)(B, S, D)), 'W_gate': _w((lambda *s: torch.randn(*s, dtype=torch.bfloat16) * 0.4)(D, F) * (2.0 / D) ** 0.5), 'W_up': _w((lambda *s: torch.randn(*s, dtype=torch.bfloat16) * 0.4)(D, F) * (2.0 / D) ** 0.5), 'W_down': _w((lambda *s: torch.randn(*s, dtype=torch.bfloat16) * 0.4)(F, D) * (1.0 / F) ** 0.5)}
    _B_in = _B_setup()
    return {'A_x': _A_in['x'], 'A_k_cache': _A_in['k_cache'], 'A_v_cache': _A_in['v_cache'], 'A_wq': _A_in['wq'], 'A_wo': _A_in['wo'], 'B_x': _B_in['x'], 'B_W_gate': _B_in['W_gate'], 'B_W_up': _B_in['W_up'], 'B_W_down': _B_in['W_down'], 'c': _w((lambda *s: torch.randn(*s, dtype=torch.bfloat16) * 0.2)(1, 576) * 8.0), 'w_mod': _w((lambda *s: torch.randn(*s, dtype=torch.bfloat16) * 0.2)(576, 6 * 576) * 576 ** (-0.5) * 8.0)}

def reference(A_x, A_k_cache, A_v_cache, A_wq, A_wo, B_x, B_W_gate, B_W_up, B_W_down, c, w_mod):

    def _A_ref(x, k_cache, v_cache, wq, wo):
        B, Sq, D = x.shape
        Hq, Dh = (32, 128)
        Hkv, Sk = (k_cache.shape[1], k_cache.shape[2])
        W = min(4096, max(256, Sk // 4))
        rep = Hq // Hkv
        q = (lambda x, *d: x.permute(*d))((x @ wq).reshape(B, Sq, Hq, Dh), 0, 2, 1, 3)
        k = (lambda x, n, axis: torch.repeat_interleave(x, n, dim=axis))(k_cache[:, :, Sk - W:, :], rep, axis=1)
        v = (lambda x, n, axis: torch.repeat_interleave(x, n, dim=axis))(v_cache[:, :, Sk - W:, :], rep, axis=1)
        scores = torch.einsum('bhqd,bhkd->bhqk', q, k) * Dh ** (-0.5)
        p = _softmax(scores, axis=-1)
        o = torch.einsum('bhqk,bhkd->bhqd', p, v)
        return (lambda x, *d: x.permute(*d))(o, 0, 2, 1, 3).reshape(B, Sq, Hq * Dh) @ wo

    def _B_ref(x, W_gate, W_up, W_down):
        gate = torch.einsum('bsd,df->bsf', x, W_gate)
        up = torch.einsum('bsd,df->bsf', x, W_up)
        gate = gate * (lambda x, dt: x.to(dt))(gate > 0, gate.dtype)
        h = gate * up
        return torch.einsum('bsf,fd->bsd', h, W_down)
    x = A_x
    mod = (_silu(c) @ w_mod).reshape(c.shape[0], 6, 1, -1)
    sa, ca, ga, sb, cb, gb = (mod[:, 0].clone(), mod[:, 1].clone(), mod[:, 2].clone(), mod[:, 3].clone(), mod[:, 4].clone(), mod[:, 5].clone())
    h = _layernorm(x, axis=-1) * (1 + ca) + sa
    x = x + ga * _A_ref(h, A_k_cache, A_v_cache, A_wq, A_wo)
    h = _layernorm(x, axis=-1) * (1 + cb) + sb
    x = x + gb * _B_ref(h, B_W_gate, B_W_up, B_W_down)
    return x
```

```cpp
#include <hip/hip_runtime.h>
#include <hip/hip_bf16.h>
typedef unsigned short u16;
__device__ __forceinline__ float b2f(u16 v){ return __uint_as_float(((unsigned)v)<<16); }
__device__ __forceinline__ u16 f2b(float f){ unsigned u=__float_as_uint(f); u += 0x7fffu + ((u>>16)&1u); return (u16)(u>>16); }
constexpr int S=2048, D=576, HQ=32, HKV=16, SK=2048, DH=128, W=512, F=4608;

__global__ void k_mod(const u16* c, const u16* wm, float* mod){
  int n = blockIdx.x*blockDim.x+threadIdx.x; if(n>=6*D) return;
  float acc=0; for(int k=0;k<D;k++){ float cv=b2f(c[k]); float s=cv/(1.f+expf(-cv)); acc += s*b2f(wm[(size_t)k*6*D+n]); }
  mod[n]=acc;
}
template<typename T> __global__ void k_ln(const T* x, const float* sh, const float* sc, float* h){
  int r=blockIdx.x; __shared__ float red[2];
  __shared__ float xs[D];
  for(int i=threadIdx.x;i<D;i+=blockDim.x){ if constexpr(sizeof(T)==2) xs[i]=b2f(x[(size_t)r*D+i]); else xs[i]=x[(size_t)r*D+i]; }
  __syncthreads();
  if(threadIdx.x==0){ float m=0; for(int i=0;i<D;i++) m+=xs[i]; m/=D; float v=0; for(int i=0;i<D;i++) v+=(xs[i]-m)*(xs[i]-m); v/=D; red[0]=m; red[1]=1.f/sqrtf(v+1e-5f);}
  __syncthreads();
  for(int i=threadIdx.x;i<D;i+=blockDim.x) h[(size_t)r*D+i]=b2f(f2b((xs[i]-red[0])*red[1]*(1.f+sc[i])+sh[i]));
}
__global__ void k_gemm(const float* A, const u16* B, float* C, int M,int N,int K){
  __shared__ float As[16][17], Bs[16][17];
  int tx=threadIdx.x, ty=threadIdx.y; int r=blockIdx.y*16+ty, c=blockIdx.x*16+tx; float acc=0;
  for(int k0=0;k0<K;k0+=16){ As[ty][tx]=A[(size_t)r*K+k0+tx]; Bs[ty][tx]=b2f(B[(size_t)(k0+ty)*N+c]); __syncthreads();
    for(int k=0;k<16;k++) acc+=As[ty][k]*Bs[k][tx]; __syncthreads(); }
  C[(size_t)r*N+c]=acc;
}
__global__ void k_attn(const float* q, const u16* kc, const u16* vc, float* o){
  int r=blockIdx.x, h=blockIdx.y, kv=h/2; __shared__ float qs[DH], p[W], red[1];
  int t=threadIdx.x;
  qs[t]=b2f(f2b(q[(size_t)r*HQ*DH+h*DH+t])); __syncthreads();
  for(int j=t;j<W;j+=128){ const u16* kp=kc+((size_t)kv*SK+SK-W+j)*DH; float a=0; for(int d=0;d<DH;d++) a+=qs[d]*b2f(kp[d]); p[j]=a*0.08838834764831845f; }
  __syncthreads();
  if(t==0){ float m=-1e30f; for(int j=0;j<W;j++) m=fmaxf(m,p[j]); float s=0; for(int j=0;j<W;j++){ p[j]=expf(p[j]-m); s+=p[j]; } red[0]=1.f/s; }
  __syncthreads();
  float a=0; for(int j=0;j<W;j++) a+=p[j]*b2f(vc[((size_t)kv*SK+SK-W+j)*DH+t]);
  o[(size_t)r*HQ*DH+h*DH+t]=b2f(f2b(a*red[0]));
}
__global__ void k_res1(const u16* x, const float* g, const float* a, float* x1){ int i=blockIdx.x*256+threadIdx.x; x1[i]=b2f(x[i])+g[i%D]*a[i]; }
__global__ void k_glu(const float* g, const float* u, float* h){ size_t i=(size_t)blockIdx.x*256+threadIdx.x; float gv=g[i]; h[i]=b2f(f2b((gv>0?gv:0.f)*u[i])); }
__global__ void k_res2(const float* x1, const float* g, const float* a, u16* out){ int i=blockIdx.x*256+threadIdx.x; out[i]=f2b(x1[i]+g[i%D]*a[i]); }

extern "C" void kernel_launch(void* const* d_in, const int* in_sizes, int n_in, void* d_out, int out_size, void* d_ws, size_t ws_size, hipStream_t stream){
  const u16* x=(const u16*)d_in[0]; const u16* kc=(const u16*)d_in[1]; const u16* vc=(const u16*)d_in[2]; const u16* wq=(const u16*)d_in[3]; const u16* wo=(const u16*)d_in[4];
  const u16* wg=(const u16*)d_in[6]; const u16* wu=(const u16*)d_in[7]; const u16* wd=(const u16*)d_in[8]; const u16* c=(const u16*)d_in[9]; const u16* wm=(const u16*)d_in[10];
  float* ws=(float*)d_ws; float* mod=ws; float* h=mod+4096; float* q=h+(size_t)S*D; float* o=q+(size_t)S*4096; float* a=o+(size_t)S*4096; float* x1=a+(size_t)S*D; float* g=x1+(size_t)S*D; float* u=g+(size_t)S*F; float* hh=q;
  k_mod<<<(6*D+63)/64,64,0,stream>>>(c,wm,mod);
  k_ln<u16><<<S,64,0,stream>>>(x,mod,mod+D,h);
  k_gemm<<<dim3(4096/16,S/16),dim3(16,16),0,stream>>>(h,wq,q,S,4096,D);
  k_attn<<<dim3(S,HQ),128,0,stream>>>(q,kc,vc,o);
  k_gemm<<<dim3(D/16,S/16),dim3(16,16),0,stream>>>(o,wo,a,S,D,4096);
  k_res1<<<S*D/256,256,0,stream>>>(x,mod+2*D,a,x1);
  k_ln<float><<<S,64,0,stream>>>(x1,mod+3*D,mod+4*D,h);
  k_gemm<<<dim3(F/16,S/16),dim3(16,16),0,stream>>>(h,wg,g,S,F,D);
  k_gemm<<<dim3(F/16,S/16),dim3(16,16),0,stream>>>(h,wu,u,S,F,D);
  k_glu<<<(size_t)S*F/256,256,0,stream>>>(g,u,hh);
  k_gemm<<<dim3(D/16,S/16),dim3(16,16),0,stream>>>(hh,wd,a,S,D,F);
  k_res2<<<S*D/256,256,0,stream>>>(x1,mod+5*D,a,(u16*)d_out);
}
```

```cpp
#include <hip/hip_runtime.h>
#include <hip/hip_bf16.h>
typedef unsigned short u16;
typedef unsigned int u32;

__global__ __launch_bounds__(512) void k_main(char* ws,const u16* x,const void* wq,const void* kc,const void* vc,const void* wo,
     const void* wg,const void* wu,const void* wd,void* out,const u16* c,const u16* wm){
  __shared__ __attribute__((aligned(16))) char lds[163840];
  u32 tid=threadIdx.x,bid=blockIdx.x,lb=(u32)(size_t)lds;

  asm volatile(
"v_lshrrev_b32 v2, 6, v0\n"
"v_and_b32 v1, 63, v0\n"
"v_readfirstlane_b32 s16, v2\n"
"s_and_b32 s19, s14, 7\n"
"s_lshr_b32 s18, s14, 3\n"
"s_and_b32 s17, s16, 3\n"
"s_lshr_b32 s43, s16, 2\n"
"s_lshr_b32 s40, s16, 1\n"
"s_and_b32 s40, s40, 1\n"
"s_lshl_b32 s40, s40, 3\n"
"v_xor_b32 v2, s40, v1\n"
"v_lshlrev_b32 v2, 4, v2\n"
"s_lshl_b32 s40, s16, 15\n"
"v_add_u32 v133, s40, v2\n"
"v_xor_b32 v2, 32, v133\n"
"v_add_u32 v134, 0x1c00, v2\n"
"v_xor_b32 v2, 64, v133\n"
"v_add_u32 v135, 0x3800, v2\n"
"v_xor_b32 v2, 0x60, v133\n"
"v_add_u32 v136, 0x5400, v2\n"
"s_lshl_b32 s40, s19, 10\n"
"s_add_u32 s20, s8, s40\n"
"s_addc_u32 s21, s9, 0\n"
"s_lshl_b32 s28, s16, 12\n"
"s_lshr_b32 s40, s18, 2\n"
"s_lshl_b32 s40, s40, 3\n"
"s_add_u32 s86, s40, s19\n"
"v_mov_b32 v7, 0x4000\n"
"s_lshl_b32 s40, s14, 3\n"
"s_add_u32 s40, s40, s16\n"
"s_mul_i32 s40, s40, 0x480\n"
"s_add_u32 s22, s6, s40\n"
"s_addc_u32 s23, s7, 0\n"
"v_lshlrev_b32 v64, 4, v1\n"
"v_and_b32 v65, 31, v1\n"
"v_lshlrev_b32 v65, 2, v65\n"
"s_and_b32 s40, s14, 3\n"
"s_lshr_b32 s41, s14, 2\n"
"s_lshl_b32 s41, s41, 5\n"
"s_add_u32 s40, s40, s41\n"
"s_lshl_b32 s39, s40, 2\n"
"s_lshl_b32 s40, s19, 3\n"
"s_add_u32 s41, s40, s16\n"
"s_lshr_b32 s42, s41, 2\n"
"s_sub_u32 s42, 0, s42\n"
"s_and_b32 s42, s42, 3\n"
"s_lshl_b32 s41, s41, 6\n"
"v_and_b32 v2, 3, v1\n"
"v_xor_b32 v2, s42, v2\n"
"v_lshlrev_b32 v2, 4, v2\n"
"v_lshrrev_b32 v3, 2, v1\n"
"v_lshl_or_b32 v2, v3, 12, v2\n"
"v_add_u32 v32, s41, v2\n"
"v_bfe_u32 v2, v1, 2, 2\n"
"v_xor_b32 v2, s42, v2\n"
"v_lshlrev_b32 v2, 4, v2\n"
"v_bfe_u32 v3, v1, 4, 1\n"
"v_lshl_or_b32 v2, v3, 12, v2\n"
"s_add_u32 s41, s41, 0x10000\n"
"v_add_u32 v33, s41, v2\n"
"s_mul_i32 s40, s18, 0x12000\n"
"s_add_u32 s40, s40, 0x24e000\n"
"s_add_u32 s26, s4, s40\n"
"s_addc_u32 s27, s5, 0\n"
"s_cmp_lg_u32 s16, 0\ns_cbranch_scc1 FR_B\n"
"s_setprio 3\nv_lshrrev_b32 v5, 1, v1\nv_mul_u32_u24 v5, 0x1b00, v5\nv_and_b32 v6, 1, v1\nv_lshl_add_u32 v5, v6, 4, v5\ns_min_u32 s40, s86, 53\ns_mul_i32 s41, s40, 57\ns_lshr_b32 s41, s41, 10\ns_mul_i32 s42, s41, 18\ns_sub_u32 s40, s40, s42\ns_lshl_b32 s40, s40, 2\ns_and_b32 s42, s18, 3\ns_add_u32 s35, s40, s42\ns_mul_i32 s40, s41, 0x144000\ns_lshl_b32 s42, s35, 5\ns_add_u32 s40, s40, s42\ns_add_u32 s30, s90, s40\ns_addc_u32 s31, s91, 0\ns_mul_i32 s40, s41, 0x180\nv_min_u32 v12, 47, v1\nv_lshlrev_b32 v12, 3, v12\nv_add_u32 v13, s40, v12\nglobal_load_dwordx2 v[52:53], v13, s[88:89] offset:0\ns_mov_b32 m0, 0x20000\ns_nop 0\nglobal_load_lds_dwordx4 v5, s[30:31]\ns_add_u32 s30, s30, 0x36000\ns_addc_u32 s31, s31, 0\ns_mov_b32 m0, 0x20400\ns_nop 0\nglobal_load_lds_dwordx4 v5, s[30:31]\ns_add_u32 s30, s30, 0x36000\ns_addc_u32 s31, s31, 0\ns_mov_b32 m0, 0x20800\ns_nop 0\nglobal_load_lds_dwordx4 v5, s[30:31]\ns_add_u32 s30, s30, 0x36000\ns_addc_u32 s31, s31, 0\ns_mov_b32 m0, 0x20c00\ns_nop 0\nglobal_load_lds_dwordx4 v5, s[30:31]\ns_add_u32 s30, s30, 0x36000\ns_addc_u32 s31, s31, 0\ns_mov_b32 m0, 0x21000\ns_nop 0\nglobal_load_lds_dwordx4 v5, s[30:31]\ns_add_u32 s30, s30, 0x36000\ns_addc_u32 s31, s31, 0\ns_mov_b32 m0, 0x21400\ns_nop 0\nglobal_load_lds_dwordx4 v5, s[30:31]\nglobal_load_dword v8, v7, s[4:5] offset:0 sc1\ns_lshl_b32 s40, s18, 9\nv_lshlrev_b32 v6, 6, v1\nv_add_u32 v6, s40, v6\nv_mov_b32 v7, 0\ns_mov_b64 exec, 15\nglobal_atomic_add v4, v6, v7, s[4:5] offset:0 sc0\ns_mov_b64 exec, -1\nglobal_load_dwordx4 v[16:19], v64, s[22:23] offset:0\nglobal_load_dword v20, v65, s[22:23] offset:1024\nv_lshrrev_b32 v2, 4, v1\nv_bfe_u32 v3, v1, 2, 2\nv_lshl_add_u32 v3, v2, 3, v3\nv_and_b32 v6, 3, v1\nv_lshlrev_b32 v6, 3, v6\nv_lshl_or_b32 v9, v3, 5, v6\nv_add_u32 v9, 0x20000, v9\ns_mul_i32 s41, s41, 0x1200\ns_lshl_b32 s42, s35, 6\ns_add_u32 s41, s41, s42\ns_add_u32 s41, s41, 0x4200\nv_lshl_add_u32 v11, v2, 4, s41\nv_lshlrev_b32 v10, 4, v2\nv_add_u32 v10, 0x21800, v10\nv_add_u32 v12, 0x21800, v12\ns_sub_u32 s40, s86, 54\ns_lshl_b32 s40, s40, 2\ns_and_b32 s42, s18, 3\ns_add_u32 s40, s40, s42\ns_lshl_b32 s40, s40, 4\ns_add_u32 s40, s40, 0x7800\ns_cmp_lt_u32 s86, 54\ns_cselect_b32 s30, 0x10001, 1\ns_cselect_b32 s31, s30, 0\ns_cselect_b64 vcc, -1, 0\nv_mov_b32 v57, s40\nv_cndmask_b32 v11, v57, v11, vcc\ns_waitcnt vmcnt(10)\nv_lshlrev_b32 v66, 16, v52\nv_and_b32 v67, 0xffff0000, v52\nv_lshlrev_b32 v68, 16, v53\nv_and_b32 v69, 0xffff0000, v53\nv_mul_f32 v70, 0xbfb8aa3b, v66\nv_mul_f32 v71, 0xbfb8aa3b, v67\nv_mul_f32 v72, 0xbfb8aa3b, v68\nv_mul_f32 v73, 0xbfb8aa3b, v69\nv_exp_f32 v70, v70\nv_exp_f32 v71, v71\nv_exp_f32 v72, v72\nv_exp_f32 v73, v73\nv_add_f32 v70, 1.0, v70\nv_add_f32 v71, 1.0, v71\nv_add_f32 v72, 1.0, v72\nv_add_f32 v73, 1.0, v73\nv_rcp_f32 v70, v70\nv_rcp_f32 v71, v71\nv_rcp_f32 v72, v72\nv_rcp_f32 v73, v73\nv_mul_f32 v66, v66, v70\nv_mul_f32 v67, v67, v71\nv_mul_f32 v68, v68, v72\nv_mul_f32 v69, v69, v73\nv_cvt_pk_bf16_f32 v52, v66, v67\nv_cvt_pk_bf16_f32 v53, v68, v69\nds_write_b64 v12, v[52:53] offset:0\nds_read_b128 v[104:107], v10 offset:0\nds_read_b128 v[108:111], v10 offset:64\nds_read_b128 v[112:115], v10 offset:128\nds_read_b128 v[116:119], v10 offset:192\nds_read_b128 v[120:123], v10 offset:256\nds_read_b128 v[124:127], v10 offset:320\ns_waitcnt vmcnt(4)\nds_read_b64_tr_b16 v[80:81], v9 offset:0\nds_read_b64_tr_b16 v[82:83], v9 offset:128\nds_read_b64_tr_b16 v[84:85], v9 offset:1024\nds_read_b64_tr_b16 v[86:87], v9 offset:1152\nds_read_b64_tr_b16 v[88:89], v9 offset:2048\nds_read_b64_tr_b16 v[90:91], v9 offset:2176\nds_read_b64_tr_b16 v[92:93], v9 offset:3072\nds_read_b64_tr_b16 v[94:95], v9 offset:3200\nds_read_b64_tr_b16 v[96:97], v9 offset:4096\nds_read_b64_tr_b16 v[98:99], v9 offset:4224\nds_read_b64_tr_b16 v[100:101], v9 offset:5120\nds_read_b64_tr_b16 v[102:103], v9 offset:5248\ns_waitcnt lgkmcnt(10)\nv_mfma_f32_16x16x32_bf16 v[52:55], v[80:83], v[104:107], 0\ns_waitcnt lgkmcnt(8)\nv_mfma_f32_16x16x32_bf16 v[52:55], v[84:87], v[108:111], v[52:55]\ns_waitcnt lgkmcnt(6)\nv_mfma_f32_16x16x32_bf16 v[52:55], v[88:91], v[112:115], v[52:55]\ns_waitcnt lgkmcnt(4)\nv_mfma_f32_16x16x32_bf16 v[52:55], v[92:95], v[116:119], v[52:55]\ns_waitcnt lgkmcnt(2)\nv_mfma_f32_16x16x32_bf16 v[52:55], v[96:99], v[120:123], v[52:55]\ns_waitcnt lgkmcnt(0)\nv_mfma_f32_16x16x32_bf16 v[52:55], v[100:103], v[124:127], v[52:55]\ns_waitcnt vmcnt(2)\nv_readfirstlane_b32 s15, v8\ns_nop 1\ns_add_u32 s15, s15, 1\ns_and_b32 s40, s15, 0xff\ns_mov_b32 s41, 0xffffff00\nv_mov_b32 v58, s40\nv_and_or_b32 v52, v52, s41, v58\nv_and_or_b32 v53, v53, s41, v58\nv_and_or_b32 v54, v54, s41, v58\nv_and_or_b32 v55, v55, s41, v58\ns_mov_b64 exec, s[30:31]\nglobal_store_dwordx4 v11, v[52:55], s[4:5] offset:0 sc1\ns_mov_b64 exec, -1\ns_waitcnt vmcnt(1)\nv_readlane_b32 s94, v4, 0\nv_readlane_b32 s95, v4, 1\nv_readlane_b32 s96, v4, 2\nv_readlane_b32 s97, v4, 3\ns_add_u32 s94, s94, 8\ns_add_u32 s95, s95, 8\ns_add_u32 s96, s96, 8\ns_add_u32 s97, s97, 8\ns_setprio 0\ns_nop 1\n"
"s_branch FR_J\nFR_B:\n"
"s_sleep 2\nglobal_load_dword v8, v7, s[4:5] offset:0 sc1\nglobal_load_dwordx4 v[16:19], v64, s[22:23] offset:0\nglobal_load_dword v20, v65, s[22:23] offset:1024\nv_add_u32 v66, 0xffffffc0, v0\ns_mul_i32 s40, s18, 0x90\nv_min_u32 v67, 0x8f, v66\nv_add_u32 v67, s40, v67\nv_lshrrev_b32 v68, 3, v67\nv_and_b32 v67, 7, v67\nv_lshlrev_b32 v67, 7, v67\nv_lshl_or_b32 v67, v68, 13, v67\ns_lshl_b32 s40, s19, 10\nv_add_u32 v67, s40, v67\ns_getpc_b64 s[24:25]\nFR_PC:\ns_mov_b32 s41, K_END-FR_PC\nv_min_u32 v68, 23, v1\ns_mul_i32 s40, s18, 24\nv_add_u32 v68, s40, v68\nv_lshlrev_b32 v68, 7, v68\nv_min_u32 v68, s41, v68\ns_cmp_eq_u32 s16, 4\ns_cselect_b64 s[24:25], s[24:25], s[8:9]\ns_cselect_b64 vcc, -1, 0\nv_cndmask_b32 v67, v67, v68, vcc\ns_waitcnt vmcnt(0)\ns_sleep 8\ns_cmp_ge_u32 s16, 5\ns_cbranch_scc1 FR_W1\nglobal_load_dword v200, v67, s[24:25]\nFR_W1:\nv_readfirstlane_b32 s15, v8\ns_nop 1\ns_add_u32 s15, s15, 1\ns_nop 1\n"
"FR_J:\n"
"s_add_u32 m0, s28, 0x10000\n"
"s_nop 0\n"
"global_load_lds_dwordx4 v133, s[20:21]\n"
"global_load_lds_dwordx4 v134, s[20:21] offset:1024\n"
"global_load_lds_dwordx4 v135, s[20:21] offset:2048\n"
"global_load_lds_dwordx4 v136, s[20:21] offset:3072\n"
"s_add_u32 s20, s20, 0x40000\n"
"s_addc_u32 s21, s21, 0\n"
"s_add_u32 m0, s28, 0x18000\n"
"s_nop 0\n"
"global_load_lds_dwordx4 v133, s[20:21]\n"
"global_load_lds_dwordx4 v134, s[20:21] offset:1024\n"
"global_load_lds_dwordx4 v135, s[20:21] offset:2048\n"
"global_load_lds_dwordx4 v136, s[20:21] offset:3072\n"
"s_add_u32 s20, s20, 0x40000\n"
"s_addc_u32 s21, s21, 0\n"
"v_mov_b32 v31, 0x3f803f80\n"
"v_cmp_gt_u32 vcc, 32, v1\n"
"s_nop 1\n"
"v_cndmask_b32 v21, 0, v20, vcc\n"
"v_dot2_f32_bf16 v36, v16, v31, 0\n"
"v_dot2_f32_bf16 v37, v16, v16, 0\n"
"v_dot2_f32_bf16 v36, v17, v31, v36\n"
"v_dot2_f32_bf16 v37, v17, v17, v37\n"
"v_dot2_f32_bf16 v36, v18, v31, v36\n"
"v_dot2_f32_bf16 v37, v18, v18, v37\n"
"v_dot2_f32_bf16 v36, v19, v31, v36\n"
"v_dot2_f32_bf16 v37, v19, v19, v37\n"
"v_dot2_f32_bf16 v36, v21, v31, v36\n"
"v_dot2_f32_bf16 v37, v21, v21, v37\n"
"s_nop 1\n"
"v_add_f32_dpp v36, v36, v36 quad_perm:[1,0,3,2] row_mask:0xf bank_mask:0xf\n"
"v_add_f32_dpp v37, v37, v37 quad_perm:[1,0,3,2] row_mask:0xf bank_mask:0xf\n"
"s_nop 0\n"
"v_add_f32_dpp v36, v36, v36 quad_perm:[2,3,0,1] row_mask:0xf bank_mask:0xf\n"
"v_add_f32_dpp v37, v37, v37 quad_perm:[2,3,0,1] row_mask:0xf bank_mask:0xf\n"
"s_nop 0\n"
"v_add_f32_dpp v36, v36, v36 row_ror:4 row_mask:0xf bank_mask:0xf\n"
"v_add_f32_dpp v37, v37, v37 row_ror:4 row_mask:0xf bank_mask:0xf\n"
"s_nop 0\n"
"v_add_f32_dpp v36, v36, v36 row_ror:8 row_mask:0xf bank_mask:0xf\n"
"v_add_f32_dpp v37, v37, v37 row_ror:8 row_mask:0xf bank_mask:0xf\n"
"v_mov_b32 v38, v36\n"
"v_mov_b32 v39, v37\n"
"s_nop 0\n"
"v_permlane32_swap_b32 v36, v38\n"
"v_permlane32_swap_b32 v37, v39\n"
"v_add_f32 v36, v36, v38\n"
"v_add_f32 v37, v37, v39\n"
"v_mov_b32 v38, v36\n"
"v_mov_b32 v39, v37\n"
"s_nop 0\n"
"v_permlane16_swap_b32 v36, v38\n"
"v_permlane16_swap_b32 v37, v39\n"
"v_add_f32 v36, v36, v38\n"
"v_add_f32 v37, v37, v39\n"
"v_mul_f32 v36, 0x3ae38e39, v36\n"
"v_mul_f32 v37, 0x3ae38e39, v37\n"
"v_fma_f32 v37, -v36, v36, v37\n"
"v_max_f32 v37, 0, v37\n"
"v_add_f32 v37, 0x3727c5ac, v37\n"
"v_rsq_f32 v37, v37\n"
"v_lshlrev_b32 v44, 16, v16\n"
"v_and_b32 v45, 0xffff0000, v16\n"
"v_lshlrev_b32 v46, 16, v17\n"
"v_and_b32 v47, 0xffff0000, v17\n"
"v_lshlrev_b32 v48, 16, v18\n"
"v_and_b32 v49, 0xffff0000, v18\n"
"v_lshlrev_b32 v50, 16, v19\n"
"v_and_b32 v51, 0xffff0000, v19\n"
"v_lshlrev_b32 v52, 16, v20\n"
"v_and_b32 v53, 0xffff0000, v20\n"
"v_sub_f32 v44, v44, v36\n"
"v_sub_f32 v45, v45, v36\n"
"v_sub_f32 v46, v46, v36\n"
"v_sub_f32 v47, v47, v36\n"
"v_sub_f32 v48, v48, v36\n"
"v_sub_f32 v49, v49, v36\n"
"v_sub_f32 v50, v50, v36\n"
"v_sub_f32 v51, v51, v36\n"
"v_sub_f32 v52, v52, v36\n"
"v_sub_f32 v53, v53, v36\n"
"v_mul_f32 v44, v44, v37\n"
"v_mul_f32 v45, v45, v37\n"
"v_mul_f32 v46, v46, v37\n"
"v_mul_f32 v47, v47, v37\n"
"v_mul_f32 v48, v48, v37\n"
"v_mul_f32 v49, v49, v37\n"
"v_mul_f32 v50, v50, v37\n"
"v_mul_f32 v51, v51, v37\n"
"v_mul_f32 v52, v52, v37\n"
"v_mul_f32 v53, v53, v37\n"
"v_lshrrev_b32 v140, 4, v1\n"
"v_bfe_u32 v141, v1, 2, 2\n"
"v_and_b32 v142, 1, v140\n"
"v_lshl_or_b32 v142, v142, 2, v141\n"
"v_and_b32 v143, 3, v1\n"
"v_lshlrev_b32 v143, 3, v143\n"
"v_lshl_or_b32 v143, v142, 5, v143\n"
"v_lshl_add_u32 v142, v140, 3, v141\n"
"v_lshl_or_b32 v142, v142, 10, v143\n"
"s_lshr_b32 s40, s16, 1\n"
"s_lshl_b32 s40, s40, 8\n"
"v_or_b32 v142, s40, v142\n"
"v_xor_b32 v116, 0, v142\n"
"v_add_u32 v124, 0x10000, v116\n"
"v_xor_b32 v117, 32, v142\n"
"v_add_u32 v125, 0x10000, v117\n"
"v_xor_b32 v118, 64, v142\n"
"v_add_u32 v126, 0x10000, v118\n"
"v_xor_b32 v119, 0x60, v142\n"
"v_add_u32 v127, 0x10000, v119\n"
"v_xor_b32 v120, 0x80, v142\n"
"v_add_u32 v128, 0x10000, v120\n"
"v_xor_b32 v121, 0xa0, v142\n"
"v_add_u32 v129, 0x10000, v121\n"
"v_xor_b32 v122, 0xc0, v142\n"
"v_add_u32 v130, 0x10000, v122\n"
"v_xor_b32 v123, 0xe0, v142\n"
"v_add_u32 v131, 0x10000, v123\n"
"v_lshl_add_u32 v142, v140, 2, v141\n"
"v_lshl_or_b32 v142, v142, 8, v143\n"
"s_lshl_b32 s40, s43, 13\n"
"s_add_u32 s41, s40, 0x4000\n"
"v_or_b32 v142, s41, v142\n"
"v_xor_b32 v232, 0, v142\n"
"v_add_u32 v220, 0x18000, v232\n"
"v_xor_b32 v233, 32, v142\n"
"v_add_u32 v221, 0x18000, v233\n"
"v_xor_b32 v234, 64, v142\n"
"v_add_u32 v222, 0x18000, v234\n"
"v_xor_b32 v235, 0x60, v142\n"
"v_add_u32 v223, 0x18000, v235\n"
"v_xor_b32 v236, 0x80, v142\n"
"v_add_u32 v224, 0x18000, v236\n"
"v_xor_b32 v237, 0xa0, v142\n"
"v_add_u32 v225, 0x18000, v237\n"
"v_xor_b32 v238, 0xc0, v142\n"
"v_add_u32 v226, 0x18000, v238\n"
"v_xor_b32 v239, 0xe0, v142\n"
"v_add_u32 v227, 0x18000, v239\n"
"v_and_b32 v143, 15, v1\n"
"v_xor_b32 v142, v140, v143\n"
"v_lshlrev_b32 v142, 4, v142\n"
"v_lshl_or_b32 v142, v143, 8, v142\n"
"v_or_b32 v142, s40, v142\n"
"v_xor_b32 v228, 0, v142\n"
"v_add_u32 v216, 0x18000, v228\n"
"v_xor_b32 v229, 64, v142\n"
"v_add_u32 v217, 0x18000, v229\n"
"v_xor_b32 v230, 0x80, v142\n"
"v_add_u32 v218, 0x18000, v230\n"
"v_xor_b32 v231, 0xc0, v142\n"
"v_add_u32 v219, 0x18000, v231\n"
"s_lshl_b32 s40, s17, 3\n"
"v_add_u32 v142, s40, v140\n"
"v_and_b32 v141, 15, v142\n"
"v_xor_b32 v141, v141, v143\n"
"v_lshlrev_b32 v141, 4, v141\n"
"v_lshl_or_b32 v240, v142, 8, v141\n"
"v_xor_b32 v241, 64, v240\n"
"v_and_b32 v141, 7, v142\n"
"v_lshlrev_b32 v141, 1, v141\n"
"v_xor_b32 v141, v141, v143\n"
"v_lshlrev_b32 v141, 4, v141\n"
"v_lshl_or_b32 v242, v142, 8, v141\n"
"v_xor_b32 v243, 0x80, v242\n"
"v_bfe_u32 v141, v1, 2, 2\n"
"v_sub_u32 v141, 0, v141\n"
"v_lshrrev_b32 v140, 4, v1\n"
"v_xor_b32 v141, v141, v140\n"
"v_and_b32 v141, 3, v141\n"
"v_lshlrev_b32 v141, 4, v141\n"
"v_and_b32 v140, 15, v1\n"
"v_lshl_or_b32 v140, v140, 6, v141\n"
"s_and_b32 s40, s16, 1\n"
"s_lshl_b32 s40, s40, 11\n"
"s_add_u32 s40, s40, 0x20000\n"
"v_add_u32 v132, s40, v140\n"
"v_lshlrev_b32 v137, 4, v0\n"
"s_lshl_b32 s38, s16, 10\n"
"v_lshlrev_b32 v3, 5, v1\n"
"v_add_u32 v3, 0x26000, v3\n"
"v_lshlrev_b32 v4, 1, v65\n"
"v_add_u32 v4, 0x26800, v4\n"
"v_min_u32 v12, 0x11f, v0\n"
"v_lshlrev_b32 v13, 4, v12\n"
"v_cmp_lt_u32 vcc, 143, v12\n"
"s_nop 1\n"
"v_cndmask_b32 v14, 0, 1.0, vcc\n"
"v_add_u32 v5, 0x26000, v13\n"
"v_add_u32 v12, 0xfffffee0, v0\n"
"v_min_u32 v12, 39, v12\n"
"v_lshlrev_b32 v12, 4, v12\n"
"v_add_u32 v12, 0x7800, v12\n"
"v_cmp_gt_u32 vcc, 0x120, v0\n"
"v_add_u32 v66, 0x4200, v13\n"
"s_nop 0\n"
"v_cndmask_b32 v66, v12, v66, vcc\n"
"v_add_u32 v67, 0x5400, v13\n"
"v_cndmask_b32 v67, v12, v67, vcc\n"
"v_add_u32 v68, 0x6600, v13\n"
"v_cndmask_b32 v68, v12, v68, vcc\n"
"s_and_b32 s35, s15, 0xff\n"
"s_lshl_b32 s40, s18, 9\n"
"s_add_u32 s36, s4, s40\n"
"s_addc_u32 s37, s5, 0\n"
"v_mov_b32 v60, 0\n"
"v_mov_b32 v61, 1\n"
"s_mov_b32 s42, 0x40000\nMP_L:\nglobal_load_dwordx4 v[16:19], v66, s[4:5] sc1\nglobal_load_dwordx4 v[20:23], v67, s[4:5] sc1\nglobal_load_dwordx4 v[24:27], v68, s[4:5] sc1\ns_waitcnt vmcnt(0)\nv_xor_b32 v70, s35, v16\nv_xor_b32 v71, s35, v17\nv_xor_b32 v72, s35, v18\nv_xor_b32 v73, s35, v19\nv_xor_b32 v74, s35, v20\nv_xor_b32 v75, s35, v21\nv_xor_b32 v76, s35, v22\nv_xor_b32 v77, s35, v23\nv_xor_b32 v78, s35, v24\nv_xor_b32 v79, s35, v25\nv_xor_b32 v80, s35, v26\nv_xor_b32 v81, s35, v27\nv_or3_b32 v70, v70, v71, v72\nv_or3_b32 v73, v73, v74, v75\nv_or3_b32 v76, v76, v77, v78\nv_or3_b32 v79, v79, v80, v81\nv_or3_b32 v70, v70, v73, v76\nv_or_b32 v70, v70, v79\nv_and_b32 v70, 0xff, v70\nv_cmp_ne_u32 vcc, 0, v70\ns_sub_u32 s42, s42, 1\ns_nop 1\ns_cselect_b64 vcc, 0, vcc\ns_cmp_eq_u64 vcc, 0\ns_cbranch_scc1 TW_OKM\ns_sleep 2\ns_branch MP_L\nTW_OKM:\n"
"v_and_b32 v16, 0xffffff00, v16\n"
"v_and_b32 v17, 0xffffff00, v17\n"
"v_and_b32 v18, 0xffffff00, v18\n"
"v_and_b32 v19, 0xffffff00, v19\n"
"v_and_b32 v20, 0xffffff00, v20\n"
"v_and_b32 v21, 0xffffff00, v21\n"
"v_and_b32 v22, 0xffffff00, v22\n"
"v_and_b32 v23, 0xffffff00, v23\n"
"v_and_b32 v24, 0xffffff00, v24\n"
"v_and_b32 v25, 0xffffff00, v25\n"
"v_and_b32 v26, 0xffffff00, v26\n"
"v_and_b32 v27, 0xffffff00, v27\n"
"v_add_f32 v16, v16, v20\n"
"v_add_f32 v17, v17, v21\n"
"v_add_f32 v18, v18, v22\n"
"v_add_f32 v19, v19, v23\n"
"v_add_f32 v16, v16, v24\n"
"v_add_f32 v17, v17, v25\n"
"v_add_f32 v18, v18, v26\n"
"v_add_f32 v19, v19, v27\n"
"v_cvt_pk_bf16_f32 v20, v16, v17\n"
"v_cvt_pk_bf16_f32 v21, v18, v19\n"
"v_lshlrev_b32 v16, 16, v20\n"
"v_and_b32 v17, 0xffff0000, v20\n"
"v_lshlrev_b32 v18, 16, v21\n"
"v_and_b32 v19, 0xffff0000, v21\n"
"v_add_f32 v16, v16, v14\n"
"v_add_f32 v17, v17, v14\n"
"v_add_f32 v18, v18, v14\n"
"v_add_f32 v19, v19, v14\n"
"v_cvt_pk_bf16_f32 v20, v16, v17\n"
"v_cvt_pk_bf16_f32 v21, v18, v19\n"
"v_lshlrev_b32 v16, 16, v20\n"
"v_and_b32 v17, 0xffff0000, v20\n"
"v_lshlrev_b32 v18, 16, v21\n"
"v_and_b32 v19, 0xffff0000, v21\n"
"v_mul_f32 v16, 0x3e0293ee, v16\n"
"v_mul_f32 v17, 0x3e0293ee, v17\n"
"v_mul_f32 v18, 0x3e0293ee, v18\n"
"v_mul_f32 v19, 0x3e0293ee, v19\n"
"v_cmp_gt_u32 vcc, 0x120, v0\n"
"s_nop 1\n"
"s_mov_b64 exec, vcc\n"
"ds_write_b128 v5, v[16:19] offset:0\n"
"s_mov_b64 exec, -1\n"
"s_waitcnt vmcnt(0) lgkmcnt(0)\n"
"s_barrier\n"
"ds_read_b128 v[72:75], v3 offset:2304\n"
"ds_read_b128 v[84:87], v3 offset:0\n"
"ds_read_b128 v[76:79], v3 offset:2320\n"
"ds_read_b128 v[88:91], v3 offset:16\n"
"ds_read_b64 v[80:81], v4 offset:2304\n"
"ds_read_b64 v[92:93], v4 offset:0\n"
"s_waitcnt lgkmcnt(4)\n"
"v_fma_f32 v44, v44, v72, v84\n"
"v_fma_f32 v45, v45, v73, v85\n"
"v_fma_f32 v46, v46, v74, v86\n"
"v_fma_f32 v47, v47, v75, v87\n"
"s_waitcnt lgkmcnt(2)\n"
"v_fma_f32 v48, v48, v76, v88\n"
"v_fma_f32 v49, v49, v77, v89\n"
"v_fma_f32 v50, v50, v78, v90\n"
"v_fma_f32 v51, v51, v79, v91\n"
"s_waitcnt lgkmcnt(0)\n"
"v_fma_f32 v52, v52, v80, v92\n"
"v_fma_f32 v53, v53, v81, v93\n"
"v_cvt_pk_bf16_f32 v16, v44, v45\n"
"v_cvt_pk_bf16_f32 v17, v46, v47\n"
"v_cvt_pk_bf16_f32 v18, v48, v49\n"
"v_cvt_pk_bf16_f32 v19, v50, v51\n"
"v_cvt_pk_bf16_f32 v20, v52, v53\n"
"global_store_dwordx4 v32, v[16:19], s[26:27] offset:0 sc1\n"
"s_nop 0\n"
"v_mov_b32_dpp v24, v20 quad_perm:[0,0,0,0] row_mask:0xf bank_mask:0xf\n"
"v_mov_b32_dpp v25, v20 quad_perm:[1,1,1,1] row_mask:0xf bank_mask:0xf\n"
"v_mov_b32_dpp v26, v20 quad_perm:[2,2,2,2] row_mask:0xf bank_mask:0xf\n"
"v_mov_b32_dpp v27, v20 quad_perm:[3,3,3,3] row_mask:0xf bank_mask:0xf\n"
"s_mov_b64 exec, 0x11111111\n"
"global_store_dwordx4 v33, v[24:27], s[26:27] offset:0 sc1\n"
"s_mov_b64 exec, -1\n"
"v_mov_b32 v144, v32\n"
"v_mov_b32 v145, v33\n"
"s_waitcnt vmcnt(0) lgkmcnt(0)\n"
"s_barrier\n"
"s_cmp_eq_u32 s16, 0\n"
"s_cselect_b64 exec, 1, 0\n"
"global_atomic_add v62, v60, v61, s[36:37] offset:192 sc0\n"
"s_mov_b64 exec, -1\n"
"s_cmp_eq_u32 s16, 0\ns_cbranch_scc1 FR_R1\n"
"s_mov_b32 m0, s28\n"
"s_nop 0\n"
"global_load_lds_dwordx4 v133, s[20:21]\n"
"global_load_lds_dwordx4 v134, s[20:21] offset:1024\n"
"global_load_lds_dwordx4 v135, s[20:21] offset:2048\n"
"global_load_lds_dwordx4 v136, s[20:21] offset:3072\n"
"s_add_u32 s20, s20, 0x40000\n"
"s_addc_u32 s21, s21, 0\n"
"s_add_u32 m0, s28, 0x8000\n"
"s_nop 0\n"
"global_load_lds_dwordx4 v133, s[20:21]\n"
"global_load_lds_dwordx4 v134, s[20:21] offset:1024\n"
"global_load_lds_dwordx4 v135, s[20:21] offset:2048\n"
"global_load_lds_dwordx4 v136, s[20:21] offset:3072\n"
"s_add_u32 s20, s20, 0x40000\n"
"s_addc_u32 s21, s21, 0\n"
"FR_R1:\n"
"s_waitcnt vmcnt(0)\n"
"v_readfirstlane_b32 s40, v62\n"
"s_nop 1\n"
"s_cmp_lg_u32 s16, 0\ns_cbranch_scc1 CW_OK1\ns_add_u32 s40, s40, 1\ns_cmp_eq_u32 s40, s97\ns_cbranch_scc1 CW_OK1\ns_mov_b32 s42, 0x40000\ns_mov_b64 exec, 1\nCW1:\ns_sleep 1\nglobal_atomic_add v62, v60, v60, s[36:37] offset:192 sc0\ns_waitcnt vmcnt(0)\nv_readfirstlane_b32 s40, v62\ns_sub_u32 s42, s42, 1\ns_cselect_b32 s40, s97, s40\ns_cmp_lg_u32 s40, s97\ns_cbranch_scc1 CW1\ns_mov_b64 exec, -1\nCW_OK1:\n"
"s_barrier\n"
"s_add_u32 m0, s38, 0x20000\n"
"s_nop 0\n"
"global_load_lds_dwordx4 v137, s[26:27] sc1\n"
"s_add_u32 s26, s26, 0x2000\n"
"s_addc_u32 s27, s27, 0\n"
"s_add_u32 m0, s38, 0x22000\n"
"s_nop 0\n"
"global_load_lds_dwordx4 v137, s[26:27] sc1\n"
"s_add_u32 s26, s26, 0x2000\n"
"s_addc_u32 s27, s27, 0\n"
"s_add_u32 m0, s38, 0x24000\n"
"s_nop 0\n"
"global_load_lds_dwordx4 v137, s[26:27] sc1\n"
"s_add_u32 s26, s26, 0x2000\n"
"s_addc_u32 s27, s27, 0\n"
"s_cmp_lg_u32 s16, 0\ns_cbranch_scc1 FR_R0\n"
"s_mov_b32 m0, s28\n"
"s_nop 0\n"
"global_load_lds_dwordx4 v133, s[20:21]\n"
"global_load_lds_dwordx4 v134, s[20:21] offset:1024\n"
"global_load_lds_dwordx4 v135, s[20:21] offset:2048\n"
"global_load_lds_dwordx4 v136, s[20:21] offset:3072\n"
"s_add_u32 s20, s20, 0x40000\n"
"s_addc_u32 s21, s21, 0\n"
"s_add_u32 m0, s28, 0x8000\n"
"s_nop 0\n"
"global_load_lds_dwordx4 v133, s[20:21]\n"
"global_load_lds_dwordx4 v134, s[20:21] offset:1024\n"
"global_load_lds_dwordx4 v135, s[20:21] offset:2048\n"
"global_load_lds_dwordx4 v136, s[20:21] offset:3072\n"
"s_add_u32 s20, s20, 0x40000\n"
"s_addc_u32 s21, s21, 0\n"
"FR_R0:\n"
"s_lshl_b32 s40, s19, 1\n"
"s_add_u32 s40, s40, s43\n"
"s_lshl_b32 s40, s40, 19\n"
"s_add_u32 s40, s40, 0x60000\n"
"s_add_u32 s30, s10, s40\n"
"s_addc_u32 s31, s11, 0\n"
"s_add_u32 s34, s12, s40\n"
"s_addc_u32 s35, s13, 0\n"
"s_lshl_b32 s29, s16, 11\n"
"v_lshrrev_b32 v140, 4, v1\n"
"v_bfe_u32 v141, v1, 2, 2\n"
"v_mul_u32_u24 v142, 0xc00, v140\n"
"v_lshl_or_b32 v142, v141, 7, v142\n"
"v_and_b32 v143, 3, v1\n"
"v_lshl_or_b32 v142, v143, 3, v142\n"
"v_lshrrev_b32 v141, 1, v141\n"
"v_and_b32 v140, 1, v140\n"
"v_lshl_or_b32 v141, v140, 1, v141\n"
"v_lshl_or_b32 v142, v141, 5, v142\n"
"s_mul_i32 s41, s17, 3\n"
"s_add_u32 s42, s41, 0\n"
"s_and_b32 s40, s42, 3\n"
"s_lshl_b32 s40, s40, 5\n"
"s_lshr_b32 s42, s42, 2\n"
"s_lshl_b32 s42, s42, 10\n"
"s_add_u32 s42, s42, 0x10000\n"
"v_xor_b32 v143, s40, v142\n"
"v_add_u32 v2, s42, v143\n"
"s_add_u32 s42, s41, 1\n"
"s_and_b32 s40, s42, 3\n"
"s_lshl_b32 s40, s40, 5\n"
"s_lshr_b32 s42, s42, 2\n"
"s_lshl_b32 s42, s42, 10\n"
"s_add_u32 s42, s42, 0x10000\n"
"v_xor_b32 v143, s40, v142\n"
"v_add_u32 v3, s42, v143\n"
"s_add_u32 s42, s41, 2\n"
"s_and_b32 s40, s42, 3\n"
"s_lshl_b32 s40, s40, 5\n"
"s_lshr_b32 s42, s42, 2\n"
"s_cmp_eq_u32 s16, 0\ns_cbranch_scc1 FR_Q0\ns_waitcnt vmcnt(2)\nFR_Q0:\ns_waitcnt vmcnt(10)\n"
"s_barrier\n"
"ds_read_b128 v[100:103], v132 offset:0\n"
"ds_read_b128 v[104:107], v132 offset:1024\n"
"ds_read_b64_tr_b16 v[80:81], v124 offset:0\n"
"ds_read_b64_tr_b16 v[82:83], v124 offset:4096\n"
"ds_read_b64_tr_b16 v[84:85], v125 offset:0\n"
"ds_read_b64_tr_b16 v[86:87], v125 offset:4096\n"
"ds_read_b64_tr_b16 v[88:89], v126 offset:0\n"
"ds_read_b64_tr_b16 v[90:91], v126 offset:4096\n"
"ds_read_b64_tr_b16 v[92:93], v127 offset:0\n"
"ds_read_b64_tr_b16 v[94:95], v127 offset:4096\n"
"s_waitcnt lgkmcnt(6)\n"
"v_mfma_f32_16x16x32_bf16 v[16:19], v[80:83], v[100:103], 0\n"
"ds_read_b64_tr_b16 v[96:97], v128 offset:0\n"
"ds_read_b64_tr_b16 v[98:99], v128 offset:4096\n"
"v_mfma_f32_16x16x32_bf16 v[20:23], v[80:83], v[104:107], 0\n"
"s_waitcnt lgkmcnt(6)\n"
"v_mfma_f32_16x16x32_bf16 v[24:27], v[84:87], v[100:103], 0\n"
"ds_read_b64_tr_b16 v[80:81], v129 offset:0\n"
"ds_read_b64_tr_b16 v[82:83], v129 offset:4096\n"
"v_mfma_f32_16x16x32_bf16 v[28:31], v[84:87], v[104:107], 0\n"
"s_waitcnt lgkmcnt(6)\n"
"v_mfma_f32_16x16x32_bf16 v[32:35], v[88:91], v[100:103], 0\n"
"ds_read_b64_tr_b16 v[84:85], v130 offset:0\n"
"ds_read_b64_tr_b16 v[86:87], v130 offset:4096\n"
"v_mfma_f32_16x16x32_bf16 v[36:39], v[88:91], v[104:107], 0\n"
"s_waitcnt lgkmcnt(6)\n"
"v_mfma_f32_16x16x32_bf16 v[40:43], v[92:95], v[100:103], 0\n"
"ds_read_b64_tr_b16 v[88:89], v131 offset:0\n"
"ds_read_b64_tr_b16 v[90:91], v131 offset:4096\n"
"v_mfma_f32_16x16x32_bf16 v[44:47], v[92:95], v[104:107], 0\n"
"s_waitcnt lgkmcnt(6)\n"
"v_mfma_f32_16x16x32_bf16 v[48:51], v[96:99], v[100:103], 0\n"
"v_mfma_f32_16x16x32_bf16 v[52:55], v[96:99], v[104:107], 0\n"
"s_waitcnt vmcnt(0) lgkmcnt(0)\n"
"s_barrier\n"
"ds_read_b128 v[108:111], v132 offset:4096\n"
"ds_read_b128 v[112:115], v132 offset:5120\n"
"ds_read_b64_tr_b16 v[92:93], v124 offset:32768\n"
"ds_read_b64_tr_b16 v[94:95], v124 offset:36864\n"
"s_add_u32 m0, s28, 0x10000\n"
"v_mfma_f32_16x16x32_bf16 v[56:59], v[80:83], v[100:103], 0\n"
"ds_read_b64_tr_b16 v[96:97], v125 offset:32768\n"
"ds_read_b64_tr_b16 v[98:99], v125 offset:36864\n"
"global_load_lds_dwordx4 v133, s[20:21]\n"
"v_mfma_f32_16x16x32_bf16 v[60:63], v[80:83], v[104:107], 0\n"
"ds_read_b64_tr_b16 v[80:81], v126 offset:32768\n"
"ds_read_b64_tr_b16 v[82:83], v126 offset:36864\n"
"v_mfma_f32_16x16x32_bf16 v[64:67], v[84:87], v[100:103], 0\n"
"v_mfma_f32_16x16x32_bf16 v[68:71], v[84:87], v[104:107], 0\n"
"ds_read_b64_tr_b16 v[84:85], v127 offset:32768\n"
"ds_read_b64_tr_b16 v[86:87], v127 offset:36864\n"
"global_load_lds_dwordx4 v134, s[20:21] offset:1024\n"
"v_mfma_f32_16x16x32_bf16 v[72:75], v[88:91], v[100:103], 0\n"
"v_mfma_f32_16x16x32_bf16 v[76:79], v[88:91], v[104:107], 0\n"
"s_waitcnt lgkmcnt(6)\n"
"v_mfma_f32_16x16x32_bf16 v[16:19], v[92:95], v[108:111], v[16:19]\n"
"ds_read_b64_tr_b16 v[88:89], v128 offset:32768\n"
"ds_read_b64_tr_b16 v[90:91], v128 offset:36864\n"
"s_lshl_b32 s42, s42, 10\n"
"v_mfma_f32_16x16x32_bf16 v[20:23], v[92:95], v[112:115], v[20:23]\n"
"global_load_lds_dwordx4 v135, s[20:21] offset:2048\n"
"s_add_u32 s42, s42, 0x10000\n"
"s_waitcnt lgkmcnt(6)\n"
"v_mfma_f32_16x16x32_bf16 v[24:27], v[96:99], v[108:111], v[24:27]\n"
"ds_read_b64_tr_b16 v[92:93], v129 offset:32768\n"
"ds_read_b64_tr_b16 v[94:95], v129 offset:36864\n"
"v_xor_b32 v143, s40, v142\n"
"v_mfma_f32_16x16x32_bf16 v[28:31], v[96:99], v[112:115], v[28:31]\n"
"v_add_u32 v4, s42, v143\n"
"s_waitcnt lgkmcnt(6)\n"
"v_mfma_f32_16x16x32_bf16 v[32:35], v[80:83], v[108:111], v[32:35]\n"
"ds_read_b64_tr_b16 v[96:97], v130 offset:32768\n"
"ds_read_b64_tr_b16 v[98:99], v130 offset:36864\n"
"v_bfe_u32 v141, v1, 2, 2\n"
"v_mfma_f32_16x16x32_bf16 v[36:39], v[80:83], v[112:115], v[36:39]\n"
"global_load_lds_dwordx4 v136, s[20:21] offset:3072\n"
"s_add_u32 s20, s20, 0x40000\n"
"s_addc_u32 s21, s21, 0\n"
"v_sub_u32 v141, 0, v141\n"
"s_waitcnt lgkmcnt(6)\n"
"v_mfma_f32_16x16x32_bf16 v[40:43], v[84:87], v[108:111], v[40:43]\n"
"ds_read_b64_tr_b16 v[80:81], v131 offset:32768\n"
"ds_read_b64_tr_b16 v[82:83], v131 offset:36864\n"
"v_lshrrev_b32 v140, 4, v1\n"
"v_mfma_f32_16x16x32_bf16 v[44:47], v[84:87], v[112:115], v[44:47]\n"
"v_xor_b32 v141, v141, v140\n"
"s_waitcnt lgkmcnt(6)\n"
"v_mfma_f32_16x16x32_bf16 v[48:51], v[88:91], v[108:111], v[48:51]\n"
"v_and_b32 v141, 3, v141\n"
"v_mfma_f32_16x16x32_bf16 v[52:55], v[88:91], v[112:115], v[52:55]\n"
"s_waitcnt vmcnt(4) lgkmcnt(0)\n"
"s_barrier\n"
"ds_read_b128 v[100:103], v132 offset:8192\n"
"ds_read_b128 v[104:107], v132 offset:9216\n"
"ds_read_b64_tr_b16 v[84:85], v116 offset:0\n"
"ds_read_b64_tr_b16 v[86:87], v116 offset:4096\n"
"s_add_u32 m0, s38, 0x20000\n"
"v_lshlrev_b32 v141, 4, v141\n"
"v_mfma_f32_16x16x32_bf16 v[56:59], v[92:95], v[108:111], v[56:59]\n"
"ds_read_b64_tr_b16 v[88:89], v117 offset:0\n"
"ds_read_b64_tr_b16 v[90:91], v117 offset:4096\n"
"global_load_lds_dwordx4 v137, s[26:27] sc1\n"
"s_add_u32 s26, s26, 0x2000\n"
"s_addc_u32 s27, s27, 0\n"
"v_and_b32 v140, 15, v1\n"
"v_mfma_f32_16x16x32_bf16 v[60:63], v[92:95], v[112:115], v[60:63]\n"
"ds_read_b64_tr_b16 v[92:93], v118 offset:0\n"
"ds_read_b64_tr_b16 v[94:95], v118 offset:4096\n"
"v_lshl_or_b32 v140, v140, 6, v141\n"
"v_mfma_f32_16x16x32_bf16 v[64:67], v[96:99], v[108:111], v[64:67]\n"
"s_add_u32 m0, s28, 0x18000\n"
"v_mfma_f32_16x16x32_bf16 v[68:71], v[96:99], v[112:115], v[68:71]\n"
"ds_read_b64_tr_b16 v[96:97], v119 offset:0\n"
"ds_read_b64_tr_b16 v[98:99], v119 offset:4096\n"
"global_load_lds_dwordx4 v133, s[20:21]\n"
"v_mfma_f32_16x16x32_bf16 v[72:75], v[80:83], v[108:111], v[72:75]\n"
"v_mfma_f32_16x16x32_bf16 v[76:79], v[80:83], v[112:115], v[76:79]\n"
"s_waitcnt lgkmcnt(6)\n"
"v_mfma_f32_16x16x32_bf16 v[16:19], v[84:87], v[100:103], v[16:19]\n"
"ds_read_b64_tr_b16 v[80:81], v120 offset:0\n"
"ds_read_b64_tr_b16 v[82:83], v120 offset:4096\n"
"s_lshl_b32 s40, s43, 11\n"
"v_mfma_f32_16x16x32_bf16 v[20:23], v[84:87], v[104:107], v[20:23]\n"
"global_load_lds_dwordx4 v134, s[20:21] offset:1024\n"
"s_add_u32 s40, s40, 0\n"
"s_waitcnt lgkmcnt(6)\n"
"v_mfma_f32_16x16x32_bf16 v[24:27], v[88:91], v[100:103], v[24:27]\n"
"ds_read_b64_tr_b16 v[84:85], v121 offset:0\n"
"ds_read_b64_tr_b16 v[86:87], v121 offset:4096\n"
"v_add_u32 v5, s40, v140\n"
"v_mfma_f32_16x16x32_bf16 v[28:31], v[88:91], v[104:107], v[28:31]\n"
"global_load_lds_dwordx4 v135, s[20:21] offset:2048\n"
"v_lshrrev_b32 v140, 3, v1\n"
"s_waitcnt lgkmcnt(6)\n"
"v_mfma_f32_16x16x32_bf16 v[32:35], v[92:95], v[100:103], v[32:35]\n"
"ds_read_b64_tr_b16 v[88:89], v122 offset:0\n"
"ds_read_b64_tr_b16 v[90:91], v122 offset:4096\n"
"v_bfe_u32 v141, v1, 4, 1\n"
"v_mfma_f32_16x16x32_bf16 v[36:39], v[92:95], v[104:107], v[36:39]\n"
"v_lshlrev_b32 v141, 1, v141\n"
"s_waitcnt lgkmcnt(6)\n"
"v_mfma_f32_16x16x32_bf16 v[40:43], v[96:99], v[100:103], v[40:43]\n"
"ds_read_b64_tr_b16 v[92:93], v123 offset:0\n"
"ds_read_b64_tr_b16 v[94:95], v123 offset:4096\n"
"global_load_lds_dwordx4 v136, s[20:21] offset:3072\n"
"s_add_u32 s20, s20, 0x40000\n"
"s_addc_u32 s21, s21, 0\n"
"v_and_b32 v142, 7, v1\n"
"v_mfma_f32_16x16x32_bf16 v[44:47], v[96:99], v[104:107], v[44:47]\n"
"v_xor_b32 v141, v141, v142\n"
"s_waitcnt lgkmcnt(6)\n"
"v_mfma_f32_16x16x32_bf16 v[48:51], v[80:83], v[100:103], v[48:51]\n"
"v_lshlrev_b32 v141, 4, v141\n"
"v_mfma_f32_16x16x32_bf16 v[52:55], v[80:83], v[104:107], v[52:55]\n"
"s_waitcnt vmcnt(9) lgkmcnt(0)\n"
"s_barrier\n"
"ds_read_b128 v[108:111], v132 offset:12288\n"
"ds_read_b128 v[112:115], v132 offset:13312\n"
"ds_read_b64_tr_b16 v[96:97], v116 offset:32768\n"
"ds_read_b64_tr_b16 v[98:99], v116 offset:36864\n"
"s_mov_b32 m0, s28\n"
"s_and_b32 s40, s16, 1\n"
"v_mfma_f32_16x16x32_bf16 v[56:59], v[84:87], v[100:103], v[56:59]\n"
"ds_read_b64_tr_b16 v[80:81], v117 offset:32768\n"
"ds_read_b64_tr_b16 v[82:83], v117 offset:36864\n"
"global_load_lds_dwordx4 v133, s[20:21]\n"
"s_lshl_b32 s40, s40, 4\n"
"v_mfma_f32_16x16x32_bf16 v[60:63], v[84:87], v[104:107], v[60:63]\n"
"ds_read_b64_tr_b16 v[84:85], v118 offset:32768\n"
"ds_read_b64_tr_b16 v[86:87], v118 offset:36864\n"
"v_add_u32 v140, s40, v140\n"
"v_mfma_f32_16x16x32_bf16 v[64:67], v[88:91], v[100:103], v[64:67]\n"
"v_mfma_f32_16x16x32_bf16 v[68:71], v[88:91], v[104:107], v[68:71]\n"
"ds_read_b64_tr_b16 v[88:89], v119 offset:32768\n"
"ds_read_b64_tr_b16 v[90:91], v119 offset:36864\n"
"global_load_lds_dwordx4 v134, s[20:21] offset:1024\n"
"v_mfma_f32_16x16x32_bf16 v[72:75], v[92:95], v[100:103], v[72:75]\n"
"v_mfma_f32_16x16x32_bf16 v[76:79], v[92:95], v[104:107], v[76:79]\n"
"s_waitcnt lgkmcnt(6)\n"
"v_mfma_f32_16x16x32_bf16 v[16:19], v[96:99], v[108:111], v[16:19]\n"
"ds_read_b64_tr_b16 v[92:93], v120 offset:32768\n"
"ds_read_b64_tr_b16 v[94:95], v120 offset:36864\n"
"v_mul_u32_u24 v140, 0x480, v140\n"
"v_mfma_f32_16x16x32_bf16 v[20:23], v[96:99], v[112:115], v[20:23]\n"
"global_load_lds_dwordx4 v135, s[20:21] offset:2048\n"
"v_add_u32 v140, 0x1000, v140\n"
"s_waitcnt lgkmcnt(6)\n"
"v_mfma_f32_16x16x32_bf16 v[24:27], v[80:83], v[108:111], v[24:27]\n"
"ds_read_b64_tr_b16 v[96:97], v121 offset:32768\n"
"ds_read_b64_tr_b16 v[98:99], v121 offset:36864\n"
"v_add_u32 v6, v140, v141\n"
"v_mfma_f32_16x16x32_bf16 v[28:31], v[80:83], v[112:115], v[28:31]\n"
"v_xor_b32 v141, 64, v141\n"
"s_waitcnt lgkmcnt(6)\n"
"v_mfma_f32_16x16x32_bf16 v[32:35], v[84:87], v[108:111], v[32:35]\n"
"ds_read_b64_tr_b16 v[80:81], v122 offset:32768\n"
"ds_read_b64_tr_b16 v[82:83], v122 offset:36864\n"
"v_add_u32 v140, 0x2400, v140\n"
"v_mfma_f32_16x16x32_bf16 v[36:39], v[84:87], v[112:115], v[36:39]\n"
"global_load_lds_dwordx4 v136, s[20:21] offset:3072\n"
"s_add_u32 s20, s20, 0x40000\n"
"s_addc_u32 s21, s21, 0\n"
"v_add_u32 v9, v140, v141\n"
"s_waitcnt lgkmcnt(6)\n"
"v_mfma_f32_16x16x32_bf16 v[40:43], v[88:91], v[108:111], v[40:43]\n"
"ds_read_b64_tr_b16 v[84:85], v123 offset:32768\n"
"ds_read_b64_tr_b16 v[86:87], v123 offset:36864\n"
"v_add_u32 v7, 0xfffffc80, v6\n"
"v_mfma_f32_16x16x32_bf16 v[44:47], v[88:91], v[112:115], v[44:47]\n"
"v_add_u32 v8, 0xfffff900, v6\n"
"s_waitcnt lgkmcnt(6)\n"
"v_mfma_f32_16x16x32_bf16 v[48:51], v[92:95], v[108:111], v[48:51]\n"
"v_add_u32 v10, 0xfffffc80, v9\n"
"v_mfma_f32_16x16x32_bf16 v[52:55], v[92:95], v[112:115], v[52:55]\n"
"s_waitcnt vmcnt(9) lgkmcnt(0)\n"
"s_barrier\n"
"ds_read_b128 v[100:103], v132 offset:16384\n"
"ds_read_b128 v[104:107], v132 offset:17408\n"
"ds_read_b64_tr_b16 v[88:89], v124 offset:0\n"
"ds_read_b64_tr_b16 v[90:91], v124 offset:4096\n"
"s_add_u32 m0, s38, 0x22000\n"
"v_add_u32 v11, 0xfffff900, v9\n"
"v_mfma_f32_16x16x32_bf16 v[56:59], v[96:99], v[108:111], v[56:59]\n"
"ds_read_b64_tr_b16 v[92:93], v125 offset:0\n"
"ds_read_b64_tr_b16 v[94:95], v125 offset:4096\n"
"global_load_lds_dwordx4 v137, s[26:27] sc1\n"
"s_add_u32 s26, s26, 0x2000\n"
"s_addc_u32 s27, s27, 0\n"
"s_mul_i32 s44, s16, 0x1800\n"
"v_mfma_f32_16x16x32_bf16 v[60:63], v[96:99], v[112:115], v[60:63]\n"
"ds_read_b64_tr_b16 v[96:97], v126 offset:0\n"
"ds_read_b64_tr_b16 v[98:99], v126 offset:4096\n"
"s_mul_i32 s40, s19, 0x90000\n"
"v_mfma_f32_16x16x32_bf16 v[64:67], v[80:83], v[108:111], v[64:67]\n"
"s_add_u32 m0, s28, 0x8000\n"
"v_mfma_f32_16x16x32_bf16 v[68:71], v[80:83], v[112:115], v[68:71]\n"
"ds_read_b64_tr_b16 v[80:81], v127 offset:0\n"
"ds_read_b64_tr_b16 v[82:83], v127 offset:4096\n"
"global_load_lds_dwordx4 v133, s[20:21]\n"
"v_mfma_f32_16x16x32_bf16 v[72:75], v[84:87], v[108:111], v[72:75]\n"
"v_mfma_f32_16x16x32_bf16 v[76:79], v[84:87], v[112:115], v[76:79]\n"
"s_waitcnt lgkmcnt(6)\n"
"v_mfma_f32_16x16x32_bf16 v[16:19], v[88:91], v[100:103], v[16:19]\n"
"ds_read_b64_tr_b16 v[84:85], v128 offset:0\n"
"ds_read_b64_tr_b16 v[86:87], v128 offset:4096\n"
"s_lshr_b32 s42, s16, 1\n"
"v_mfma_f32_16x16x32_bf16 v[20:23], v[88:91], v[104:107], v[20:23]\n"
"global_load_lds_dwordx4 v134, s[20:21] offset:1024\n"
"s_mul_i32 s42, s42, 0x9000\n"
"s_waitcnt lgkmcnt(6)\n"
"v_mfma_f32_16x16x32_bf16 v[24:27], v[92:95], v[100:103], v[24:27]\n"
"ds_read_b64_tr_b16 v[88:89], v129 offset:0\n"
"ds_read_b64_tr_b16 v[90:91], v129 offset:4096\n"
"s_add_u32 s40, s40, s42\n"
"v_mfma_f32_16x16x32_bf16 v[28:31], v[92:95], v[104:107], v[28:31]\n"
"global_load_lds_dwordx4 v135, s[20:21] offset:2048\n"
"s_add_u32 s50, s62, s40\n"
"s_addc_u32 s51, s63, 0\n"
"s_waitcnt lgkmcnt(6)\n"
"v_mfma_f32_16x16x32_bf16 v[32:35], v[96:99], v[100:103], v[32:35]\n"
"ds_read_b64_tr_b16 v[92:93], v130 offset:0\n"
"ds_read_b64_tr_b16 v[94:95], v130 offset:4096\n"
"s_sub_u32 s50, s50, 0x1000\n"
"s_subb_u32 s51, s51, 0\n"
"v_mfma_f32_16x16x32_bf16 v[36:39], v[96:99], v[104:107], v[36:39]\n"
"v_bfe_u32 v140, v1, 3, 1\n"
"s_waitcnt lgkmcnt(6)\n"
"v_mfma_f32_16x16x32_bf16 v[40:43], v[80:83], v[100:103], v[40:43]\n"
"ds_read_b64_tr_b16 v[96:97], v131 offset:0\n"
"ds_read_b64_tr_b16 v[98:99], v131 offset:4096\n"
"global_load_lds_dwordx4 v136, s[20:21] offset:3072\n"
"s_add_u32 s20, s20, 0x40000\n"
"s_addc_u32 s21, s21, 0\n"
"s_lshl_b32 s40, s43, 2\n"
"v_mfma_f32_16x16x32_bf16 v[44:47], v[80:83], v[104:107], v[44:47]\n"
"v_add_u32 v140, s40, v140\n"
"s_waitcnt lgkmcnt(6)\n"
"v_mfma_f32_16x16x32_bf16 v[48:51], v[84:87], v[100:103], v[48:51]\n"
"s_mov_b32 s40, 0x2400\n"
"v_mfma_f32_16x16x32_bf16 v[52:55], v[84:87], v[104:107], v[52:55]\n"
"s_waitcnt vmcnt(9) lgkmcnt(0)\n"
"s_barrier\n"
"ds_read_b128 v[108:111], v132 offset:20480\n"
"ds_read_b128 v[112:115], v132 offset:21504\n"
"ds_read_b64_tr_b16 v[80:81], v124 offset:32768\n"
"ds_read_b64_tr_b16 v[82:83], v124 offset:36864\n"
"s_add_u32 m0, s28, 0x10000\n"
"v_mul_u32_u24 v140, s40, v140\n"
"v_mfma_f32_16x16x32_bf16 v[56:59], v[88:91], v[100:103], v[56:59]\n"
"ds_read_b64_tr_b16 v[84:85], v125 offset:32768\n"
"ds_read_b64_tr_b16 v[86:87], v125 offset:36864\n"
"global_load_lds_dwordx4 v133, s[20:21]\n"
"v_mfma_f32_16x16x32_bf16 v[60:63], v[88:91], v[104:107], v[60:63]\n"
"ds_read_b64_tr_b16 v[88:89], v126 offset:32768\n"
"ds_read_b64_tr_b16 v[90:91], v126 offset:36864\n"
"v_mfma_f32_16x16x32_bf16 v[64:67], v[92:95], v[100:103], v[64:67]\n"
"v_mfma_f32_16x16x32_bf16 v[68:71], v[92:95], v[104:107], v[68:71]\n"
"ds_read_b64_tr_b16 v[92:93], v127 offset:32768\n"
"ds_read_b64_tr_b16 v[94:95], v127 offset:36864\n"
"global_load_lds_dwordx4 v134, s[20:21] offset:1024\n"
"v_mfma_f32_16x16x32_bf16 v[72:75], v[96:99], v[100:103], v[72:75]\n"
"v_mfma_f32_16x16x32_bf16 v[76:79], v[96:99], v[104:107], v[76:79]\n"
"s_waitcnt lgkmcnt(6)\n"
"v_mfma_f32_16x16x32_bf16 v[16:19], v[80:83], v[108:111], v[16:19]\n"
"ds_read_b64_tr_b16 v[96:97], v128 offset:32768\n"
"ds_read_b64_tr_b16 v[98:99], v128 offset:36864\n"
"v_and_b32 v141, 7, v1\n"
"v_mfma_f32_16x16x32_bf16 v[20:23], v[80:83], v[112:115], v[20:23]\n"
"global_load_lds_dwordx4 v135, s[20:21] offset:2048\n"
"v_lshl_add_u32 v140, v141, 4, v140\n"
"s_waitcnt lgkmcnt(6)\n"
"v_mfma_f32_16x16x32_bf16 v[24:27], v[84:87], v[108:111], v[24:27]\n"
"ds_read_b64_tr_b16 v[80:81], v129 offset:32768\n"
"ds_read_b64_tr_b16 v[82:83], v129 offset:36864\n"
"s_mul_i32 s42, s17, 0x300\n"
"v_mfma_f32_16x16x32_bf16 v[28:31], v[84:87], v[112:115], v[28:31]\n"
"s_add_u32 s42, s42, 0xc00\n"
"s_waitcnt lgkmcnt(6)\n"
"v_mfma_f32_16x16x32_bf16 v[32:35], v[88:91], v[108:111], v[32:35]\n"
"ds_read_b64_tr_b16 v[84:85], v130 offset:32768\n"
"ds_read_b64_tr_b16 v[86:87], v130 offset:36864\n"
"v_add_u32 v140, s42, v140\n"
"v_mfma_f32_16x16x32_bf16 v[36:39], v[88:91], v[112:115], v[36:39]\n"
"global_load_lds_dwordx4 v136, s[20:21] offset:3072\n"
"s_add_u32 s20, s20, 0x40000\n"
"s_addc_u32 s21, s21, 0\n"
"v_lshrrev_b32 v141, 4, v1\n"
"s_waitcnt lgkmcnt(6)\n"
"v_mfma_f32_16x16x32_bf16 v[40:43], v[92:95], v[108:111], v[40:43]\n"
"ds_read_b64_tr_b16 v[88:89], v131 offset:32768\n"
"ds_read_b64_tr_b16 v[90:91], v131 offset:36864\n"
"v_lshl_add_u32 v12, v141, 7, v140\n"
"v_mfma_f32_16x16x32_bf16 v[44:47], v[92:95], v[112:115], v[44:47]\n"
"v_lshrrev_b32 v142, 1, v141\n"
"s_waitcnt lgkmcnt(6)\n"
"v_mfma_f32_16x16x32_bf16 v[48:51], v[96:99], v[108:111], v[48:51]\n"
"v_lshl_add_u32 v140, v142, 7, v140\n"
"v_mfma_f32_16x16x32_bf16 v[52:55], v[96:99], v[112:115], v[52:55]\n"
"s_waitcnt vmcnt(9) lgkmcnt(0)\n"
"s_barrier\n"
"ds_read_b128 v[100:103], v132 offset:0\n"
"ds_read_b128 v[104:107], v132 offset:1024\n"
"ds_read_b64_tr_b16 v[92:93], v116 offset:0\n"
"ds_read_b64_tr_b16 v[94:95], v116 offset:4096\n"
"s_add_u32 m0, s38, 0x24000\n"
"v_add_u32 v140, 0x200, v140\n"
"v_mfma_f32_16x16x32_bf16 v[56:59], v[80:83], v[108:111], v[56:59]\n"
"ds_read_b64_tr_b16 v[96:97], v117 offset:0\n"
"ds_read_b64_tr_b16 v[98:99], v117 offset:4096\n"
"global_load_lds_dwordx4 v137, s[26:27] sc1\n"
"s_add_u32 s26, s26, 0x2000\n"
"s_addc_u32 s27, s27, 0\n"
"v_and_b32 v141, 1, v141\n"
"v_mfma_f32_16x16x32_bf16 v[60:63], v[80:83], v[112:115], v[60:63]\n"
"ds_read_b64_tr_b16 v[80:81], v118 offset:0\n"
"ds_read_b64_tr_b16 v[82:83], v118 offset:4096\n"
"s_mov_b32 s40, 0x4800\n"
"v_mfma_f32_16x16x32_bf16 v[64:67], v[84:87], v[108:111], v[64:67]\n"
"s_add_u32 m0, s28, 0x18000\n"
"v_mfma_f32_16x16x32_bf16 v[68:71], v[84:87], v[112:115], v[68:71]\n"
"ds_read_b64_tr_b16 v[84:85], v119 offset:0\n"
"ds_read_b64_tr_b16 v[86:87], v119 offset:4096\n"
"global_load_lds_dwordx4 v133, s[20:21]\n"
"v_mfma_f32_16x16x32_bf16 v[72:75], v[88:91], v[108:111], v[72:75]\n"
"v_mfma_f32_16x16x32_bf16 v[76:79], v[88:91], v[112:115], v[76:79]\n"
"s_waitcnt lgkmcnt(6)\n"
"v_mfma_f32_16x16x32_bf16 v[16:19], v[92:95], v[100:103], v[16:19]\n"
"ds_read_b64_tr_b16 v[88:89], v120 offset:0\n"
"ds_read_b64_tr_b16 v[90:91], v120 offset:4096\n"
"v_mul_u32_u24 v141, s40, v141\n"
"v_mfma_f32_16x16x32_bf16 v[20:23], v[92:95], v[104:107], v[20:23]\n"
"global_load_lds_dwordx4 v134, s[20:21] offset:1024\n"
"v_add_u32 v13, v140, v141\n"
"s_waitcnt lgkmcnt(6)\n"
"v_mfma_f32_16x16x32_bf16 v[24:27], v[96:99], v[100:103], v[24:27]\n"
"ds_read_b64_tr_b16 v[92:93], v121 offset:0\n"
"ds_read_b64_tr_b16 v[94:95], v121 offset:4096\n"
"s_mul_i32 s40, s18, 0x90000\n"
"v_mfma_f32_16x16x32_bf16 v[28:31], v[96:99], v[104:107], v[28:31]\n"
"global_load_lds_dwordx4 v135, s[20:21] offset:2048\n"
"s_mul_i32 s42, s19, 0x12000\n"
"s_waitcnt lgkmcnt(6)\n"
"v_mfma_f32_16x16x32_bf16 v[32:35], v[80:83], v[100:103], v[32:35]\n"
"ds_read_b64_tr_b16 v[96:97], v122 offset:0\n"
"ds_read_b64_tr_b16 v[98:99], v122 offset:4096\n"
"s_add_u32 s40, s40, s42\n"
"v_mfma_f32_16x16x32_bf16 v[36:39], v[80:83], v[104:107], v[36:39]\n"
"s_add_u32 s40, s40, 0xa8e000\n"
"s_waitcnt lgkmcnt(6)\n"
"v_mfma_f32_16x16x32_bf16 v[40:43], v[84:87], v[100:103], v[40:43]\n"
"ds_read_b64_tr_b16 v[80:81], v123 offset:0\n"
"ds_read_b64_tr_b16 v[82:83], v123 offset:4096\n"
"global_load_lds_dwordx4 v136, s[20:21] offset:3072\n"
"s_add_u32 s20, s20, 0x40000\n"
"s_addc_u32 s21, s21, 0\n"
"s_add_u32 s52, s4, s40\n"
"s_addc_u32 s53, s5, 0\n"
"v_mfma_f32_16x16x32_bf16 v[44:47], v[84:87], v[104:107], v[44:47]\n"
"s_add_u32 s54, s52, 0x4800\n"
"s_addc_u32 s55, s53, 0\n"
"s_waitcnt lgkmcnt(6)\n"
"v_mfma_f32_16x16x32_bf16 v[48:51], v[88:91], v[100:103], v[48:51]\n"
"v_bfe_u32 v141, v1, 2, 2\n"
"v_mfma_f32_16x16x32_bf16 v[52:55], v[88:91], v[104:107], v[52:55]\n"
"s_waitcnt vmcnt(9) lgkmcnt(0)\n"
"s_barrier\n"
"ds_read_b128 v[108:111], v132 offset:4096\n"
"ds_read_b128 v[112:115], v132 offset:5120\n"
"ds_read_b64_tr_b16 v[84:85], v116 offset:32768\n"
"ds_read_b64_tr_b16 v[86:87], v116 offset:36864\n"
"s_mov_b32 m0, s28\n"
"v_sub_u32 v141, 0, v141\n"
"v_mfma_f32_16x16x32_bf16 v[56:59], v[92:95], v[100:103], v[56:59]\n"
"ds_read_b64_tr_b16 v[88:89], v117 offset:32768\n"
"ds_read_b64_tr_b16 v[90:91], v117 offset:36864\n"
"global_load_lds_dwordx4 v133, s[20:21]\n"
"v_mfma_f32_16x16x32_bf16 v[60:63], v[92:95], v[104:107], v[60:63]\n"
"ds_read_b64_tr_b16 v[92:93], v118 offset:32768\n"
"ds_read_b64_tr_b16 v[94:95], v118 offset:36864\n"
"v_mfma_f32_16x16x32_bf16 v[64:67], v[96:99], v[100:103], v[64:67]\n"
"v_mfma_f32_16x16x32_bf16 v[68:71], v[96:99], v[104:107], v[68:71]\n"
"ds_read_b64_tr_b16 v[96:97], v119 offset:32768\n"
"ds_read_b64_tr_b16 v[98:99], v119 offset:36864\n"
"global_load_lds_dwordx4 v134, s[20:21] offset:1024\n"
"v_mfma_f32_16x16x32_bf16 v[72:75], v[80:83], v[100:103], v[72:75]\n"
"v_mfma_f32_16x16x32_bf16 v[76:79], v[80:83], v[104:107], v[76:79]\n"
"s_waitcnt lgkmcnt(6)\n"
"v_mfma_f32_16x16x32_bf16 v[16:19], v[84:87], v[108:111], v[16:19]\n"
"ds_read_b64_tr_b16 v[80:81], v120 offset:32768\n"
"ds_read_b64_tr_b16 v[82:83], v120 offset:36864\n"
"v_lshrrev_b32 v140, 5, v1\n"
"v_mfma_f32_16x16x32_bf16 v[20:23], v[84:87], v[112:115], v[20:23]\n"
"global_load_lds_dwordx4 v135, s[20:21] offset:2048\n"
"v_xor_b32 v141, v141, v140\n"
"s_waitcnt lgkmcnt(6)\n"
"v_mfma_f32_16x16x32_bf16 v[24:27], v[88:91], v[108:111], v[24:27]\n"
"ds_read_b64_tr_b16 v[84:85], v121 offset:32768\n"
"ds_read_b64_tr_b16 v[86:87], v121 offset:36864\n"
"v_and_b32 v141, 3, v141\n"
"v_mfma_f32_16x16x32_bf16 v[28:31], v[88:91], v[112:115], v[28:31]\n"
"v_lshlrev_b32 v141, 4, v141\n"
"s_waitcnt lgkmcnt(6)\n"
"v_mfma_f32_16x16x32_bf16 v[32:35], v[92:95], v[108:111], v[32:35]\n"
"ds_read_b64_tr_b16 v[88:89], v122 offset:32768\n"
"ds_read_b64_tr_b16 v[90:91], v122 offset:36864\n"
"v_and_b32 v140, 15, v1\n"
"v_mfma_f32_16x16x32_bf16 v[36:39], v[92:95], v[112:115], v[36:39]\n"
"global_load_lds_dwordx4 v136, s[20:21] offset:3072\n"
"s_add_u32 s20, s20, 0x40000\n"
"s_addc_u32 s21, s21, 0\n"
"v_lshl_or_b32 v140, v140, 6, v141\n"
"s_waitcnt lgkmcnt(6)\n"
"v_mfma_f32_16x16x32_bf16 v[40:43], v[96:99], v[108:111], v[40:43]\n"
"ds_read_b64_tr_b16 v[92:93], v123 offset:32768\n"
"ds_read_b64_tr_b16 v[94:95], v123 offset:36864\n"
"v_bfe_u32 v141, v1, 4, 1\n"
"v_mfma_f32_16x16x32_bf16 v[44:47], v[96:99], v[112:115], v[44:47]\n"
"v_lshl_or_b32 v140, v141, 3, v140\n"
"s_waitcnt lgkmcnt(6)\n"
"v_mfma_f32_16x16x32_bf16 v[48:51], v[80:83], v[108:111], v[48:51]\n"
"s_lshr_b32 s40, s16, 1\n"
"v_mfma_f32_16x16x32_bf16 v[52:55], v[80:83], v[112:115], v[52:55]\n"
"s_waitcnt vmcnt(9) lgkmcnt(0)\n"
"s_barrier\n"
"ds_read_b128 v[100:103], v132 offset:8192\n"
"ds_read_b128 v[104:107], v132 offset:9216\n"
"ds_read_b64_tr_b16 v[96:97], v124 offset:0\n"
"ds_read_b64_tr_b16 v[98:99], v124 offset:4096\n"
"s_add_u32 m0, s38, 0x20000\n"
"s_lshl_b32 s40, s40, 14\n"
"v_mfma_f32_16x16x32_bf16 v[56:59], v[84:87], v[108:111], v[56:59]\n"
"ds_read_b64_tr_b16 v[80:81], v125 offset:0\n"
"ds_read_b64_tr_b16 v[82:83], v125 offset:4096\n"
"global_load_lds_dwordx4 v137, s[26:27] sc1\n"
"s_add_u32 s26, s26, 0x2000\n"
"s_addc_u32 s27, s27, 0\n"
"s_and_b32 s41, s16, 1\n"
"v_mfma_f32_16x16x32_bf16 v[60:63], v[84:87], v[112:115], v[60:63]\n"
"ds_read_b64_tr_b16 v[84:85], v126 offset:0\n"
"ds_read_b64_tr_b16 v[86:87], v126 offset:4096\n"
"s_lshl_b32 s41, s41, 11\n"
"v_mfma_f32_16x16x32_bf16 v[64:67], v[88:91], v[108:111], v[64:67]\n"
"s_add_u32 m0, s28, 0x8000\n"
"v_mfma_f32_16x16x32_bf16 v[68:71], v[88:91], v[112:115], v[68:71]\n"
"ds_read_b64_tr_b16 v[88:89], v127 offset:0\n"
"ds_read_b64_tr_b16 v[90:91], v127 offset:4096\n"
"global_load_lds_dwordx4 v133, s[20:21]\n"
"v_mfma_f32_16x16x32_bf16 v[72:75], v[92:95], v[108:111], v[72:75]\n"
"v_mfma_f32_16x16x32_bf16 v[76:79], v[92:95], v[112:115], v[76:79]\n"
"s_waitcnt lgkmcnt(6)\n"
"v_mfma_f32_16x16x32_bf16 v[16:19], v[96:99], v[100:103], v[16:19]\n"
"ds_read_b64_tr_b16 v[92:93], v128 offset:0\n"
"ds_read_b64_tr_b16 v[94:95], v128 offset:4096\n"
"s_add_u32 s40, s40, s41\n"
"v_mfma_f32_16x16x32_bf16 v[20:23], v[96:99], v[104:107], v[20:23]\n"
"global_load_lds_dwordx4 v134, s[20:21] offset:1024\n"
"v_add_u32 v14, s40, v140\n"
"s_waitcnt lgkmcnt(6)\n"
"v_mfma_f32_16x16x32_bf16 v[24:27], v[80:83], v[100:103], v[24:27]\n"
"ds_read_b64_tr_b16 v[96:97], v129 offset:0\n"
"ds_read_b64_tr_b16 v[98:99], v129 offset:4096\n"
"s_cmp_ge_u32 s16, 3\ns_cselect_b32 s40, 192, 0\ns_cselect_b32 s41, 18, 0\ns_add_u32 s41, s41, 144\n"
"v_mfma_f32_16x16x32_bf16 v[28:31], v[80:83], v[104:107], v[28:31]\n"
"global_load_lds_dwordx4 v135, s[20:21] offset:2048\n"
"v_subrev_u32 v140, s40, v0\n"
"s_waitcnt lgkmcnt(6)\n"
"v_mfma_f32_16x16x32_bf16 v[32:35], v[84:87], v[100:103], v[32:35]\n"
"ds_read_b64_tr_b16 v[80:81], v130 offset:0\n"
"ds_read_b64_tr_b16 v[82:83], v130 offset:4096\n"
"v_min_u32 v141, 0xa1, v140\n"
"v_mfma_f32_16x16x32_bf16 v[36:39], v[84:87], v[104:107], v[36:39]\n"
"s_sub_u32 s42, s41, 1\n"
"s_waitcnt lgkmcnt(6)\n"
"v_mfma_f32_16x16x32_bf16 v[40:43], v[88:91], v[100:103], v[40:43]\n"
"ds_read_b64_tr_b16 v[84:85], v131 offset:0\n"
"ds_read_b64_tr_b16 v[86:87], v131 offset:4096\n"
"global_load_lds_dwordx4 v136, s[20:21] offset:3072\n"
"s_add_u32 s20, s20, 0x40000\n"
"s_addc_u32 s21, s21, 0\n"
"v_min_u32 v140, s42, v140\n"
"v_mfma_f32_16x16x32_bf16 v[44:47], v[88:91], v[104:107], v[44:47]\n"
"s_mul_i32 s40, s18, s41\n"
"s_waitcnt lgkmcnt(6)\n"
"v_mfma_f32_16x16x32_bf16 v[48:51], v[92:95], v[100:103], v[48:51]\n"
"v_add_u32 v140, s40, v140\n"
"v_mfma_f32_16x16x32_bf16 v[52:55], v[92:95], v[104:107], v[52:55]\n"
"s_waitcnt vmcnt(9) lgkmcnt(0)\n"
"s_barrier\n"
"ds_read_b128 v[108:111], v132 offset:12288\n"
"ds_read_b128 v[112:115], v132 offset:13312\n"
"ds_read_b64_tr_b16 v[88:89], v124 offset:32768\n"
"ds_read_b64_tr_b16 v[90:91], v124 offset:36864\n"
"s_add_u32 m0, s28, 0x10000\n"
"s_mul_i32 s41, s41, s19\n"
"v_mfma_f32_16x16x32_bf16 v[56:59], v[96:99], v[100:103], v[56:59]\n"
"ds_read_b64_tr_b16 v[92:93], v125 offset:32768\n"
"ds_read_b64_tr_b16 v[94:95], v125 offset:36864\n"
"global_load_lds_dwordx4 v133, s[20:21]\n"
"s_lshl_b32 s41, s41, 5\n"
"v_mfma_f32_16x16x32_bf16 v[60:63], v[96:99], v[104:107], v[60:63]\n"
"ds_read_b64_tr_b16 v[96:97], v126 offset:32768\n"
"ds_read_b64_tr_b16 v[98:99], v126 offset:36864\n"
"v_add_u32 v140, s41, v140\n"
"v_mfma_f32_16x16x32_bf16 v[64:67], v[80:83], v[100:103], v[64:67]\n"
"v_mfma_f32_16x16x32_bf16 v[68:71], v[80:83], v[104:107], v[68:71]\n"
"ds_read_b64_tr_b16 v[80:81], v127 offset:32768\n"
"ds_read_b64_tr_b16 v[82:83], v127 offset:36864\n"
"global_load_lds_dwordx4 v134, s[20:21] offset:1024\n"
"v_mfma_f32_16x16x32_bf16 v[72:75], v[84:87], v[100:103], v[72:75]\n"
"v_mfma_f32_16x16x32_bf16 v[76:79], v[84:87], v[104:107], v[76:79]\n"
"s_waitcnt lgkmcnt(6)\n"
"v_mfma_f32_16x16x32_bf16 v[16:19], v[88:91], v[108:111], v[16:19]\n"
"ds_read_b64_tr_b16 v[84:85], v128 offset:32768\n"
"ds_read_b64_tr_b16 v[86:87], v128 offset:36864\n"
"v_lshlrev_b32 v253, 7, v140\n"
"v_mfma_f32_16x16x32_bf16 v[20:23], v[88:91], v[112:115], v[20:23]\n"
"global_load_lds_dwordx4 v135, s[20:21] offset:2048\n"
"s_mul_i32 s40, s18, 0xa2\n"
"s_waitcnt lgkmcnt(6)\n"
"v_mfma_f32_16x16x32_bf16 v[24:27], v[92:95], v[108:111], v[24:27]\n"
"ds_read_b64_tr_b16 v[88:89], v129 offset:32768\n"
"ds_read_b64_tr_b16 v[90:91], v129 offset:36864\n"
"v_add_u32 v141, s40, v141\n"
"v_mfma_f32_16x16x32_bf16 v[28:31], v[92:95], v[112:115], v[28:31]\n"
"v_mul_u32_u24 v142, 0x1c72, v141\n"
"s_waitcnt lgkmcnt(6)\n"
"v_mfma_f32_16x16x32_bf16 v[32:35], v[96:99], v[108:111], v[32:35]\n"
"ds_read_b64_tr_b16 v[92:93], v130 offset:32768\n"
"ds_read_b64_tr_b16 v[94:95], v130 offset:36864\n"
"v_lshrrev_b32 v142, 16, v142\n"
"v_mfma_f32_16x16x32_bf16 v[36:39], v[96:99], v[112:115], v[36:39]\n"
"global_load_lds_dwordx4 v136, s[20:21] offset:3072\n"
"s_add_u32 s20, s20, 0x40000\n"
"s_addc_u32 s21, s21, 0\n"
"v_mul_u32_u24 v142, 0x1f80, v142\n"
"s_waitcnt lgkmcnt(6)\n"
"v_mfma_f32_16x16x32_bf16 v[40:43], v[80:83], v[108:111], v[40:43]\n"
"ds_read_b64_tr_b16 v[96:97], v131 offset:32768\n"
"ds_read_b64_tr_b16 v[98:99], v131 offset:36864\n"
"v_lshlrev_b32 v141, 7, v141\n"
"v_mfma_f32_16x16x32_bf16 v[44:47], v[80:83], v[112:115], v[44:47]\n"
"v_add_u32 v141, v141, v142\n"
"s_waitcnt lgkmcnt(6)\n"
"v_mfma_f32_16x16x32_bf16 v[48:51], v[84:87], v[108:111], v[48:51]\n"
"s_mul_i32 s40, s19, 0x480\n"
"v_mfma_f32_16x16x32_bf16 v[52:55], v[84:87], v[112:115], v[52:55]\n"
"s_waitcnt vmcnt(9) lgkmcnt(0)\n"
"s_barrier\n"
"ds_read_b128 v[100:103], v132 offset:16384\n"
"ds_read_b128 v[104:107], v132 offset:17408\n"
"ds_read_b64_tr_b16 v[80:81], v116 offset:0\n"
"ds_read_b64_tr_b16 v[82:83], v116 offset:4096\n"
"s_add_u32 m0, s38, 0x22000\n"
"v_add_u32 v254, s40, v141\n"
"v_mfma_f32_16x16x32_bf16 v[56:59], v[88:91], v[108:111], v[56:59]\n"
"ds_read_b64_tr_b16 v[84:85], v117 offset:0\n"
"ds_read_b64_tr_b16 v[86:87], v117 offset:4096\n"
"global_load_lds_dwordx4 v137, s[26:27] sc1\n"
"s_add_u32 s26, s26, 0x2000\n"
"s_addc_u32 s27, s27, 0\n"
"s_lshl_b32 s40, s18, 6\n"
"v_mfma_f32_16x16x32_bf16 v[60:63], v[88:91], v[112:115], v[60:63]\n"
"ds_read_b64_tr_b16 v[88:89], v118 offset:0\n"
"ds_read_b64_tr_b16 v[90:91], v118 offset:4096\n"
"v_add_u32 v138, s40, v1\n"
"v_mfma_f32_16x16x32_bf16 v[64:67], v[92:95], v[108:111], v[64:67]\n"
"s_add_u32 m0, s28, 0x18000\n"
"v_mfma_f32_16x16x32_bf16 v[68:71], v[92:95], v[112:115], v[68:71]\n"
"ds_read_b64_tr_b16 v[92:93], v119 offset:0\n"
"ds_read_b64_tr_b16 v[94:95], v119 offset:4096\n"
"global_load_lds_dwordx4 v133, s[20:21]\n"
"v_mfma_f32_16x16x32_bf16 v[72:75], v[96:99], v[108:111], v[72:75]\n"
"v_mfma_f32_16x16x32_bf16 v[76:79], v[96:99], v[112:115], v[76:79]\n"
"s_waitcnt lgkmcnt(6)\n"
"v_mfma_f32_16x16x32_bf16 v[16:19], v[80:83], v[100:103], v[16:19]\n"
"ds_read_b64_tr_b16 v[96:97], v120 offset:0\n"
"ds_read_b64_tr_b16 v[98:99], v120 offset:4096\n"
"v_lshrrev_b32 v142, 10, v138\n"
"v_mfma_f32_16x16x32_bf16 v[20:23], v[80:83], v[104:107], v[20:23]\n"
"global_load_lds_dwordx4 v134, s[20:21] offset:1024\n"
"v_and_b32 v138, 0x3ff, v138\n"
"s_waitcnt lgkmcnt(6)\n"
"v_mfma_f32_16x16x32_bf16 v[24:27], v[84:87], v[100:103], v[24:27]\n"
"ds_read_b64_tr_b16 v[80:81], v121 offset:0\n"
"ds_read_b64_tr_b16 v[82:83], v121 offset:4096\n"
"v_lshlrev_b32 v138, 7, v138\n"
"v_mfma_f32_16x16x32_bf16 v[28:31], v[84:87], v[104:107], v[28:31]\n"
"global_load_lds_dwordx4 v135, s[20:21] offset:2048\n"
"v_lshl_or_b32 v138, v142, 19, v138\n"
"s_waitcnt lgkmcnt(6)\n"
"v_mfma_f32_16x16x32_bf16 v[32:35], v[88:91], v[100:103], v[32:35]\n"
"ds_read_b64_tr_b16 v[84:85], v122 offset:0\n"
"ds_read_b64_tr_b16 v[86:87], v122 offset:4096\n"
"s_lshl_b32 s40, s19, 20\n"
"v_mfma_f32_16x16x32_bf16 v[36:39], v[88:91], v[104:107], v[36:39]\n"
"s_add_u32 s40, s40, 0x60000\n"
"s_waitcnt lgkmcnt(6)\n"
"v_mfma_f32_16x16x32_bf16 v[40:43], v[92:95], v[100:103], v[40:43]\n"
"ds_read_b64_tr_b16 v[88:89], v123 offset:0\n"
"ds_read_b64_tr_b16 v[90:91], v123 offset:4096\n"
"global_load_lds_dwordx4 v136, s[20:21] offset:3072\n"
"s_add_u32 s20, s20, 0x40000\n"
"s_addc_u32 s21, s21, 0\n"
"v_add_u32 v138, s40, v138\n"
"v_mfma_f32_16x16x32_bf16 v[44:47], v[92:95], v[104:107], v[44:47]\n"
"s_waitcnt lgkmcnt(6)\n"
"v_mfma_f32_16x16x32_bf16 v[48:51], v[96:99], v[100:103], v[48:51]\n"
"v_mfma_f32_16x16x32_bf16 v[52:55], v[96:99], v[104:107], v[52:55]\n"
"s_waitcnt vmcnt(9) lgkmcnt(0)\n"
"s_barrier\n"
"ds_read_b128 v[108:111], v132 offset:20480\n"
"ds_read_b128 v[112:115], v132 offset:21504\n"
"ds_read_b64_tr_b16 v[92:93], v116 offset:32768\n"
"ds_read_b64_tr_b16 v[94:95], v116 offset:36864\n"
"s_mov_b32 m0, s28\n"
"v_mfma_f32_16x16x32_bf16 v[56:59], v[80:83], v[100:103], v[56:59]\n"
"ds_read_b64_tr_b16 v[96:97], v117 offset:32768\n"
"ds_read_b64_tr_b16 v[98:99], v117 offset:36864\n"
"global_load_lds_dwordx4 v133, s[20:21]\n"
"v_mfma_f32_16x16x32_bf16 v[60:63], v[80:83], v[104:107], v[60:63]\n"
"ds_read_b64_tr_b16 v[80:81], v118 offset:32768\n"
"ds_read_b64_tr_b16 v[82:83], v118 offset:36864\n"
"v_mfma_f32_16x16x32_bf16 v[64:67], v[84:87], v[100:103], v[64:67]\n"
"v_mfma_f32_16x16x32_bf16 v[68:71], v[84:87], v[104:107], v[68:71]\n"
"ds_read_b64_tr_b16 v[84:85], v119 offset:32768\n"
"ds_read_b64_tr_b16 v[86:87], v119 offset:36864\n"
"global_load_lds_dwordx4 v134, s[20:21] offset:1024\n"
"v_mfma_f32_16x16x32_bf16 v[72:75], v[88:91], v[100:103], v[72:75]\n"
"v_mfma_f32_16x16x32_bf16 v[76:79], v[88:91], v[104:107], v[76:79]\n"
"s_waitcnt lgkmcnt(6)\n"
"v_mfma_f32_16x16x32_bf16 v[16:19], v[92:95], v[108:111], v[16:19]\n"
"ds_read_b64_tr_b16 v[88:89], v120 offset:32768\n"
"ds_read_b64_tr_b16 v[90:91], v120 offset:36864\n"
"v_mfma_f32_16x16x32_bf16 v[20:23], v[92:95], v[112:115], v[20:23]\n"
"global_load_lds_dwordx4 v135, s[20:21] offset:2048\n"
"s_waitcnt lgkmcnt(6)\n"
"v_mfma_f32_16x16x32_bf16 v[24:27], v[96:99], v[108:111], v[24:27]\n"
"ds_read_b64_tr_b16 v[92:93], v121 offset:32768\n"
"ds_read_b64_tr_b16 v[94:95], v121 offset:36864\n"
"v_mfma_f32_16x16x32_bf16 v[28:31], v[96:99], v[112:115], v[28:31]\n"
"s_waitcnt lgkmcnt(6)\n"
"v_mfma_f32_16x16x32_bf16 v[32:35], v[80:83], v[108:111], v[32:35]\n"
"ds_read_b64_tr_b16 v[96:97], v122 offset:32768\n"
"ds_read_b64_tr_b16 v[98:99], v122 offset:36864\n"
"v_mfma_f32_16x16x32_bf16 v[36:39], v[80:83], v[112:115], v[36:39]\n"
"global_load_lds_dwordx4 v136, s[20:21] offset:3072\n"
"s_add_u32 s20, s20, 0x40000\n"
"s_addc_u32 s21, s21, 0\n"
"s_waitcnt lgkmcnt(6)\n"
"v_mfma_f32_16x16x32_bf16 v[40:43], v[84:87], v[108:111], v[40:43]\n"
"ds_read_b64_tr_b16 v[80:81], v123 offset:32768\n"
"ds_read_b64_tr_b16 v[82:83], v123 offset:36864\n"
"v_mfma_f32_16x16x32_bf16 v[44:47], v[84:87], v[112:115], v[44:47]\n"
"s_waitcnt lgkmcnt(6)\n"
"v_mfma_f32_16x16x32_bf16 v[48:51], v[88:91], v[108:111], v[48:51]\n"
"v_mfma_f32_16x16x32_bf16 v[52:55], v[88:91], v[112:115], v[52:55]\n"
"s_waitcnt vmcnt(9) lgkmcnt(0)\n"
"s_barrier\n"
"ds_read_b128 v[100:103], v132 offset:0\n"
"ds_read_b128 v[104:107], v132 offset:1024\n"
"ds_read_b64_tr_b16 v[84:85], v124 offset:0\n"
"ds_read_b64_tr_b16 v[86:87], v124 offset:4096\n"
"s_add_u32 m0, s38, 0x24000\n"
"v_mfma_f32_16x16x32_bf16 v[56:59], v[92:95], v[108:111], v[56:59]\n"
"ds_read_b64_tr_b16 v[88:89], v125 offset:0\n"
"ds_read_b64_tr_b16 v[90:91], v125 offset:4096\n"
"global_load_lds_dwordx4 v137, s[26:27] sc1\n"
"s_add_u32 s26, s26, 0x2000\n"
"s_addc_u32 s27, s27, 0\n"
"v_mfma_f32_16x16x32_bf16 v[60:63], v[92:95], v[112:115], v[60:63]\n"
"ds_read_b64_tr_b16 v[92:93], v126 offset:0\n"
"ds_read_b64_tr_b16 v[94:95], v126 offset:4096\n"
"v_mfma_f32_16x16x32_bf16 v[64:67], v[96:99], v[108:111], v[64:67]\n"
"s_add_u32 m0, s28, 0x8000\n"
"v_mfma_f32_16x16x32_bf16 v[68:71], v[96:99], v[112:115], v[68:71]\n"
"ds_read_b64_tr_b16 v[96:97], v127 offset:0\n"
"ds_read_b64_tr_b16 v[98:99], v127 offset:4096\n"
"global_load_lds_dwordx4 v133, s[20:21]\n"
"v_mfma_f32_16x16x32_bf16 v[72:75], v[80:83], v[108:111], v[72:75]\n"
"v_mfma_f32_16x16x32_bf16 v[76:79], v[80:83], v[112:115], v[76:79]\n"
"s_waitcnt lgkmcnt(6)\n"
"v_mfma_f32_16x16x32_bf16 v[16:19], v[84:87], v[100:103], v[16:19]\n"
"ds_read_b64_tr_b16 v[80:81], v128 offset:0\n"
"ds_read_b64_tr_b16 v[82:83], v128 offset:4096\n"
"v_mfma_f32_16x16x32_bf16 v[20:23], v[84:87], v[104:107], v[20:23]\n"
"global_load_lds_dwordx4 v134, s[20:21] offset:1024\n"
"s_waitcnt lgkmcnt(6)\n"
"v_mfma_f32_16x16x32_bf16 v[24:27], v[88:91], v[100:103], v[24:27]\n"
"ds_read_b64_tr_b16 v[84:85], v129 offset:0\n"
"ds_read_b64_tr_b16 v[86:87], v129 offset:4096\n"
"v_mfma_f32_16x16x32_bf16 v[28:31], v[88:91], v[104:107], v[28:31]\n"
"global_load_lds_dwordx4 v135, s[20:21] offset:2048\n"
"s_waitcnt lgkmcnt(6)\n"
"v_mfma_f32_16x16x32_bf16 v[32:35], v[92:95], v[100:103], v[32:35]\n"
"ds_read_b64_tr_b16 v[88:89], v130 offset:0\n"
"ds_read_b64_tr_b16 v[90:91], v130 offset:4096\n"
"v_mfma_f32_16x16x32_bf16 v[36:39], v[92:95], v[104:107], v[36:39]\n"
"s_waitcnt lgkmcnt(6)\n"
"v_mfma_f32_16x16x32_bf16 v[40:43], v[96:99], v[100:103], v[40:43]\n"
"ds_read_b64_tr_b16 v[92:93], v131 offset:0\n"
"ds_read_b64_tr_b16 v[94:95], v131 offset:4096\n"
"global_load_lds_dwordx4 v136, s[20:21] offset:3072\n"
"s_add_u32 s20, s20, 0x40000\n"
"s_addc_u32 s21, s21, 0\n"
"v_mfma_f32_16x16x32_bf16 v[44:47], v[96:99], v[104:107], v[44:47]\n"
"s_waitcnt lgkmcnt(6)\n"
"v_mfma_f32_16x16x32_bf16 v[48:51], v[80:83], v[100:103], v[48:51]\n"
"v_mfma_f32_16x16x32_bf16 v[52:55], v[80:83], v[104:107], v[52:55]\n"
"s_waitcnt vmcnt(9) lgkmcnt(0)\n"
"s_barrier\n"
"ds_read_b128 v[108:111], v132 offset:4096\n"
"ds_read_b128 v[112:115], v132 offset:5120\n"
"ds_read_b64_tr_b16 v[96:97], v124 offset:32768\n"
"ds_read_b64_tr_b16 v[98:99], v124 offset:36864\n"
"s_add_u32 m0, s28, 0x10000\n"
"v_mfma_f32_16x16x32_bf16 v[56:59], v[84:87], v[100:103], v[56:59]\n"
"ds_read_b64_tr_b16 v[80:81], v125 offset:32768\n"
"ds_read_b64_tr_b16 v[82:83], v125 offset:36864\n"
"global_load_lds_dwordx4 v133, s[20:21]\n"
"v_mfma_f32_16x16x32_bf16 v[60:63], v[84:87], v[104:107], v[60:63]\n"
"ds_read_b64_tr_b16 v[84:85], v126 offset:32768\n"
"ds_read_b64_tr_b16 v[86:87], v126 offset:36864\n"
"v_mfma_f32_16x16x32_bf16 v[64:67], v[88:91], v[100:103], v[64:67]\n"
"v_mfma_f32_16x16x32_bf16 v[68:71], v[88:91], v[104:107], v[68:71]\n"
"ds_read_b64_tr_b16 v[88:89], v127 offset:32768\n"
"ds_read_b64_tr_b16 v[90:91], v127 offset:36864\n"
"global_load_lds_dwordx4 v134, s[20:21] offset:1024\n"
"v_mfma_f32_16x16x32_bf16 v[72:75], v[92:95], v[100:103], v[72:75]\n"
"v_mfma_f32_16x16x32_bf16 v[76:79], v[92:95], v[104:107], v[76:79]\n"
"s_waitcnt lgkmcnt(6)\n"
"v_mfma_f32_16x16x32_bf16 v[16:19], v[96:99], v[108:111], v[16:19]\n"
"ds_read_b64_tr_b16 v[92:93], v128 offset:32768\n"
"ds_read_b64_tr_b16 v[94:95], v128 offset:36864\n"
"v_mfma_f32_16x16x32_bf16 v[20:23], v[96:99], v[112:115], v[20:23]\n"
"global_load_lds_dwordx4 v135, s[20:21] offset:2048\n"
"s_waitcnt lgkmcnt(6)\n"
"v_mfma_f32_16x16x32_bf16 v[24:27], v[80:83], v[108:111], v[24:27]\n"
"ds_read_b64_tr_b16 v[96:97], v129 offset:32768\n"
"ds_read_b64_tr_b16 v[98:99], v129 offset:36864\n"
"v_mfma_f32_16x16x32_bf16 v[28:31], v[80:83], v[112:115], v[28:31]\n"
"s_waitcnt lgkmcnt(6)\n"
"v_mfma_f32_16x16x32_bf16 v[32:35], v[84:87], v[108:111], v[32:35]\n"
"ds_read_b64_tr_b16 v[80:81], v130 offset:32768\n"
"ds_read_b64_tr_b16 v[82:83], v130 offset:36864\n"
"v_mfma_f32_16x16x32_bf16 v[36:39], v[84:87], v[112:115], v[36:39]\n"
"global_load_lds_dwordx4 v136, s[20:21] offset:3072\n"
"s_add_u32 s20, s20, 0x40000\n"
"s_addc_u32 s21, s21, 0\n"
"s_waitcnt lgkmcnt(6)\n"
"v_mfma_f32_16x16x32_bf16 v[40:43], v[88:91], v[108:111], v[40:43]\n"
"ds_read_b64_tr_b16 v[84:85], v131 offset:32768\n"
"ds_read_b64_tr_b16 v[86:87], v131 offset:36864\n"
"v_mfma_f32_16x16x32_bf16 v[44:47], v[88:91], v[112:115], v[44:47]\n"
"s_waitcnt lgkmcnt(6)\n"
"v_mfma_f32_16x16x32_bf16 v[48:51], v[92:95], v[108:111], v[48:51]\n"
"v_mfma_f32_16x16x32_bf16 v[52:55], v[92:95], v[112:115], v[52:55]\n"
"s_waitcnt vmcnt(9) lgkmcnt(0)\n"
"s_barrier\n"
"ds_read_b128 v[100:103], v132 offset:8192\n"
"ds_read_b128 v[104:107], v132 offset:9216\n"
"ds_read_b64_tr_b16 v[88:89], v116 offset:0\n"
"ds_read_b64_tr_b16 v[90:91], v116 offset:4096\n"
"s_add_u32 m0, s28, 0x18000\n"
"v_mfma_f32_16x16x32_bf16 v[56:59], v[96:99], v[108:111], v[56:59]\n"
"ds_read_b64_tr_b16 v[92:93], v117 offset:0\n"
"ds_read_b64_tr_b16 v[94:95], v117 offset:4096\n"
"global_load_lds_dwordx4 v133, s[20:21]\n"
"v_mfma_f32_16x16x32_bf16 v[60:63], v[96:99], v[112:115], v[60:63]\n"
"ds_read_b64_tr_b16 v[96:97], v118 offset:0\n"
"ds_read_b64_tr_b16 v[98:99], v118 offset:4096\n"
"v_mfma_f32_16x16x32_bf16 v[64:67], v[80:83], v[108:111], v[64:67]\n"
"v_mfma_f32_16x16x32_bf16 v[68:71], v[80:83], v[112:115], v[68:71]\n"
"ds_read_b64_tr_b16 v[80:81], v119 offset:0\n"
"ds_read_b64_tr_b16 v[82:83], v119 offset:4096\n"
"global_load_lds_dwordx4 v134, s[20:21] offset:1024\n"
"v_mfma_f32_16x16x32_bf16 v[72:75], v[84:87], v[108:111], v[72:75]\n"
"v_mfma_f32_16x16x32_bf16 v[76:79], v[84:87], v[112:115], v[76:79]\n"
"s_waitcnt lgkmcnt(6)\n"
"v_mfma_f32_16x16x32_bf16 v[16:19], v[88:91], v[100:103], v[16:19]\n"
"ds_read_b64_tr_b16 v[84:85], v120 offset:0\n"
"ds_read_b64_tr_b16 v[86:87], v120 offset:4096\n"
"v_mfma_f32_16x16x32_bf16 v[20:23], v[88:91], v[104:107], v[20:23]\n"
"global_load_lds_dwordx4 v135, s[20:21] offset:2048\n"
"s_waitcnt lgkmcnt(6)\n"
"v_mfma_f32_16x16x32_bf16 v[24:27], v[92:95], v[100:103], v[24:27]\n"
"ds_read_b64_tr_b16 v[88:89], v121 offset:0\n"
"ds_read_b64_tr_b16 v[90:91], v121 offset:4096\n"
"v_mfma_f32_16x16x32_bf16 v[28:31], v[92:95], v[104:107], v[28:31]\n"
"global_load_lds_dwordx4 v136, s[20:21] offset:3072\n"
"s_add_u32 s20, s20, 0x40000\n"
"s_addc_u32 s21, s21, 0\n"
"s_waitcnt lgkmcnt(6)\n"
"v_mfma_f32_16x16x32_bf16 v[32:35], v[96:99], v[100:103], v[32:35]\n"
"ds_read_b64_tr_b16 v[92:93], v122 offset:0\n"
"ds_read_b64_tr_b16 v[94:95], v122 offset:4096\n"
"v_mfma_f32_16x16x32_bf16 v[36:39], v[96:99], v[104:107], v[36:39]\n"
"s_waitcnt lgkmcnt(6)\n"
"v_mfma_f32_16x16x32_bf16 v[40:43], v[80:83], v[100:103], v[40:43]\n"
"ds_read_b64_tr_b16 v[96:97], v123 offset:0\n"
"ds_read_b64_tr_b16 v[98:99], v123 offset:4096\n"
"s_cmp_eq_u32 s16, 5\ns_cselect_b64 exec, -1, 0\n"
"global_load_dword v139, v138, s[10:11] offset:0\n"
"s_cmp_eq_u32 s16, 6\ns_cselect_b64 exec, -1, 0\n"
"global_load_dword v139, v138, s[12:13] offset:0\n"
"s_mov_b64 exec, -1\n"
"v_mfma_f32_16x16x32_bf16 v[44:47], v[80:83], v[104:107], v[44:47]\n"
"s_waitcnt lgkmcnt(6)\n"
"v_mfma_f32_16x16x32_bf16 v[48:51], v[84:87], v[100:103], v[48:51]\n"
"v_mfma_f32_16x16x32_bf16 v[52:55], v[84:87], v[104:107], v[52:55]\n"
"s_waitcnt vmcnt(8) lgkmcnt(0)\n"
"s_barrier\n"
"ds_read_b128 v[108:111], v132 offset:12288\n"
"ds_read_b128 v[112:115], v132 offset:13312\n"
"ds_read_b64_tr_b16 v[80:81], v116 offset:32768\n"
"ds_read_b64_tr_b16 v[82:83], v116 offset:36864\n"
"s_mov_b32 m0, s29\n"
"v_mfma_f32_16x16x32_bf16 v[56:59], v[88:91], v[100:103], v[56:59]\n"
"ds_read_b64_tr_b16 v[84:85], v117 offset:32768\n"
"ds_read_b64_tr_b16 v[86:87], v117 offset:36864\n"
"global_load_lds_dwordx4 v240, s[30:31]\n"
"v_mfma_f32_16x16x32_bf16 v[60:63], v[88:91], v[104:107], v[60:63]\n"
"ds_read_b64_tr_b16 v[88:89], v118 offset:32768\n"
"ds_read_b64_tr_b16 v[90:91], v118 offset:36864\n"
"v_mfma_f32_16x16x32_bf16 v[64:67], v[92:95], v[100:103], v[64:67]\n"
"v_mfma_f32_16x16x32_bf16 v[68:71], v[92:95], v[104:107], v[68:71]\n"
"ds_read_b64_tr_b16 v[92:93], v119 offset:32768\n"
"ds_read_b64_tr_b16 v[94:95], v119 offset:36864\n"
"global_load_lds_dwordx4 v241, s[30:31] offset:1024\n"
"s_add_u32 s30, s30, 0x2000\n"
"s_addc_u32 s31, s31, 0\n"
"v_mfma_f32_16x16x32_bf16 v[72:75], v[96:99], v[100:103], v[72:75]\n"
"v_mfma_f32_16x16x32_bf16 v[76:79], v[96:99], v[104:107], v[76:79]\n"
"s_waitcnt lgkmcnt(6)\n"
"v_mfma_f32_16x16x32_bf16 v[16:19], v[80:83], v[108:111], v[16:19]\n"
"ds_read_b64_tr_b16 v[96:97], v120 offset:32768\n"
"ds_read_b64_tr_b16 v[98:99], v120 offset:36864\n"
"v_mfma_f32_16x16x32_bf16 v[20:23], v[80:83], v[112:115], v[20:23]\n"
"s_waitcnt lgkmcnt(6)\n"
"v_mfma_f32_16x16x32_bf16 v[24:27], v[84:87], v[108:111], v[24:27]\n"
"ds_read_b64_tr_b16 v[80:81], v121 offset:32768\n"
"ds_read_b64_tr_b16 v[82:83], v121 offset:36864\n"
"v_mfma_f32_16x16x32_bf16 v[28:31], v[84:87], v[112:115], v[28:31]\n"
"s_waitcnt lgkmcnt(6)\n"
"v_mfma_f32_16x16x32_bf16 v[32:35], v[88:91], v[108:111], v[32:35]\n"
"ds_read_b64_tr_b16 v[84:85], v122 offset:32768\n"
"ds_read_b64_tr_b16 v[86:87], v122 offset:36864\n"
"v_mfma_f32_16x16x32_bf16 v[36:39], v[88:91], v[112:115], v[36:39]\n"
"s_waitcnt lgkmcnt(6)\n"
"v_mfma_f32_16x16x32_bf16 v[40:43], v[92:95], v[108:111], v[40:43]\n"
"ds_read_b64_tr_b16 v[88:89], v123 offset:32768\n"
"ds_read_b64_tr_b16 v[90:91], v123 offset:36864\n"
"v_mfma_f32_16x16x32_bf16 v[44:47], v[92:95], v[112:115], v[44:47]\n"
"s_waitcnt lgkmcnt(6)\n"
"v_mfma_f32_16x16x32_bf16 v[48:51], v[96:99], v[108:111], v[48:51]\n"
"v_mfma_f32_16x16x32_bf16 v[52:55], v[96:99], v[112:115], v[52:55]\n"
"s_waitcnt vmcnt(6) lgkmcnt(0)\n"
"s_barrier\n"
"ds_read_b128 v[100:103], v132 offset:16384\n"
"ds_read_b128 v[104:107], v132 offset:17408\n"
"ds_read_b64_tr_b16 v[92:93], v124 offset:0\n"
"ds_read_b64_tr_b16 v[94:95], v124 offset:4096\n"
"s_add_u32 m0, s29, 0x8000\n"
"v_mfma_f32_16x16x32_bf16 v[56:59], v[80:83], v[108:111], v[56:59]\n"
"ds_read_b64_tr_b16 v[96:97], v125 offset:0\n"
"ds_read_b64_tr_b16 v[98:99], v125 offset:4096\n"
"global_load_lds_dwordx4 v240, s[30:31]\n"
"v_mfma_f32_16x16x32_bf16 v[60:63], v[80:83], v[112:115], v[60:63]\n"
"ds_read_b64_tr_b16 v[80:81], v126 offset:0\n"
"ds_read_b64_tr_b16 v[82:83], v126 offset:4096\n"
"v_mfma_f32_16x16x32_bf16 v[64:67], v[84:87], v[108:111], v[64:67]\n"
"v_mfma_f32_16x16x32_bf16 v[68:71], v[84:87], v[112:115], v[68:71]\n"
"ds_read_b64_tr_b16 v[84:85], v127 offset:0\n"
"ds_read_b64_tr_b16 v[86:87], v127 offset:4096\n"
"global_load_lds_dwordx4 v241, s[30:31] offset:1024\n"
"s_add_u32 s30, s30, 0x2000\n"
"s_addc_u32 s31, s31, 0\n"
"v_mfma_f32_16x16x32_bf16 v[72:75], v[88:91], v[108:111], v[72:75]\n"
"v_mfma_f32_16x16x32_bf16 v[76:79], v[88:91], v[112:115], v[76:79]\n"
"s_waitcnt lgkmcnt(6)\n"
"v_mfma_f32_16x16x32_bf16 v[16:19], v[92:95], v[100:103], v[16:19]\n"
"ds_read_b64_tr_b16 v[88:89], v128 offset:0\n"
"ds_read_b64_tr_b16 v[90:91], v128 offset:4096\n"
"v_mfma_f32_16x16x32_bf16 v[20:23], v[92:95], v[104:107], v[20:23]\n"
"s_waitcnt lgkmcnt(6)\n"
"v_mfma_f32_16x16x32_bf16 v[24:27], v[96:99], v[100:103], v[24:27]\n"
"ds_read_b64_tr_b16 v[92:93], v129 offset:0\n"
"ds_read_b64_tr_b16 v[94:95], v129 offset:4096\n"
"v_mfma_f32_16x16x32_bf16 v[28:31], v[96:99], v[104:107], v[28:31]\n"
"s_waitcnt lgkmcnt(6)\n"
"v_mfma_f32_16x16x32_bf16 v[32:35], v[80:83], v[100:103], v[32:35]\n"
"ds_read_b64_tr_b16 v[96:97], v130 offset:0\n"
"ds_read_b64_tr_b16 v[98:99], v130 offset:4096\n"
"v_mfma_f32_16x16x32_bf16 v[36:39], v[80:83], v[104:107], v[36:39]\n"
"s_waitcnt lgkmcnt(6)\n"
"v_mfma_f32_16x16x32_bf16 v[40:43], v[84:87], v[100:103], v[40:43]\n"
"ds_read_b64_tr_b16 v[80:81], v131 offset:0\n"
"ds_read_b64_tr_b16 v[82:83], v131 offset:4096\n"
"v_mfma_f32_16x16x32_bf16 v[44:47], v[84:87], v[104:107], v[44:47]\n"
"s_waitcnt lgkmcnt(6)\n"
"v_mfma_f32_16x16x32_bf16 v[48:51], v[88:91], v[100:103], v[48:51]\n"
"v_mfma_f32_16x16x32_bf16 v[52:55], v[88:91], v[104:107], v[52:55]\n"
"s_waitcnt vmcnt(4) lgkmcnt(0)\n"
"s_barrier\n"
"ds_read_b128 v[108:111], v132 offset:20480\n"
"ds_read_b128 v[112:115], v132 offset:21504\n"
"ds_read_b64_tr_b16 v[84:85], v124 offset:32768\n"
"ds_read_b64_tr_b16 v[86:87], v124 offset:36864\n"
"s_mul_i32 s40, s18, 0x12000\n"
"s_add_u32 s40, s40, 0xe000\n"
"s_add_u32 s24, s4, s40\n"
"s_addc_u32 s25, s5, 0\n"
"v_mfma_f32_16x16x32_bf16 v[56:59], v[92:95], v[100:103], v[56:59]\n"
"ds_read_b64_tr_b16 v[88:89], v125 offset:32768\n"
"ds_read_b64_tr_b16 v[90:91], v125 offset:36864\n"
"global_store_dwordx4 v144, v[148:151], s[24:25] offset:0 sc1\n"
"s_mov_b64 exec, 0x11111111\n"
"global_store_dwordx4 v145, v[148:151], s[24:25] offset:0 sc1\n"
"s_mov_b64 exec, -1\n"
"v_mfma_f32_16x16x32_bf16 v[60:63], v[92:95], v[104:107], v[60:63]\n"
"ds_read_b64_tr_b16 v[92:93], v126 offset:32768\n"
"ds_read_b64_tr_b16 v[94:95], v126 offset:36864\n"
"v_mfma_f32_16x16x32_bf16 v[64:67], v[96:99], v[100:103], v[64:67]\n"
"v_mfma_f32_16x16x32_bf16 v[68:71], v[96:99], v[104:107], v[68:71]\n"
"ds_read_b64_tr_b16 v[96:97], v127 offset:32768\n"
"ds_read_b64_tr_b16 v[98:99], v127 offset:36864\n"
"v_mfma_f32_16x16x32_bf16 v[72:75], v[80:83], v[100:103], v[72:75]\n"
"v_mfma_f32_16x16x32_bf16 v[76:79], v[80:83], v[104:107], v[76:79]\n"
"s_waitcnt lgkmcnt(6)\n"
"v_mfma_f32_16x16x32_bf16 v[16:19], v[84:87], v[108:111], v[16:19]\n"
"ds_read_b64_tr_b16 v[80:81], v128 offset:32768\n"
"ds_read_b64_tr_b16 v[82:83], v128 offset:36864\n"
"v_mfma_f32_16x16x32_bf16 v[20:23], v[84:87], v[112:115], v[20:23]\n"
"s_waitcnt lgkmcnt(6)\n"
"v_mfma_f32_16x16x32_bf16 v[24:27], v[88:91], v[108:111], v[24:27]\n"
"ds_read_b64_tr_b16 v[84:85], v129 offset:32768\n"
"ds_read_b64_tr_b16 v[86:87], v129 offset:36864\n"
"v_mfma_f32_16x16x32_bf16 v[28:31], v[88:91], v[112:115], v[28:31]\n"
"s_waitcnt lgkmcnt(6)\n"
"v_mfma_f32_16x16x32_bf16 v[32:35], v[92:95], v[108:111], v[32:35]\n"
"ds_read_b64_tr_b16 v[88:89], v130 offset:32768\n"
"ds_read_b64_tr_b16 v[90:91], v130 offset:36864\n"
"v_mfma_f32_16x16x32_bf16 v[36:39], v[92:95], v[112:115], v[36:39]\n"
"s_waitcnt lgkmcnt(6)\n"
"v_mfma_f32_16x16x32_bf16 v[40:43], v[96:99], v[108:111], v[40:43]\n"
"ds_read_b64_tr_b16 v[92:93], v131 offset:32768\n"
"ds_read_b64_tr_b16 v[94:95], v131 offset:36864\n"
"v_mfma_f32_16x16x32_bf16 v[44:47], v[96:99], v[112:115], v[44:47]\n"
"v_cvt_pk_bf16_f32 v176, v16, v17\n"
"v_cvt_pk_bf16_f32 v177, v18, v19\n"
"v_cvt_pk_bf16_f32 v178, v24, v25\n"
"v_cvt_pk_bf16_f32 v179, v26, v27\n"
"v_cvt_pk_bf16_f32 v192, v20, v21\n"
"v_cvt_pk_bf16_f32 v193, v22, v23\n"
"v_cvt_pk_bf16_f32 v194, v28, v29\n"
"v_cvt_pk_bf16_f32 v195, v30, v31\n"
"s_waitcnt lgkmcnt(6)\n"
"v_mfma_f32_16x16x32_bf16 v[48:51], v[80:83], v[108:111], v[48:51]\n"
"v_mfma_f32_16x16x32_bf16 v[52:55], v[80:83], v[112:115], v[52:55]\n"
"s_waitcnt vmcnt(4) lgkmcnt(0)\n"
"s_barrier\n"
"v_permlane32_swap_b32 v176, v178\n"
"v_permlane32_swap_b32 v177, v179\n"
"v_permlane32_swap_b32 v192, v194\n"
"v_permlane32_swap_b32 v193, v195\n"
"v_mfma_f32_16x16x32_bf16 v[56:59], v[84:87], v[108:111], v[56:59]\n"
"v_mfma_f32_16x16x32_bf16 v[60:63], v[84:87], v[112:115], v[60:63]\n"
"v_permlane16_swap_b32 v176, v178\n"
"v_permlane16_swap_b32 v177, v179\n"
"v_permlane16_swap_b32 v192, v194\n"
"v_permlane16_swap_b32 v193, v195\n"
"v_cvt_pk_bf16_f32 v180, v32, v33\n"
"v_cvt_pk_bf16_f32 v181, v34, v35\n"
"v_cvt_pk_bf16_f32 v182, v40, v41\n"
"v_cvt_pk_bf16_f32 v183, v42, v43\n"
"v_cvt_pk_bf16_f32 v196, v36, v37\n"
"v_cvt_pk_bf16_f32 v197, v38, v39\n"
"v_cvt_pk_bf16_f32 v198, v44, v45\n"
"v_cvt_pk_bf16_f32 v199, v46, v47\n"
"v_mfma_f32_16x16x32_bf16 v[64:67], v[88:91], v[108:111], v[64:67]\n"
"v_mfma_f32_16x16x32_bf16 v[68:71], v[88:91], v[112:115], v[68:71]\n"
"v_permlane32_swap_b32 v180, v182\n"
"v_permlane32_swap_b32 v181, v183\n"
"v_permlane32_swap_b32 v196, v198\n"
"v_permlane32_swap_b32 v197, v199\n"
"v_mfma_f32_16x16x32_bf16 v[72:75], v[92:95], v[108:111], v[72:75]\n"
"v_mfma_f32_16x16x32_bf16 v[76:79], v[92:95], v[112:115], v[76:79]\n"
"v_permlane16_swap_b32 v180, v182\n"
"v_permlane16_swap_b32 v181, v183\n"
"v_permlane16_swap_b32 v196, v198\n"
"v_permlane16_swap_b32 v197, v199\n"
"v_cvt_pk_bf16_f32 v184, v48, v49\n"
"v_cvt_pk_bf16_f32 v185, v50, v51\n"
"v_cvt_pk_bf16_f32 v186, v56, v57\n"
"v_cvt_pk_bf16_f32 v187, v58, v59\n"
"v_cvt_pk_bf16_f32 v200, v52, v53\n"
"v_cvt_pk_bf16_f32 v201, v54, v55\n"
"v_cvt_pk_bf16_f32 v202, v60, v61\n"
"v_cvt_pk_bf16_f32 v203, v62, v63\n"
"v_permlane32_swap_b32 v184, v186\n"
"v_permlane32_swap_b32 v185, v187\n"
"v_permlane32_swap_b32 v200, v202\n"
"v_permlane32_swap_b32 v201, v203\n"
"v_permlane16_swap_b32 v184, v186\n"
"v_permlane16_swap_b32 v185, v187\n"
"v_permlane16_swap_b32 v200, v202\n"
"v_permlane16_swap_b32 v201, v203\n"
"s_mov_b32 s27, 0\n"
"ds_read_b128 v[80:83], v228 offset:0\n"
"ds_read_b128 v[84:87], v229 offset:0\n"
"ds_read_b128 v[88:91], v230 offset:0\n"
"v_cvt_pk_bf16_f32 v188, v64, v65\n"
"v_cvt_pk_bf16_f32 v189, v66, v67\n"
"v_cvt_pk_bf16_f32 v190, v72, v73\n"
"v_cvt_pk_bf16_f32 v191, v74, v75\n"
"v_cvt_pk_bf16_f32 v204, v68, v69\n"
"v_cvt_pk_bf16_f32 v205, v70, v71\n"
"v_cvt_pk_bf16_f32 v206, v76, v77\n"
"v_cvt_pk_bf16_f32 v207, v78, v79\n"
"v_permlane32_swap_b32 v188, v190\n"
"v_permlane32_swap_b32 v189, v191\n"
"v_permlane32_swap_b32 v204, v206\n"
"v_permlane32_swap_b32 v205, v207\n"
"v_permlane16_swap_b32 v188, v190\n"
"v_permlane16_swap_b32 v189, v191\n"
"v_permlane16_swap_b32 v204, v206\n"
"v_permlane16_swap_b32 v205, v207\n"
"v_mov_b32 v172, 0xff800000\n"
"v_mov_b32 v173, 0xff800000\n"
"F1_ATT:\n"
"v_mov_b32 v168, 0\n"
"v_mov_b32 v169, 0\n"
"v_mov_b32 v170, 0\n"
"v_mov_b32 v171, 0\n"
"s_waitcnt lgkmcnt(2)\n"
"v_mfma_f32_16x16x32_bf16 v[112:115], v[80:83], v[176:179], 0\n"
"ds_read_b128 v[92:95], v231 offset:0\n"
"s_add_u32 m0, s29, 0x18000\n"
"v_mfma_f32_16x16x32_bf16 v[116:119], v[80:83], v[192:195], 0\n"
"global_load_lds_dwordx4 v240, s[30:31]\n"
"s_waitcnt lgkmcnt(2)\n"
"v_mfma_f32_16x16x32_bf16 v[112:115], v[84:87], v[180:183], v[112:115]\n"
"ds_read_b128 v[80:83], v228 offset:4096\n"
"global_load_lds_dwordx4 v241, s[30:31] offset:1024\n"
"s_add_u32 s30, s30, 0x2000\n"
"s_addc_u32 s31, s31, 0\n"
"v_mfma_f32_16x16x32_bf16 v[116:119], v[84:87], v[196:199], v[116:119]\n"
"s_add_u32 m0, s29, 0x1c000\n"
"s_waitcnt lgkmcnt(2)\n"
"v_mfma_f32_16x16x32_bf16 v[112:115], v[88:91], v[184:187], v[112:115]\n"
"ds_read_b128 v[84:87], v229 offset:4096\n"
"global_load_lds_dwordx4 v242, s[34:35]\n"
"v_mfma_f32_16x16x32_bf16 v[116:119], v[88:91], v[200:203], v[116:119]\n"
"s_waitcnt lgkmcnt(2)\n"
"v_mfma_f32_16x16x32_bf16 v[112:115], v[92:95], v[188:191], v[112:115]\n"
"ds_read_b128 v[88:91], v230 offset:4096\n"
"global_load_lds_dwordx4 v243, s[34:35] offset:1024\n"
"s_add_u32 s34, s34, 0x2000\n"
"s_addc_u32 s35, s35, 0\n"
"v_mfma_f32_16x16x32_bf16 v[116:119], v[92:95], v[204:207], v[116:119]\n"
"s_add_u32 m0, s29, 0x20000\n"
"s_waitcnt lgkmcnt(2)\n"
"v_mfma_f32_16x16x32_bf16 v[120:123], v[80:83], v[176:179], 0\n"
"ds_read_b128 v[92:95], v231 offset:4096\n"
"global_load_lds_dwordx4 v240, s[30:31]\n"
"v_mfma_f32_16x16x32_bf16 v[124:127], v[80:83], v[192:195], 0\n"
"global_load_lds_dwordx4 v241, s[30:31] offset:1024\n"
"s_add_u32 s30, s30, 0x2000\n"
"s_addc_u32 s31, s31, 0\n"
"s_waitcnt lgkmcnt(2)\n"
"v_mfma_f32_16x16x32_bf16 v[120:123], v[84:87], v[180:183], v[120:123]\n"
"s_add_u32 m0, s29, 0x24000\n"
"v_mfma_f32_16x16x32_bf16 v[124:127], v[84:87], v[196:199], v[124:127]\n"
"s_waitcnt vmcnt(8) lgkmcnt(0)\n"
"s_barrier\n"
"global_load_lds_dwordx4 v242, s[34:35]\n"
"v_mfma_f32_16x16x32_bf16 v[120:123], v[88:91], v[184:187], v[120:123]\n"
"ds_read_b128 v[80:83], v228 offset:32768\n"
"ds_read_b128 v[84:87], v229 offset:32768\n"
"v_mfma_f32_16x16x32_bf16 v[124:127], v[88:91], v[200:203], v[124:127]\n"
"ds_read_b128 v[88:91], v230 offset:32768\n"
"global_load_lds_dwordx4 v243, s[34:35] offset:1024\n"
"s_add_u32 s34, s34, 0x2000\n"
"s_addc_u32 s35, s35, 0\n"
"v_mfma_f32_16x16x32_bf16 v[120:123], v[92:95], v[188:191], v[120:123]\n"
"v_mfma_f32_16x16x32_bf16 v[124:127], v[92:95], v[204:207], v[124:127]\n"
"v_max3_f32 v208, v112, v113, v114\n"
"s_nop 5\n"
"v_max3_f32 v209, v115, v120, v121\n"
"v_max3_f32 v210, v116, v117, v118\n"
"v_max3_f32 v211, v119, v124, v125\n"
"v_max3_f32 v208, v208, v122, v123\n"
"v_max3_f32 v210, v210, v126, v127\n"
"v_max3_f32 v208, v208, v209, v172\n"
"v_max3_f32 v210, v210, v211, v173\n"
"v_mov_b32 v209, v208\n"
"v_mov_b32 v211, v210\n"
"s_nop 0\n"
"v_permlane32_swap_b32 v208, v209\n"
"v_permlane32_swap_b32 v210, v211\n"
"v_max_f32 v208, v208, v209\n"
"v_max_f32 v210, v210, v211\n"
"v_mov_b32 v209, v208\n"
"v_mov_b32 v211, v210\n"
"s_nop 0\n"
"v_permlane16_swap_b32 v208, v209\n"
"v_permlane16_swap_b32 v210, v211\n"
"v_max_f32 v208, v208, v209\n"
"v_max_f32 v210, v210, v211\n"
"v_sub_f32 v160, 0, v208\n"
"v_sub_f32 v161, 0, v208\n"
"v_sub_f32 v162, 0, v208\n"
"v_sub_f32 v163, 0, v208\n"
"v_sub_f32 v164, 0, v210\n"
"v_sub_f32 v165, 0, v210\n"
"v_sub_f32 v166, 0, v210\n"
"v_sub_f32 v167, 0, v210\n"
"v_sub_f32 v112, v112, v208\n"
"v_sub_f32 v113, v113, v208\n"
"v_sub_f32 v114, v114, v208\n"
"v_sub_f32 v115, v115, v208\n"
"v_sub_f32 v116, v116, v210\n"
"v_sub_f32 v117, v117, v210\n"
"v_sub_f32 v118, v118, v210\n"
"v_sub_f32 v119, v119, v210\n"
"v_sub_f32 v120, v120, v208\n"
"v_sub_f32 v121, v121, v208\n"
"v_sub_f32 v122, v122, v208\n"
"v_sub_f32 v123, v123, v208\n"
"v_sub_f32 v124, v124, v210\n"
"v_sub_f32 v125, v125, v210\n"
"v_sub_f32 v126, v126, v210\n"
"v_sub_f32 v127, v127, v210\n"
"s_waitcnt lgkmcnt(2)\n"
"v_mfma_f32_16x16x32_bf16 v[128:131], v[80:83], v[176:179], v[160:163]\n"
"ds_read_b128 v[92:95], v231 offset:32768\n"
"s_mov_b32 m0, s29\n"
"v_exp_f32 v112, v112\n"
"v_exp_f32 v113, v113\n"
"v_mfma_f32_16x16x32_bf16 v[132:135], v[80:83], v[192:195], v[164:167]\n"
"global_load_lds_dwordx4 v240, s[30:31]\n"
"v_add_f32 v168, v168, v112\n"
"v_add_f32 v169, v169, v113\n"
"v_cvt_pk_bf16_f32 v144, v112, v113\n"
"v_exp_f32 v114, v114\n"
"s_waitcnt lgkmcnt(2)\n"
"v_mfma_f32_16x16x32_bf16 v[128:131], v[84:87], v[180:183], v[128:131]\n"
"ds_read_b128 v[80:83], v228 offset:36864\n"
"s_cmp_lt_u32 s86, 36\n"
"s_cselect_b32 s36, 1, 0\n"
"s_min_u32 s37, s86, 35\n"
"s_lshl_b32 s37, s37, 2\n"
"s_and_b32 s38, s18, 3\n"
"s_add_u32 s37, s37, s38\n"
"s_mul_i32 s24, s16, 0x36000\n"
"s_lshl_b32 s38, s37, 5\n"
"s_add_u32 s24, s24, s38\n"
"s_add_u32 s24, s24, 0x900\n"
"v_add_f32 v168, v168, v114\n"
"v_exp_f32 v115, v115\n"
"v_mfma_f32_16x16x32_bf16 v[132:135], v[84:87], v[196:199], v[132:135]\n"
"v_add_f32 v169, v169, v115\n"
"v_cvt_pk_bf16_f32 v145, v114, v115\n"
"v_exp_f32 v116, v116\n"
"s_waitcnt lgkmcnt(2)\n"
"v_mfma_f32_16x16x32_bf16 v[128:131], v[88:91], v[184:187], v[128:131]\n"
"ds_read_b128 v[84:87], v229 offset:36864\n"
"global_load_lds_dwordx4 v241, s[30:31] offset:1024\n"
"s_add_u32 s30, s30, 0x2000\n"
"s_addc_u32 s31, s31, 0\n"
"v_add_f32 v170, v170, v116\n"
"v_exp_f32 v117, v117\n"
"v_mfma_f32_16x16x32_bf16 v[132:135], v[88:91], v[200:203], v[132:135]\n"
"v_add_f32 v171, v171, v117\n"
"v_cvt_pk_bf16_f32 v148, v116, v117\n"
"v_exp_f32 v118, v118\n"
"s_waitcnt lgkmcnt(2)\n"
"v_mfma_f32_16x16x32_bf16 v[128:131], v[92:95], v[188:191], v[128:131]\n"
"ds_read_b128 v[88:91], v230 offset:36864\n"
"s_add_u32 s24, s90, s24\n"
"s_addc_u32 s25, s91, 0\n"
"s_lshl_b32 s38, s16, 10\n"
"v_lshrrev_b32 v214, 1, v1\n"
"v_mul_u32_u24 v214, 0x1b00, v214\n"
"v_and_b32 v215, 1, v1\n"
"v_lshl_add_u32 v214, v215, 4, v214\n"
"v_min_u32 v174, 0x11f, v0\n"
"v_lshlrev_b32 v174, 2, v174\n"
"global_load_dword v215, v174, s[88:89] offset:0\n"
"v_add_f32 v170, v170, v118\n"
"v_exp_f32 v119, v119\n"
"v_mfma_f32_16x16x32_bf16 v[132:135], v[92:95], v[204:207], v[132:135]\n"
"s_add_u32 m0, s29, 0x4000\n"
"v_add_f32 v171, v171, v119\n"
"v_cvt_pk_bf16_f32 v149, v118, v119\n"
"v_exp_f32 v120, v120\n"
"v_exp_f32 v121, v121\n"
"s_waitcnt lgkmcnt(2)\n"
"v_mfma_f32_16x16x32_bf16 v[136:139], v[80:83], v[176:179], v[160:163]\n"
"ds_read_b128 v[92:95], v231 offset:36864\n"
"global_load_lds_dwordx4 v242, s[34:35]\n"
"v_add_f32 v168, v168, v120\n"
"v_add_f32 v169, v169, v121\n"
"v_cvt_pk_bf16_f32 v146, v120, v121\n"
"v_exp_f32 v122, v122\n"
"v_mfma_f32_16x16x32_bf16 v[140:143], v[80:83], v[192:195], v[164:167]\n"
"v_add_f32 v168, v168, v122\n"
"v_exp_f32 v123, v123\n"
"s_waitcnt lgkmcnt(2)\n"
"v_mfma_f32_16x16x32_bf16 v[136:139], v[84:87], v[180:183], v[136:139]\n"
"v_add_f32 v169, v169, v123\n"
"v_cvt_pk_bf16_f32 v147, v122, v123\n"
"v_exp_f32 v124, v124\n"
"v_mfma_f32_16x16x32_bf16 v[140:143], v[84:87], v[196:199], v[140:143]\n"
"s_waitcnt vmcnt(8) lgkmcnt(0)\n"
"s_barrier\n"
"global_load_lds_dwordx4 v243, s[34:35] offset:1024\n"
"s_add_u32 s34, s34, 0x2000\n"
"s_addc_u32 s35, s35, 0\n"
"s_cmp_eq_u32 s36, 0\ns_cbranch_scc1 GV_NO\n"
"s_add_u32 m0, s38, 0x10000\n"
"s_nop 0\n"
"global_load_lds_dwordx4 v214, s[24:25]\n"
"s_add_u32 s24, s24, 0x1b0000\n"
"s_addc_u32 s25, s25, 0\n"
"s_add_u32 m0, s38, 0x12000\n"
"s_nop 0\n"
"global_load_lds_dwordx4 v214, s[24:25]\n"
"s_cmp_ge_u32 s16, 2\ns_cbranch_scc1 GV_NO\n"
"s_add_u32 s24, s24, 0x1b0000\n"
"s_addc_u32 s25, s25, 0\n"
"s_add_u32 m0, s38, 0x14000\n"
"s_nop 0\n"
"global_load_lds_dwordx4 v214, s[24:25]\n"
"GV_NO:\n"
"v_add_f32 v170, v170, v124\n"
"v_exp_f32 v125, v125\n"
"v_mfma_f32_16x16x32_bf16 v[136:139], v[88:91], v[184:187], v[136:139]\n"
"ds_read_b64_tr_b16 v[96:97], v220 offset:0\n"
"ds_read_b64_tr_b16 v[100:101], v221 offset:0\n"
"v_add_f32 v171, v171, v125\n"
"v_cvt_pk_bf16_f32 v150, v124, v125\n"
"v_exp_f32 v126, v126\n"
"v_mfma_f32_16x16x32_bf16 v[140:143], v[88:91], v[200:203], v[140:143]\n"
"ds_read_b64_tr_b16 v[98:99], v220 offset:4096\n"
"ds_read_b64_tr_b16 v[102:103], v221 offset:4096\n"
"ds_read_b64_tr_b16 v[104:105], v222 offset:0\n"
"v_add_f32 v170, v170, v126\n"
"v_exp_f32 v127, v127\n"
"v_mfma_f32_16x16x32_bf16 v[136:139], v[92:95], v[188:191], v[136:139]\n"
"ds_read_b128 v[80:83], v216 offset:0\n"
"ds_read_b128 v[84:87], v217 offset:0\n"
"ds_read_b64_tr_b16 v[106:107], v222 offset:4096\n"
"v_add_f32 v171, v171, v127\n"
"v_cvt_pk_bf16_f32 v151, v126, v127\n"
"v_mfma_f32_16x16x32_bf16 v[140:143], v[92:95], v[204:207], v[140:143]\n"
"ds_read_b128 v[88:91], v218 offset:0\n"
"s_waitcnt lgkmcnt(3)\n"
"v_mfma_f32_16x16x32_bf16 v[112:115], v[80:83], v[176:179], v[160:163]\n"
"ds_read_b64_tr_b16 v[108:109], v223 offset:0\n"
"s_add_u32 m0, s29, 0x8000\n"
"v_exp_f32 v128, v128\n"
"v_mfma_f32_16x16x32_bf16 v[116:119], v[80:83], v[192:195], v[164:167]\n"
"ds_read_b64_tr_b16 v[110:111], v223 offset:4096\n"
"global_load_lds_dwordx4 v240, s[30:31]\n"
"v_add_f32 v168, v168, v128\n"
"v_exp_f32 v129, v129\n"
"v_mfma_f32_16x16x32_bf16 v[16:19], v[96:99], v[144:147], 0\n"
"ds_read_b128 v[92:95], v219 offset:0\n"
"v_add_f32 v169, v169, v129\n"
"v_cvt_pk_bf16_f32 v152, v128, v129\n"
"v_mfma_f32_16x16x32_bf16 v[20:23], v[96:99], v[148:151], 0\n"
"v_exp_f32 v130, v130\n"
"s_waitcnt lgkmcnt(5)\n"
"v_mfma_f32_16x16x32_bf16 v[112:115], v[84:87], v[180:183], v[112:115]\n"
"ds_read_b64_tr_b16 v[96:97], v224 offset:0\n"
"v_add_f32 v168, v168, v130\n"
"v_mfma_f32_16x16x32_bf16 v[116:119], v[84:87], v[196:199], v[116:119]\n"
"ds_read_b64_tr_b16 v[98:99], v224 offset:4096\n"
"v_exp_f32 v131, v131\n"
"v_mfma_f32_16x16x32_bf16 v[24:27], v[100:103], v[144:147], 0\n"
"ds_read_b128 v[80:83], v216 offset:4096\n"
"v_add_f32 v169, v169, v131\n"
"v_cvt_pk_bf16_f32 v153, v130, v131\n"
"v_mfma_f32_16x16x32_bf16 v[28:31], v[100:103], v[148:151], 0\n"
"v_exp_f32 v132, v132\n"
"s_waitcnt lgkmcnt(6)\n"
"v_mfma_f32_16x16x32_bf16 v[112:115], v[88:91], v[184:187], v[112:115]\n"
"ds_read_b64_tr_b16 v[100:101], v225 offset:0\n"
"global_load_lds_dwordx4 v241, s[30:31] offset:1024\n"
"s_add_u32 s30, s30, 0x2000\n"
"s_addc_u32 s31, s31, 0\n"
"v_add_f32 v170, v170, v132\n"
"v_mfma_f32_16x16x32_bf16 v[116:119], v[88:91], v[200:203], v[116:119]\n"
"ds_read_b64_tr_b16 v[102:103], v225 offset:4096\n"
"v_exp_f32 v133, v133\n"
"v_mfma_f32_16x16x32_bf16 v[32:35], v[104:107], v[144:147], 0\n"
"ds_read_b128 v[84:87], v217 offset:4096\n"
"v_add_f32 v171, v171, v133\n"
"v_cvt_pk_bf16_f32 v156, v132, v133\n"
"v_mfma_f32_16x16x32_bf16 v[36:39], v[104:107], v[148:151], 0\n"
"v_exp_f32 v134, v134\n"
"s_waitcnt lgkmcnt(6)\n"
"v_mfma_f32_16x16x32_bf16 v[112:115], v[92:95], v[188:191], v[112:115]\n"
"ds_read_b64_tr_b16 v[104:105], v226 offset:0\n"
"v_add_f32 v170, v170, v134\n"
"v_mfma_f32_16x16x32_bf16 v[116:119], v[92:95], v[204:207], v[116:119]\n"
"ds_read_b64_tr_b16 v[106:107], v226 offset:4096\n"
"v_exp_f32 v135, v135\n"
"v_mfma_f32_16x16x32_bf16 v[40:43], v[108:111], v[144:147], 0\n"
"ds_read_b128 v[88:91], v218 offset:4096\n"
"v_add_f32 v171, v171, v135\n"
"v_cvt_pk_bf16_f32 v157, v134, v135\n"
"v_mfma_f32_16x16x32_bf16 v[44:47], v[108:111], v[148:151], 0\n"
"s_add_u32 m0, s29, 0xc000\n"
"v_exp_f32 v136, v136\n"
"s_waitcnt lgkmcnt(6)\n"
"v_mfma_f32_16x16x32_bf16 v[120:123], v[80:83], v[176:179], v[160:163]\n"
"ds_read_b64_tr_b16 v[108:109], v227 offset:0\n"
"global_load_lds_dwordx4 v242, s[34:35]\n"
"v_add_f32 v168, v168, v136\n"
"v_exp_f32 v137, v137\n"
"v_mfma_f32_16x16x32_bf16 v[124:127], v[80:83], v[192:195], v[164:167]\n"
"ds_read_b64_tr_b16 v[110:111], v227 offset:4096\n"
"v_add_f32 v169, v169, v137\n"
"v_cvt_pk_bf16_f32 v154, v136, v137\n"
"v_mfma_f32_16x16x32_bf16 v[48:51], v[96:99], v[144:147], 0\n"
"ds_read_b128 v[92:95], v219 offset:4096\n"
"v_exp_f32 v138, v138\n"
"v_mfma_f32_16x16x32_bf16 v[52:55], v[96:99], v[148:151], 0\n"
"v_add_f32 v168, v168, v138\n"
"s_waitcnt lgkmcnt(6)\n"
"v_mfma_f32_16x16x32_bf16 v[120:123], v[84:87], v[180:183], v[120:123]\n"
"v_exp_f32 v139, v139\n"
"v_mfma_f32_16x16x32_bf16 v[124:127], v[84:87], v[196:199], v[124:127]\n"
"v_add_f32 v169, v169, v139\n"
"v_cvt_pk_bf16_f32 v155, v138, v139\n"
"v_mfma_f32_16x16x32_bf16 v[56:59], v[100:103], v[144:147], 0\n"
"v_exp_f32 v140, v140\n"
"v_mfma_f32_16x16x32_bf16 v[60:63], v[100:103], v[148:151], 0\n"
"s_waitcnt vmcnt(8) lgkmcnt(0)\n"
"s_barrier\n"
"global_load_lds_dwordx4 v243, s[34:35] offset:1024\n"
"s_add_u32 s34, s34, 0x2000\n"
"s_addc_u32 s35, s35, 0\n"
"v_add_f32 v170, v170, v140\n"
"v_mfma_f32_16x16x32_bf16 v[120:123], v[88:91], v[184:187], v[120:123]\n"
"ds_read_b64_tr_b16 v[96:97], v220 offset:32768\n"
"v_exp_f32 v141, v141\n"
"v_mfma_f32_16x16x32_bf16 v[124:127], v[88:91], v[200:203], v[124:127]\n"
"ds_read_b64_tr_b16 v[98:99], v220 offset:36864\n"
"v_add_f32 v171, v171, v141\n"
"v_cvt_pk_bf16_f32 v158, v140, v141\n"
"v_mfma_f32_16x16x32_bf16 v[64:67], v[104:107], v[144:147], 0\n"
"ds_read_b128 v[80:83], v216 offset:32768\n"
"v_exp_f32 v142, v142\n"
"v_mfma_f32_16x16x32_bf16 v[68:71], v[104:107], v[148:151], 0\n"
"ds_read_b64_tr_b16 v[100:101], v221 offset:32768\n"
"v_add_f32 v170, v170, v142\n"
"v_mfma_f32_16x16x32_bf16 v[120:123], v[92:95], v[188:191], v[120:123]\n"
"ds_read_b64_tr_b16 v[102:103], v221 offset:36864\n"
"v_exp_f32 v143, v143\n"
"v_mfma_f32_16x16x32_bf16 v[124:127], v[92:95], v[204:207], v[124:127]\n"
"ds_read_b128 v[84:87], v217 offset:32768\n"
"v_add_f32 v171, v171, v143\n"
"v_cvt_pk_bf16_f32 v159, v142, v143\n"
"v_mfma_f32_16x16x32_bf16 v[72:75], v[108:111], v[144:147], 0\n"
"ds_read_b64_tr_b16 v[104:105], v222 offset:32768\n"
"v_mfma_f32_16x16x32_bf16 v[76:79], v[108:111], v[148:151], 0\n"
"ds_read_b64_tr_b16 v[106:107], v222 offset:36864\n"
"ds_read_b128 v[88:91], v218 offset:32768\n"
"s_waitcnt lgkmcnt(6)\n"
"v_mfma_f32_16x16x32_bf16 v[128:131], v[80:83], v[176:179], v[160:163]\n"
"ds_read_b64_tr_b16 v[108:109], v223 offset:32768\n"
"s_add_u32 m0, s29, 0x18000\n"
"v_exp_f32 v112, v112\n"
"v_mfma_f32_16x16x32_bf16 v[132:135], v[80:83], v[192:195], v[164:167]\n"
"ds_read_b64_tr_b16 v[110:111], v223 offset:36864\n"
"global_load_lds_dwordx4 v240, s[30:31]\n"
"v_add_f32 v168, v168, v112\n"
"v_exp_f32 v113, v113\n"
"v_mfma_f32_16x16x32_bf16 v[16:19], v[96:99], v[152:155], v[16:19]\n"
"ds_read_b128 v[92:95], v219 offset:32768\n"
"v_add_f32 v169, v169, v113\n"
"v_cvt_pk_bf16_f32 v144, v112, v113\n"
"v_mfma_f32_16x16x32_bf16 v[20:23], v[96:99], v[156:159], v[20:23]\n"
"s_waitcnt vmcnt(7)\n"
"v_lshlrev_b32 v244, 16, v215\n"
"v_and_b32 v245, 0xffff0000, v215\n"
"v_mul_f32 v246, 0xbfb8aa3b, v244\n"
"v_mul_f32 v247, 0xbfb8aa3b, v245\n"
"v_exp_f32 v246, v246\n"
"v_exp_f32 v247, v247\n"
"v_add_f32 v246, 1.0, v246\n"
"v_add_f32 v247, 1.0, v247\n"
"v_exp_f32 v114, v114\n"
"s_waitcnt lgkmcnt(6)\n"
"v_mfma_f32_16x16x32_bf16 v[128:131], v[84:87], v[180:183], v[128:131]\n"
"ds_read_b64_tr_b16 v[96:97], v224 offset:32768\n"
"v_add_f32 v168, v168, v114\n"
"v_mfma_f32_16x16x32_bf16 v[132:135], v[84:87], v[196:199], v[132:135]\n"
"ds_read_b64_tr_b16 v[98:99], v224 offset:36864\n"
"v_exp_f32 v115, v115\n"
"v_mfma_f32_16x16x32_bf16 v[24:27], v[100:103], v[152:155], v[24:27]\n"
"ds_read_b128 v[80:83], v216 offset:36864\n"
"v_add_f32 v169, v169, v115\n"
"v_cvt_pk_bf16_f32 v145, v114, v115\n"
"v_mfma_f32_16x16x32_bf16 v[28:31], v[100:103], v[156:159], v[28:31]\n"
"v_exp_f32 v116, v116\n"
"s_waitcnt lgkmcnt(6)\n"
"v_mfma_f32_16x16x32_bf16 v[128:131], v[88:91], v[184:187], v[128:131]\n"
"ds_read_b64_tr_b16 v[100:101], v225 offset:32768\n"
"global_load_lds_dwordx4 v241, s[30:31] offset:1024\n"
"s_add_u32 s30, s30, 0x2000\n"
"s_addc_u32 s31, s31, 0\n"
"v_add_f32 v170, v170, v116\n"
"v_mfma_f32_16x16x32_bf16 v[132:135], v[88:91], v[200:203], v[132:135]\n"
"ds_read_b64_tr_b16 v[102:103], v225 offset:36864\n"
"v_rcp_f32 v246, v246\n"
"v_rcp_f32 v247, v247\n"
"v_mul_f32 v244, v244, v246\n"
"v_mul_f32 v245, v245, v247\n"
"v_cvt_pk_bf16_f32 v215, v244, v245\n"
"v_add_u32 v174, 0x14800, v174\n"
"ds_write_b32 v174, v215 offset:0\n"
"v_exp_f32 v117, v117\n"
"v_mfma_f32_16x16x32_bf16 v[32:35], v[104:107], v[152:155], v[32:35]\n"
"ds_read_b128 v[84:87], v217 offset:36864\n"
"v_add_f32 v171, v171, v117\n"
"v_cvt_pk_bf16_f32 v148, v116, v117\n"
"v_mfma_f32_16x16x32_bf16 v[36:39], v[104:107], v[156:159], v[36:39]\n"
"v_exp_f32 v118, v118\n"
"s_waitcnt lgkmcnt(7)\n"
"v_mfma_f32_16x16x32_bf16 v[128:131], v[92:95], v[188:191], v[128:131]\n"
"ds_read_b64_tr_b16 v[104:105], v226 offset:32768\n"
"v_add_f32 v170, v170, v118\n"
"v_mfma_f32_16x16x32_bf16 v[132:135], v[92:95], v[204:207], v[132:135]\n"
"ds_read_b64_tr_b16 v[106:107], v226 offset:36864\n"
"v_exp_f32 v119, v119\n"
"v_mfma_f32_16x16x32_bf16 v[40:43], v[108:111], v[152:155], v[40:43]\n"
"ds_read_b128 v[88:91], v218 offset:36864\n"
"v_add_f32 v171, v171, v119\n"
"v_cvt_pk_bf16_f32 v149, v118, v119\n"
"v_mfma_f32_16x16x32_bf16 v[44:47], v[108:111], v[156:159], v[44:47]\n"
"s_add_u32 m0, s29, 0x1c000\n"
"v_exp_f32 v120, v120\n"
"s_waitcnt lgkmcnt(7)\n"
"v_mfma_f32_16x16x32_bf16 v[136:139], v[80:83], v[176:179], v[160:163]\n"
"ds_read_b64_tr_b16 v[108:109], v227 offset:32768\n"
"global_load_lds_dwordx4 v242, s[34:35]\n"
"v_add_f32 v168, v168, v120\n"
"v_exp_f32 v121, v121\n"
"v_mfma_f32_16x16x32_bf16 v[140:143], v[80:83], v[192:195], v[164:167]\n"
"ds_read_b64_tr_b16 v[110:111], v227 offset:36864\n"
"v_add_f32 v169, v169, v121\n"
"v_cvt_pk_bf16_f32 v146, v120, v121\n"
"v_mfma_f32_16x16x32_bf16 v[48:51], v[96:99], v[152:155], v[48:51]\n"
"ds_read_b128 v[92:95], v219 offset:36864\n"
"v_exp_f32 v122, v122\n"
"v_mfma_f32_16x16x32_bf16 v[52:55], v[96:99], v[156:159], v[52:55]\n"
"v_add_f32 v168, v168, v122\n"
"s_waitcnt lgkmcnt(6)\n"
"v_mfma_f32_16x16x32_bf16 v[136:139], v[84:87], v[180:183], v[136:139]\n"
"v_exp_f32 v123, v123\n"
"v_mfma_f32_16x16x32_bf16 v[140:143], v[84:87], v[196:199], v[140:143]\n"
"v_add_f32 v169, v169, v123\n"
"v_cvt_pk_bf16_f32 v147, v122, v123\n"
"v_mfma_f32_16x16x32_bf16 v[56:59], v[100:103], v[152:155], v[56:59]\n"
"v_exp_f32 v124, v124\n"
"v_mfma_f32_16x16x32_bf16 v[60:63], v[100:103], v[156:159], v[60:63]\n"
"s_waitcnt vmcnt(7) lgkmcnt(0)\n"
"s_barrier\n"
"global_load_lds_dwordx4 v243, s[34:35] offset:1024\n"
"s_add_u32 s34, s34, 0x2000\n"
"s_addc_u32 s35, s35, 0\n"
"v_add_f32 v170, v170, v124\n"
"v_mfma_f32_16x16x32_bf16 v[136:139], v[88:91], v[184:187], v[136:139]\n"
"ds_read_b64_tr_b16 v[96:97], v232 offset:0\n"
"v_exp_f32 v125, v125\n"
"v_mfma_f32_16x16x32_bf16 v[140:143], v[88:91], v[200:203], v[140:143]\n"
"ds_read_b64_tr_b16 v[98:99], v232 offset:4096\n"
"v_add_f32 v171, v171, v125\n"
"v_cvt_pk_bf16_f32 v150, v124, v125\n"
"v_mfma_f32_16x16x32_bf16 v[64:67], v[104:107], v[152:155], v[64:67]\n"
"ds_read_b128 v[80:83], v228 offset:0\n"
"v_exp_f32 v126, v126\n"
"v_mfma_f32_16x16x32_bf16 v[68:71], v[104:107], v[156:159], v[68:71]\n"
"ds_read_b64_tr_b16 v[100:101], v233 offset:0\n"
"v_add_f32 v170, v170, v126\n"
"v_mfma_f32_16x16x32_bf16 v[136:139], v[92:95], v[188:191], v[136:139]\n"
"ds_read_b64_tr_b16 v[102:103], v233 offset:4096\n"
"v_exp_f32 v127, v127\n"
"v_mfma_f32_16x16x32_bf16 v[140:143], v[92:95], v[204:207], v[140:143]\n"
"ds_read_b128 v[84:87], v229 offset:0\n"
"v_add_f32 v171, v171, v127\n"
"v_cvt_pk_bf16_f32 v151, v126, v127\n"
"v_mfma_f32_16x16x32_bf16 v[72:75], v[108:111], v[152:155], v[72:75]\n"
"ds_read_b64_tr_b16 v[104:105], v234 offset:0\n"
"v_mfma_f32_16x16x32_bf16 v[76:79], v[108:111], v[156:159], v[76:79]\n"
"ds_read_b64_tr_b16 v[106:107], v234 offset:4096\n"
"ds_read_b128 v[88:91], v230 offset:0\n"
"s_waitcnt lgkmcnt(6)\n"
"v_mfma_f32_16x16x32_bf16 v[112:115], v[80:83], v[176:179], v[160:163]\n"
"ds_read_b64_tr_b16 v[108:109], v235 offset:0\n"
"s_add_u32 m0, s29, 0x20000\n"
"v_exp_f32 v128, v128\n"
"v_mfma_f32_16x16x32_bf16 v[116:119], v[80:83], v[192:195], v[164:167]\n"
"ds_read_b64_tr_b16 v[110:111], v235 offset:4096\n"
"global_load_lds_dwordx4 v240, s[30:31]\n"
"v_add_f32 v168, v168, v128\n"
"v_exp_f32 v129, v129\n"
"v_mfma_f32_16x16x32_bf16 v[16:19], v[96:99], v[144:147], v[16:19]\n"
"ds_read_b128 v[92:95], v231 offset:0\n"
"v_add_f32 v169, v169, v129\n"
"v_cvt_pk_bf16_f32 v152, v128, v129\n"
"v_mfma_f32_16x16x32_bf16 v[20:23], v[96:99], v[148:151], v[20:23]\n"
"v_exp_f32 v130, v130\n"
"s_waitcnt lgkmcnt(6)\n"
"v_mfma_f32_16x16x32_bf16 v[112:115], v[84:87], v[180:183], v[112:115]\n"
"ds_read_b64_tr_b16 v[96:97], v236 offset:0\n"
"v_add_f32 v168, v168, v130\n"
"v_mfma_f32_16x16x32_bf16 v[116:119], v[84:87], v[196:199], v[116:119]\n"
"ds_read_b64_tr_b16 v[98:99], v236 offset:4096\n"
"v_lshrrev_b32 v174, 4, v1\n"
"v_bfe_u32 v252, v1, 2, 2\n"
"v_lshl_add_u32 v252, v174, 3, v252\n"
"v_lshlrev_b32 v252, 5, v252\n"
"v_and_b32 v175, 3, v1\n"
"v_lshl_add_u32 v252, v175, 3, v252\n"
"s_add_u32 s24, s38, 0x10000\n"
"v_add_u32 v252, s24, v252\n"
"v_lshl_add_u32 v175, v174, 4, s24\n"
"s_lshl_b32 s24, s16, 6\n"
"s_add_u32 s24, s24, 0x14800\n"
"v_lshl_add_u32 v174, v174, 4, s24\n"
"v_exp_f32 v131, v131\n"
"v_mfma_f32_16x16x32_bf16 v[24:27], v[100:103], v[144:147], v[24:27]\n"
"ds_read_b128 v[80:83], v228 offset:4096\n"
"v_add_f32 v169, v169, v131\n"
"v_cvt_pk_bf16_f32 v153, v130, v131\n"
"v_mfma_f32_16x16x32_bf16 v[28:31], v[100:103], v[148:151], v[28:31]\n"
"v_exp_f32 v132, v132\n"
"s_waitcnt lgkmcnt(6)\n"
"v_mfma_f32_16x16x32_bf16 v[112:115], v[88:91], v[184:187], v[112:115]\n"
"ds_read_b64_tr_b16 v[100:101], v237 offset:0\n"
"global_load_lds_dwordx4 v241, s[30:31] offset:1024\n"
"s_add_u32 s30, s30, 0x2000\n"
"s_addc_u32 s31, s31, 0\n"
"v_add_f32 v170, v170, v132\n"
"v_mfma_f32_16x16x32_bf16 v[116:119], v[88:91], v[200:203], v[116:119]\n"
"ds_read_b64_tr_b16 v[102:103], v237 offset:4096\n"
"v_exp_f32 v133, v133\n"
"v_mfma_f32_16x16x32_bf16 v[32:35], v[104:107], v[144:147], v[32:35]\n"
"ds_read_b128 v[84:87], v229 offset:4096\n"
"v_add_f32 v171, v171, v133\n"
"v_cvt_pk_bf16_f32 v156, v132, v133\n"
"v_mfma_f32_16x16x32_bf16 v[36:39], v[104:107], v[148:151], v[36:39]\n"
"v_exp_f32 v134, v134\n"
"s_waitcnt lgkmcnt(6)\n"
"v_mfma_f32_16x16x32_bf16 v[112:115], v[92:95], v[188:191], v[112:115]\n"
"ds_read_b64_tr_b16 v[104:105], v238 offset:0\n"
"v_add_f32 v170, v170, v134\n"
"v_mfma_f32_16x16x32_bf16 v[116:119], v[92:95], v[204:207], v[116:119]\n"
"ds_read_b64_tr_b16 v[106:107], v238 offset:4096\n"
"v_exp_f32 v135, v135\n"
"v_mfma_f32_16x16x32_bf16 v[40:43], v[108:111], v[144:147], v[40:43]\n"
"ds_read_b128 v[88:91], v230 offset:4096\n"
"v_add_f32 v171, v171, v135\n"
"v_cvt_pk_bf16_f32 v157, v134, v135\n"
"v_mfma_f32_16x16x32_bf16 v[44:47], v[108:111], v[148:151], v[44:47]\n"
"s_add_u32 m0, s29, 0x24000\n"
"v_exp_f32 v136, v136\n"
"s_waitcnt lgkmcnt(6)\n"
"v_mfma_f32_16x16x32_bf16 v[120:123], v[80:83], v[176:179], v[160:163]\n"
"ds_read_b64_tr_b16 v[108:109], v239 offset:0\n"
"global_load_lds_dwordx4 v242, s[34:35]\n"
"v_add_f32 v168, v168, v136\n"
"v_exp_f32 v137, v137\n"
"v_mfma_f32_16x16x32_bf16 v[124:127], v[80:83], v[192:195], v[164:167]\n"
"ds_read_b64_tr_b16 v[110:111], v239 offset:4096\n"
"v_add_f32 v169, v169, v137\n"
"v_cvt_pk_bf16_f32 v154, v136, v137\n"
"v_mfma_f32_16x16x32_bf16 v[48:51], v[96:99], v[144:147], v[48:51]\n"
"ds_read_b128 v[92:95], v231 offset:4096\n"
"v_exp_f32 v138, v138\n"
"v_mfma_f32_16x16x32_bf16 v[52:55], v[96:99], v[148:151], v[52:55]\n"
"v_add_f32 v168, v168, v138\n"
"s_waitcnt lgkmcnt(6)\n"
"v_mfma_f32_16x16x32_bf16 v[120:123], v[84:87], v[180:183], v[120:123]\n"
"v_exp_f32 v139, v139\n"
"v_mfma_f32_16x16x32_bf16 v[124:127], v[84:87], v[196:199], v[124:127]\n"
"v_add_f32 v169, v169, v139\n"
"v_cvt_pk_bf16_f32 v155, v138, v139\n"
"v_mfma_f32_16x16x32_bf16 v[56:59], v[100:103], v[144:147], v[56:59]\n"
"v_exp_f32 v140, v140\n"
"v_mfma_f32_16x16x32_bf16 v[60:63], v[100:103], v[148:151], v[60:63]\n"
"s_waitcnt vmcnt(7) lgkmcnt(0)\n"
"s_barrier\n"
"global_load_lds_dwordx4 v243, s[34:35] offset:1024\n"
"s_add_u32 s34, s34, 0x2000\n"
"s_addc_u32 s35, s35, 0\n"
"v_add_f32 v170, v170, v140\n"
"v_mfma_f32_16x16x32_bf16 v[120:123], v[88:91], v[184:187], v[120:123]\n"
"ds_read_b64_tr_b16 v[96:97], v232 offset:32768\n"
"v_exp_f32 v141, v141\n"
"v_mfma_f32_16x16x32_bf16 v[124:127], v[88:91], v[200:203], v[124:127]\n"
"ds_read_b64_tr_b16 v[98:99], v232 offset:36864\n"
"v_add_f32 v171, v171, v141\n"
"v_cvt_pk_bf16_f32 v158, v140, v141\n"
"v_mfma_f32_16x16x32_bf16 v[64:67], v[104:107], v[144:147], v[64:67]\n"
"ds_read_b128 v[80:83], v228 offset:32768\n"
"v_exp_f32 v142, v142\n"
"v_mfma_f32_16x16x32_bf16 v[68:71], v[104:107], v[148:151], v[68:71]\n"
"ds_read_b64_tr_b16 v[100:101], v233 offset:32768\n"
"v_add_f32 v170, v170, v142\n"
"v_mfma_f32_16x16x32_bf16 v[120:123], v[92:95], v[188:191], v[120:123]\n"
"ds_read_b64_tr_b16 v[102:103], v233 offset:36864\n"
"s_sub_u32 s26, s16, 0\ns_cmp_lt_u32 s26, 3\ns_cselect_b64 exec, -1, 0\n"
"global_load_dword v255, v253, s[62:63] offset:0\n"
"s_mov_b64 exec, -1\n"
"v_exp_f32 v143, v143\n"
"v_mfma_f32_16x16x32_bf16 v[124:127], v[92:95], v[204:207], v[124:127]\n"
"ds_read_b128 v[84:87], v229 offset:32768\n"
"v_add_f32 v171, v171, v143\n"
"v_cvt_pk_bf16_f32 v159, v142, v143\n"
"v_mfma_f32_16x16x32_bf16 v[72:75], v[108:111], v[144:147], v[72:75]\n"
"ds_read_b64_tr_b16 v[104:105], v234 offset:32768\n"
"v_mfma_f32_16x16x32_bf16 v[76:79], v[108:111], v[148:151], v[76:79]\n"
"ds_read_b64_tr_b16 v[106:107], v234 offset:36864\n"
"ds_read_b128 v[88:91], v230 offset:32768\n"
"s_waitcnt lgkmcnt(6)\n"
"v_mfma_f32_16x16x32_bf16 v[128:131], v[80:83], v[176:179], v[160:163]\n"
"ds_read_b64_tr_b16 v[108:109], v235 offset:32768\n"
"s_mov_b32 m0, s29\n"
"v_exp_f32 v112, v112\n"
"v_mfma_f32_16x16x32_bf16 v[132:135], v[80:83], v[192:195], v[164:167]\n"
"ds_read_b64_tr_b16 v[110:111], v235 offset:36864\n"
"global_load_lds_dwordx4 v240, s[30:31]\n"
"ds_read_b128 v[244:247], v174 offset:0\n"
"ds_read_b64_tr_b16 v[248:249], v252 offset:0\n"
"ds_read_b64_tr_b16 v[250:251], v252 offset:128\n"
"v_add_f32 v168, v168, v112\n"
"v_exp_f32 v113, v113\n"
"v_mfma_f32_16x16x32_bf16 v[16:19], v[96:99], v[152:155], v[16:19]\n"
"ds_read_b128 v[92:95], v231 offset:32768\n"
"v_add_f32 v169, v169, v113\n"
"v_cvt_pk_bf16_f32 v144, v112, v113\n"
"v_mfma_f32_16x16x32_bf16 v[20:23], v[96:99], v[156:159], v[20:23]\n"
"v_exp_f32 v114, v114\n"
"s_waitcnt lgkmcnt(9)\n"
"v_mfma_f32_16x16x32_bf16 v[128:131], v[84:87], v[180:183], v[128:131]\n"
"ds_read_b64_tr_b16 v[96:97], v236 offset:32768\n"
"v_add_f32 v168, v168, v114\n"
"v_mfma_f32_16x16x32_bf16 v[132:135], v[84:87], v[196:199], v[132:135]\n"
"ds_read_b64_tr_b16 v[98:99], v236 offset:36864\n"
"v_exp_f32 v115, v115\n"
"v_mfma_f32_16x16x32_bf16 v[24:27], v[100:103], v[152:155], v[24:27]\n"
"ds_read_b128 v[80:83], v228 offset:36864\n"
"s_waitcnt lgkmcnt(4)\n"
"v_mfma_f32_16x16x32_bf16 v[208:211], v[248:251], v[244:247], 0\n"
"ds_read_b128 v[244:247], v174 offset:512\n"
"ds_read_b64_tr_b16 v[248:249], v252 offset:8192\n"
"ds_read_b64_tr_b16 v[250:251], v252 offset:8320\n"
"v_add_f32 v169, v169, v115\n"
"v_cvt_pk_bf16_f32 v145, v114, v115\n"
"v_mfma_f32_16x16x32_bf16 v[28:31], v[100:103], v[156:159], v[28:31]\n"
"v_exp_f32 v116, v116\n"
"v_mfma_f32_16x16x32_bf16 v[128:131], v[88:91], v[184:187], v[128:131]\n"
"ds_read_b64_tr_b16 v[100:101], v237 offset:32768\n"
"global_load_lds_dwordx4 v241, s[30:31] offset:1024\n"
"s_add_u32 s30, s30, 0x2000\n"
"s_addc_u32 s31, s31, 0\n"
"v_add_f32 v170, v170, v116\n"
"v_mfma_f32_16x16x32_bf16 v[132:135], v[88:91], v[200:203], v[132:135]\n"
"ds_read_b64_tr_b16 v[102:103], v237 offset:36864\n"
"v_exp_f32 v117, v117\n"
"v_mfma_f32_16x16x32_bf16 v[32:35], v[104:107], v[152:155], v[32:35]\n"
"ds_read_b128 v[84:87], v229 offset:36864\n"
"v_add_f32 v171, v171, v117\n"
"v_cvt_pk_bf16_f32 v148, v116, v117\n"
"v_mfma_f32_16x16x32_bf16 v[36:39], v[104:107], v[156:159], v[36:39]\n"
"s_waitcnt lgkmcnt(3)\n"
"v_mfma_f32_16x16x32_bf16 v[208:211], v[248:251], v[244:247], v[208:211]\n"
"v_exp_f32 v118, v118\n"
"v_mfma_f32_16x16x32_bf16 v[128:131], v[92:95], v[188:191], v[128:131]\n"
"ds_read_b64_tr_b16 v[104:105], v238 offset:32768\n"
"v_add_f32 v170, v170, v118\n"
"v_mfma_f32_16x16x32_bf16 v[132:135], v[92:95], v[204:207], v[132:135]\n"
"ds_read_b64_tr_b16 v[106:107], v238 offset:36864\n"
"s_cmp_ge_u32 s16, 2\ns_cbranch_scc1 GV_NO3\nds_read_b128 v[244:247], v174 offset:1024\nds_read_b64_tr_b16 v[248:249], v252 offset:16384\nds_read_b64_tr_b16 v[250:251], v252 offset:16512\ns_waitcnt lgkmcnt(0)\nv_mfma_f32_16x16x32_bf16 v[208:211], v[248:251], v[244:247], v[208:211]\nGV_NO3:\ns_nop 7\n"
"v_exp_f32 v119, v119\n"
"v_mfma_f32_16x16x32_bf16 v[40:43], v[108:111], v[152:155], v[40:43]\n"
"ds_read_b128 v[88:91], v230 offset:36864\n"
"v_add_f32 v171, v171, v119\n"
"v_cvt_pk_bf16_f32 v149, v118, v119\n"
"v_mfma_f32_16x16x32_bf16 v[44:47], v[108:111], v[156:159], v[44:47]\n"
"s_add_u32 m0, s29, 0x4000\n"
"ds_write_b128 v175, v[208:211] offset:0\n"
"v_exp_f32 v120, v120\n"
"v_mfma_f32_16x16x32_bf16 v[136:139], v[80:83], v[176:179], v[160:163]\n"
"ds_read_b64_tr_b16 v[108:109], v239 offset:32768\n"
"global_load_lds_dwordx4 v242, s[34:35]\n"
"v_add_f32 v168, v168, v120\n"
"v_exp_f32 v121, v121\n"
"v_mfma_f32_16x16x32_bf16 v[140:143], v[80:83], v[192:195], v[164:167]\n"
"ds_read_b64_tr_b16 v[110:111], v239 offset:36864\n"
"v_add_f32 v169, v169, v121\n"
"v_cvt_pk_bf16_f32 v146, v120, v121\n"
"v_mfma_f32_16x16x32_bf16 v[48:51], v[96:99], v[152:155], v[48:51]\n"
"ds_read_b128 v[92:95], v231 offset:36864\n"
"v_exp_f32 v122, v122\n"
"v_mfma_f32_16x16x32_bf16 v[52:55], v[96:99], v[156:159], v[52:55]\n"
"v_add_f32 v168, v168, v122\n"
"s_waitcnt lgkmcnt(7)\n"
"v_mfma_f32_16x16x32_bf16 v[136:139], v[84:87], v[180:183], v[136:139]\n"
"v_exp_f32 v123, v123\n"
"v_mfma_f32_16x16x32_bf16 v[140:143], v[84:87], v[196:199], v[140:143]\n"
"v_add_f32 v169, v169, v123\n"
"v_cvt_pk_bf16_f32 v147, v122, v123\n"
"v_mfma_f32_16x16x32_bf16 v[56:59], v[100:103], v[152:155], v[56:59]\n"
"v_exp_f32 v124, v124\n"
"v_mfma_f32_16x16x32_bf16 v[60:63], v[100:103], v[156:159], v[60:63]\n"
"s_waitcnt vmcnt(7) lgkmcnt(0)\n"
"s_barrier\n"
"global_load_lds_dwordx4 v243, s[34:35] offset:1024\n"
"s_add_u32 s34, s34, 0x2000\n"
"s_addc_u32 s35, s35, 0\n"
"v_add_f32 v170, v170, v124\n"
"v_mfma_f32_16x16x32_bf16 v[136:139], v[88:91], v[184:187], v[136:139]\n"
"ds_read_b64_tr_b16 v[96:97], v220 offset:0\n"
"v_exp_f32 v125, v125\n"
"v_mfma_f32_16x16x32_bf16 v[140:143], v[88:91], v[200:203], v[140:143]\n"
"ds_read_b64_tr_b16 v[98:99], v220 offset:4096\n"
"v_add_f32 v171, v171, v125\n"
"v_cvt_pk_bf16_f32 v150, v124, v125\n"
"v_mfma_f32_16x16x32_bf16 v[64:67], v[104:107], v[152:155], v[64:67]\n"
"ds_read_b128 v[80:83], v216 offset:0\n"
"v_exp_f32 v126, v126\n"
"v_mfma_f32_16x16x32_bf16 v[68:71], v[104:107], v[156:159], v[68:71]\n"
"ds_read_b64_tr_b16 v[100:101], v221 offset:0\n"
"v_add_f32 v170, v170, v126\n"
"v_mfma_f32_16x16x32_bf16 v[136:139], v[92:95], v[188:191], v[136:139]\n"
"ds_read_b64_tr_b16 v[102:103], v221 offset:4096\n"
"v_exp_f32 v127, v127\n"
"v_mfma_f32_16x16x32_bf16 v[140:143], v[92:95], v[204:207], v[140:143]\n"
"ds_read_b128 v[84:87], v217 offset:0\n"
"v_add_f32 v171, v171, v127\n"
"v_cvt_pk_bf16_f32 v151, v126, v127\n"
"v_mfma_f32_16x16x32_bf16 v[72:75], v[108:111], v[152:155], v[72:75]\n"
"ds_read_b64_tr_b16 v[104:105], v222 offset:0\n"
"v_mfma_f32_16x16x32_bf16 v[76:79], v[108:111], v[156:159], v[76:79]\n"
"ds_read_b64_tr_b16 v[106:107], v222 offset:4096\n"
"ds_read_b128 v[88:91], v218 offset:0\n"
"s_waitcnt lgkmcnt(6)\n"
"v_mfma_f32_16x16x32_bf16 v[112:115], v[80:83], v[176:179], v[160:163]\n"
"ds_read_b64_tr_b16 v[108:109], v223 offset:0\n"
"s_add_u32 m0, s29, 0x8000\n"
"v_exp_f32 v128, v128\n"
"v_mfma_f32_16x16x32_bf16 v[116:119], v[80:83], v[192:195], v[164:167]\n"
"ds_read_b64_tr_b16 v[110:111], v223 offset:4096\n"
"global_load_lds_dwordx4 v240, s[30:31]\n"
"v_and_b32 v252, 15, v1\n"
"v_lshlrev_b32 v252, 2, v252\n"
"v_add_u32 v174, 0x10000, v252\n"
"ds_read_b32 v244, v174 offset:0\n"
"ds_read_b32 v245, v174 offset:1024\n"
"ds_read_b32 v246, v174 offset:2048\n"
"ds_read_b32 v247, v174 offset:3072\n"
"ds_read_b32 v248, v174 offset:4096\n"
"ds_read_b32 v249, v174 offset:5120\n"
"ds_read_b32 v250, v174 offset:6144\n"
"ds_read_b32 v251, v174 offset:7168\n"
"v_add_f32 v168, v168, v128\n"
"v_exp_f32 v129, v129\n"
"v_mfma_f32_16x16x32_bf16 v[16:19], v[96:99], v[144:147], v[16:19]\n"
"ds_read_b128 v[92:95], v219 offset:0\n"
"v_add_f32 v169, v169, v129\n"
"v_cvt_pk_bf16_f32 v152, v128, v129\n"
"v_mfma_f32_16x16x32_bf16 v[20:23], v[96:99], v[148:151], v[20:23]\n"
"v_exp_f32 v130, v130\n"
"s_waitcnt lgkmcnt(14)\n"
"v_mfma_f32_16x16x32_bf16 v[112:115], v[84:87], v[180:183], v[112:115]\n"
"ds_read_b64_tr_b16 v[96:97], v224 offset:0\n"
"v_add_f32 v168, v168, v130\n"
"v_mfma_f32_16x16x32_bf16 v[116:119], v[84:87], v[196:199], v[116:119]\n"
"ds_read_b64_tr_b16 v[98:99], v224 offset:4096\n"
"s_mul_i32 s38, s37, 57\n"
"s_lshr_b32 s38, s38, 11\n"
"s_lshl_b32 s24, s37, 6\n"
"s_add_u32 s24, s24, 0x7a80\n"
"v_add_u32 v252, s24, v252\n"
"s_cmp_eq_u32 s38, 2\ns_cselect_b32 s38, 1.0, 0\ns_lshl_b32 s24, s16, 1\ns_cmp_lt_u32 s24, s36\ns_cselect_b64 s[24:25], 0xffff, 0\n"
"v_exp_f32 v131, v131\n"
"v_mfma_f32_16x16x32_bf16 v[24:27], v[100:103], v[144:147], v[24:27]\n"
"ds_read_b128 v[80:83], v216 offset:4096\n"
"v_add_f32 v169, v169, v131\n"
"v_cvt_pk_bf16_f32 v153, v130, v131\n"
"v_mfma_f32_16x16x32_bf16 v[28:31], v[100:103], v[148:151], v[28:31]\n"
"v_exp_f32 v132, v132\n"
"s_waitcnt lgkmcnt(14)\n"
"v_mfma_f32_16x16x32_bf16 v[112:115], v[88:91], v[184:187], v[112:115]\n"
"ds_read_b64_tr_b16 v[100:101], v225 offset:0\n"
"global_load_lds_dwordx4 v241, s[30:31] offset:1024\n"
"s_add_u32 s30, s30, 0x2000\n"
"s_addc_u32 s31, s31, 0\n"
"v_add_f32 v170, v170, v132\n"
"v_mfma_f32_16x16x32_bf16 v[116:119], v[88:91], v[200:203], v[116:119]\n"
"ds_read_b64_tr_b16 v[102:103], v225 offset:4096\n"
"s_waitcnt lgkmcnt(9)\n"
"v_add_f32 v244, v244, v248\n"
"s_waitcnt lgkmcnt(8)\n"
"v_add_f32 v245, v245, v249\n"
"s_waitcnt lgkmcnt(7)\n"
"v_add_f32 v246, v246, v250\n"
"s_waitcnt lgkmcnt(6)\n"
"v_add_f32 v247, v247, v251\n"
"v_add_f32 v244, v244, v246\n"
"v_add_f32 v245, v245, v247\n"
"v_add_f32 v244, v244, v245\n"
"v_exp_f32 v133, v133\n"
"v_mfma_f32_16x16x32_bf16 v[32:35], v[104:107], v[144:147], v[32:35]\n"
"ds_read_b128 v[84:87], v217 offset:4096\n"
"v_add_f32 v171, v171, v133\n"
"v_cvt_pk_bf16_f32 v156, v132, v133\n"
"v_mfma_f32_16x16x32_bf16 v[36:39], v[104:107], v[148:151], v[36:39]\n"
"v_exp_f32 v134, v134\n"
"s_waitcnt lgkmcnt(6)\n"
"v_mfma_f32_16x16x32_bf16 v[112:115], v[92:95], v[188:191], v[112:115]\n"
"ds_read_b64_tr_b16 v[104:105], v226 offset:0\n"
"v_add_f32 v170, v170, v134\n"
"v_mfma_f32_16x16x32_bf16 v[116:119], v[92:95], v[204:207], v[116:119]\n"
"ds_read_b64_tr_b16 v[106:107], v226 offset:4096\n"
"v_cvt_pk_bf16_f32 v244, v244, v244\n"
"v_lshlrev_b32 v244, 16, v244\n"
"v_add_f32 v244, s38, v244\n"
"v_cvt_pk_bf16_f32 v244, v244, v244\n"
"v_lshlrev_b32 v244, 16, v244\n"
"v_exp_f32 v135, v135\n"
"v_mfma_f32_16x16x32_bf16 v[40:43], v[108:111], v[144:147], v[40:43]\n"
"ds_read_b128 v[88:91], v218 offset:4096\n"
"v_add_f32 v171, v171, v135\n"
"v_cvt_pk_bf16_f32 v157, v134, v135\n"
"v_mfma_f32_16x16x32_bf16 v[44:47], v[108:111], v[148:151], v[44:47]\n"
"s_add_u32 m0, s29, 0xc000\n"
"v_exp_f32 v136, v136\n"
"s_waitcnt lgkmcnt(6)\n"
"v_mfma_f32_16x16x32_bf16 v[120:123], v[80:83], v[176:179], v[160:163]\n"
"ds_read_b64_tr_b16 v[108:109], v227 offset:0\n"
"global_load_lds_dwordx4 v242, s[34:35]\n"
"v_add_f32 v168, v168, v136\n"
"v_exp_f32 v137, v137\n"
"v_mfma_f32_16x16x32_bf16 v[124:127], v[80:83], v[192:195], v[164:167]\n"
"ds_read_b64_tr_b16 v[110:111], v227 offset:4096\n"
"s_mov_b64 exec, s[24:25]\n"
"global_store_dword v252, v244, s[4:5] offset:0 sc1\n"
"s_mov_b64 exec, -1\n"
"v_add_f32 v169, v169, v137\n"
"v_cvt_pk_bf16_f32 v154, v136, v137\n"
"v_mfma_f32_16x16x32_bf16 v[48:51], v[96:99], v[144:147], v[48:51]\n"
"ds_read_b128 v[92:95], v219 offset:4096\n"
"v_exp_f32 v138, v138\n"
"v_mfma_f32_16x16x32_bf16 v[52:55], v[96:99], v[148:151], v[52:55]\n"
"v_add_f32 v168, v168, v138\n"
"s_waitcnt lgkmcnt(6)\n"
"v_mfma_f32_16x16x32_bf16 v[120:123], v[84:87], v[180:183], v[120:123]\n"
"v_exp_f32 v139, v139\n"
"v_mfma_f32_16x16x32_bf16 v[124:127], v[84:87], v[196:199], v[124:127]\n"
"v_add_f32 v169, v169, v139\n"
"v_cvt_pk_bf16_f32 v155, v138, v139\n"
"v_mfma_f32_16x16x32_bf16 v[56:59], v[100:103], v[144:147], v[56:59]\n"
"v_exp_f32 v140, v140\n"
"v_mfma_f32_16x16x32_bf16 v[60:63], v[100:103], v[148:151], v[60:63]\n"
"s_waitcnt vmcnt(7) lgkmcnt(0)\n"
"s_barrier\n"
"global_load_lds_dwordx4 v243, s[34:35] offset:1024\n"
"s_add_u32 s34, s34, 0x2000\n"
"s_addc_u32 s35, s35, 0\n"
"v_add_f32 v170, v170, v140\n"
"v_mfma_f32_16x16x32_bf16 v[120:123], v[88:91], v[184:187], v[120:123]\n"
"ds_read_b64_tr_b16 v[96:97], v220 offset:32768\n"
"v_exp_f32 v141, v141\n"
"v_mfma_f32_16x16x32_bf16 v[124:127], v[88:91], v[200:203], v[124:127]\n"
"ds_read_b64_tr_b16 v[98:99], v220 offset:36864\n"
"v_add_f32 v171, v171, v141\n"
"v_cvt_pk_bf16_f32 v158, v140, v141\n"
"v_mfma_f32_16x16x32_bf16 v[64:67], v[104:107], v[144:147], v[64:67]\n"
"ds_read_b128 v[80:83], v216 offset:32768\n"
"v_exp_f32 v142, v142\n"
"v_mfma_f32_16x16x32_bf16 v[68:71], v[104:107], v[148:151], v[68:71]\n"
"ds_read_b64_tr_b16 v[100:101], v221 offset:32768\n"
"v_add_f32 v170, v170, v142\n"
"v_mfma_f32_16x16x32_bf16 v[120:123], v[92:95], v[188:191], v[120:123]\n"
"ds_read_b64_tr_b16 v[102:103], v221 offset:36864\n"
"v_exp_f32 v143, v143\n"
"v_mfma_f32_16x16x32_bf16 v[124:127], v[92:95], v[204:207], v[124:127]\n"
"ds_read_b128 v[84:87], v217 offset:32768\n"
"v_add_f32 v171, v171, v143\n"
"v_cvt_pk_bf16_f32 v159, v142, v143\n"
"v_mfma_f32_16x16x32_bf16 v[72:75], v[108:111], v[144:147], v[72:75]\n"
"ds_read_b64_tr_b16 v[104:105], v222 offset:32768\n"
"v_mfma_f32_16x16x32_bf16 v[76:79], v[108:111], v[148:151], v[76:79]\n"
"ds_read_b64_tr_b16 v[106:107], v222 offset:36864\n"
"ds_read_b128 v[88:91], v218 offset:32768\n"
"s_waitcnt lgkmcnt(6)\n"
"v_mfma_f32_16x16x32_bf16 v[128:131], v[80:83], v[176:179], v[160:163]\n"
"ds_read_b64_tr_b16 v[108:109], v223 offset:32768\n"
"s_add_u32 m0, s29, 0x18000\n"
"v_exp_f32 v112, v112\n"
"v_mfma_f32_16x16x32_bf16 v[132:135], v[80:83], v[192:195], v[164:167]\n"
"ds_read_b64_tr_b16 v[110:111], v223 offset:36864\n"
"global_load_lds_dwordx4 v240, s[30:31]\n"
"v_add_f32 v168, v168, v112\n"
"v_exp_f32 v113, v113\n"
"v_mfma_f32_16x16x32_bf16 v[16:19], v[96:99], v[152:155], v[16:19]\n"
"ds_read_b128 v[92:95], v219 offset:32768\n"
"v_add_f32 v169, v169, v113\n"
"v_cvt_pk_bf16_f32 v144, v112, v113\n"
"v_mfma_f32_16x16x32_bf16 v[20:23], v[96:99], v[156:159], v[20:23]\n"
"v_exp_f32 v114, v114\n"
"s_waitcnt lgkmcnt(6)\n"
"v_mfma_f32_16x16x32_bf16 v[128:131], v[84:87], v[180:183], v[128:131]\n"
"ds_read_b64_tr_b16 v[96:97], v224 offset:32768\n"
"v_add_f32 v168, v168, v114\n"
"v_mfma_f32_16x16x32_bf16 v[132:135], v[84:87], v[196:199], v[132:135]\n"
"ds_read_b64_tr_b16 v[98:99], v224 offset:36864\n"
"v_exp_f32 v115, v115\n"
"v_mfma_f32_16x16x32_bf16 v[24:27], v[100:103], v[152:155], v[24:27]\n"
"ds_read_b128 v[80:83], v216 offset:36864\n"
"v_add_f32 v169, v169, v115\n"
"v_cvt_pk_bf16_f32 v145, v114, v115\n"
"v_mfma_f32_16x16x32_bf16 v[28:31], v[100:103], v[156:159], v[28:31]\n"
"v_exp_f32 v116, v116\n"
"s_waitcnt lgkmcnt(6)\n"
"v_mfma_f32_16x16x32_bf16 v[128:131], v[88:91], v[184:187], v[128:131]\n"
"ds_read_b64_tr_b16 v[100:101], v225 offset:32768\n"
"global_load_lds_dwordx4 v241, s[30:31] offset:1024\n"
"s_add_u32 s30, s30, 0x2000\n"
"s_addc_u32 s31, s31, 0\n"
"v_add_f32 v170, v170, v116\n"
"v_mfma_f32_16x16x32_bf16 v[132:135], v[88:91], v[200:203], v[132:135]\n"
"ds_read_b64_tr_b16 v[102:103], v225 offset:36864\n"
"v_exp_f32 v117, v117\n"
"v_mfma_f32_16x16x32_bf16 v[32:35], v[104:107], v[152:155], v[32:35]\n"
"ds_read_b128 v[84:87], v217 offset:36864\n"
"v_add_f32 v171, v171, v117\n"
"v_cvt_pk_bf16_f32 v148, v116, v117\n"
"v_mfma_f32_16x16x32_bf16 v[36:39], v[104:107], v[156:159], v[36:39]\n"
"v_exp_f32 v118, v118\n"
"s_waitcnt lgkmcnt(6)\n"
"v_mfma_f32_16x16x32_bf16 v[128:131], v[92:95], v[188:191], v[128:131]\n"
"ds_read_b64_tr_b16 v[104:105], v226 offset:32768\n"
"v_add_f32 v170, v170, v118\n"
"v_mfma_f32_16x16x32_bf16 v[132:135], v[92:95], v[204:207], v[132:135]\n"
"ds_read_b64_tr_b16 v[106:107], v226 offset:36864\n"
"v_exp_f32 v119, v119\n"
"v_mfma_f32_16x16x32_bf16 v[40:43], v[108:111], v[152:155], v[40:43]\n"
"ds_read_b128 v[88:91], v218 offset:36864\n"
"v_add_f32 v171, v171, v119\n"
"v_cvt_pk_bf16_f32 v149, v118, v119\n"
"v_mfma_f32_16x16x32_bf16 v[44:47], v[108:111], v[156:159], v[44:47]\n"
"s_add_u32 m0, s29, 0x1c000\n"
"v_exp_f32 v120, v120\n"
"s_waitcnt lgkmcnt(6)\n"
"v_mfma_f32_16x16x32_bf16 v[136:139], v[80:83], v[176:179], v[160:163]\n"
"ds_read_b64_tr_b16 v[108:109], v227 offset:32768\n"
"global_load_lds_dwordx4 v242, s[34:35]\n"
"v_add_f32 v168, v168, v120\n"
"v_exp_f32 v121, v121\n"
"v_mfma_f32_16x16x32_bf16 v[140:143], v[80:83], v[192:195], v[164:167]\n"
"ds_read_b64_tr_b16 v[110:111], v227 offset:36864\n"
"v_add_f32 v169, v169, v121\n"
"v_cvt_pk_bf16_f32 v146, v120, v121\n"
"v_mfma_f32_16x16x32_bf16 v[48:51], v[96:99], v[152:155], v[48:51]\n"
"ds_read_b128 v[92:95], v219 offset:36864\n"
"v_exp_f32 v122, v122\n"
"v_mfma_f32_16x16x32_bf16 v[52:55], v[96:99], v[156:159], v[52:55]\n"
"v_add_f32 v168, v168, v122\n"
"s_waitcnt lgkmcnt(6)\n"
"v_mfma_f32_16x16x32_bf16 v[136:139], v[84:87], v[180:183], v[136:139]\n"
"v_exp_f32 v123, v123\n"
"v_mfma_f32_16x16x32_bf16 v[140:143], v[84:87], v[196:199], v[140:143]\n"
"v_add_f32 v169, v169, v123\n"
"v_cvt_pk_bf16_f32 v147, v122, v123\n"
"v_mfma_f32_16x16x32_bf16 v[56:59], v[100:103], v[152:155], v[56:59]\n"
"v_exp_f32 v124, v124\n"
"v_mfma_f32_16x16x32_bf16 v[60:63], v[100:103], v[156:159], v[60:63]\n"
"s_waitcnt vmcnt(7) lgkmcnt(0)\n"
"s_barrier\n"
"global_load_lds_dwordx4 v243, s[34:35] offset:1024\n"
"s_add_u32 s34, s34, 0x2000\n"
"s_addc_u32 s35, s35, 0\n"
"v_add_f32 v170, v170, v124\n"
"v_mfma_f32_16x16x32_bf16 v[136:139], v[88:91], v[184:187], v[136:139]\n"
"ds_read_b64_tr_b16 v[96:97], v232 offset:0\n"
"v_exp_f32 v125, v125\n"
"v_mfma_f32_16x16x32_bf16 v[140:143], v[88:91], v[200:203], v[140:143]\n"
"ds_read_b64_tr_b16 v[98:99], v232 offset:4096\n"
"v_add_f32 v171, v171, v125\n"
"v_cvt_pk_bf16_f32 v150, v124, v125\n"
"v_mfma_f32_16x16x32_bf16 v[64:67], v[104:107], v[152:155], v[64:67]\n"
"ds_read_b128 v[80:83], v228 offset:0\n"
"v_exp_f32 v126, v126\n"
"v_mfma_f32_16x16x32_bf16 v[68:71], v[104:107], v[156:159], v[68:71]\n"
"ds_read_b64_tr_b16 v[100:101], v233 offset:0\n"
"v_add_f32 v170, v170, v126\n"
"v_mfma_f32_16x16x32_bf16 v[136:139], v[92:95], v[188:191], v[136:139]\n"
"ds_read_b64_tr_b16 v[102:103], v233 offset:4096\n"
"v_exp_f32 v127, v127\n"
"v_mfma_f32_16x16x32_bf16 v[140:143], v[92:95], v[204:207], v[140:143]\n"
"ds_read_b128 v[84:87], v229 offset:0\n"
"v_add_f32 v171, v171, v127\n"
"v_cvt_pk_bf16_f32 v151, v126, v127\n"
"v_mfma_f32_16x16x32_bf16 v[72:75], v[108:111], v[152:155], v[72:75]\n"
"ds_read_b64_tr_b16 v[104:105], v234 offset:0\n"
"v_mfma_f32_16x16x32_bf16 v[76:79], v[108:111], v[156:159], v[76:79]\n"
"ds_read_b64_tr_b16 v[106:107], v234 offset:4096\n"
"ds_read_b128 v[88:91], v230 offset:0\n"
"s_waitcnt lgkmcnt(6)\n"
"v_mfma_f32_16x16x32_bf16 v[112:115], v[80:83], v[176:179], v[160:163]\n"
"ds_read_b64_tr_b16 v[108:109], v235 offset:0\n"
"s_add_u32 m0, s29, 0x20000\n"
"v_exp_f32 v128, v128\n"
"v_mfma_f32_16x16x32_bf16 v[116:119], v[80:83], v[192:195], v[164:167]\n"
"ds_read_b64_tr_b16 v[110:111], v235 offset:4096\n"
"global_load_lds_dwordx4 v240, s[30:31]\n"
"v_add_f32 v168, v168, v128\n"
"v_exp_f32 v129, v129\n"
"v_mfma_f32_16x16x32_bf16 v[16:19], v[96:99], v[144:147], v[16:19]\n"
"ds_read_b128 v[92:95], v231 offset:0\n"
"v_add_f32 v169, v169, v129\n"
"v_cvt_pk_bf16_f32 v152, v128, v129\n"
"v_mfma_f32_16x16x32_bf16 v[20:23], v[96:99], v[148:151], v[20:23]\n"
"v_exp_f32 v130, v130\n"
"s_waitcnt lgkmcnt(6)\n"
"v_mfma_f32_16x16x32_bf16 v[112:115], v[84:87], v[180:183], v[112:115]\n"
"ds_read_b64_tr_b16 v[96:97], v236 offset:0\n"
"v_add_f32 v168, v168, v130\n"
"v_mfma_f32_16x16x32_bf16 v[116:119], v[84:87], v[196:199], v[116:119]\n"
"ds_read_b64_tr_b16 v[98:99], v236 offset:4096\n"
"v_exp_f32 v131, v131\n"
"v_mfma_f32_16x16x32_bf16 v[24:27], v[100:103], v[144:147], v[24:27]\n"
"ds_read_b128 v[80:83], v228 offset:4096\n"
"v_add_f32 v169, v169, v131\n"
"v_cvt_pk_bf16_f32 v153, v130, v131\n"
"v_mfma_f32_16x16x32_bf16 v[28:31], v[100:103], v[148:151], v[28:31]\n"
"v_exp_f32 v132, v132\n"
"s_waitcnt lgkmcnt(6)\n"
"v_mfma_f32_16x16x32_bf16 v[112:115], v[88:91], v[184:187], v[112:115]\n"
"ds_read_b64_tr_b16 v[100:101], v237 offset:0\n"
"global_load_lds_dwordx4 v241, s[30:31] offset:1024\n"
"s_add_u32 s30, s30, 0x2000\n"
"s_addc_u32 s31, s31, 0\n"
"v_add_f32 v170, v170, v132\n"
"v_mfma_f32_16x16x32_bf16 v[116:119], v[88:91], v[200:203], v[116:119]\n"
"ds_read_b64_tr_b16 v[102:103], v237 offset:4096\n"
"v_exp_f32 v133, v133\n"
"v_mfma_f32_16x16x32_bf16 v[32:35], v[104:107], v[144:147], v[32:35]\n"
"ds_read_b128 v[84:87], v229 offset:4096\n"
"v_add_f32 v171, v171, v133\n"
"v_cvt_pk_bf16_f32 v156, v132, v133\n"
"v_mfma_f32_16x16x32_bf16 v[36:39], v[104:107], v[148:151], v[36:39]\n"
"v_exp_f32 v134, v134\n"
"s_waitcnt lgkmcnt(6)\n"
"v_mfma_f32_16x16x32_bf16 v[112:115], v[92:95], v[188:191], v[112:115]\n"
"ds_read_b64_tr_b16 v[104:105], v238 offset:0\n"
"v_add_f32 v170, v170, v134\n"
"v_mfma_f32_16x16x32_bf16 v[116:119], v[92:95], v[204:207], v[116:119]\n"
"ds_read_b64_tr_b16 v[106:107], v238 offset:4096\n"
"v_exp_f32 v135, v135\n"
"v_mfma_f32_16x16x32_bf16 v[40:43], v[108:111], v[144:147], v[40:43]\n"
"ds_read_b128 v[88:91], v230 offset:4096\n"
"v_add_f32 v171, v171, v135\n"
"v_cvt_pk_bf16_f32 v157, v134, v135\n"
"v_mfma_f32_16x16x32_bf16 v[44:47], v[108:111], v[148:151], v[44:47]\n"
"s_add_u32 m0, s29, 0x24000\n"
"v_exp_f32 v136, v136\n"
"s_waitcnt lgkmcnt(6)\n"
"v_mfma_f32_16x16x32_bf16 v[120:123], v[80:83], v[176:179], v[160:163]\n"
"ds_read_b64_tr_b16 v[108:109], v239 offset:0\n"
"global_load_lds_dwordx4 v242, s[34:35]\n"
"v_add_f32 v168, v168, v136\n"
"v_exp_f32 v137, v137\n"
"v_mfma_f32_16x16x32_bf16 v[124:127], v[80:83], v[192:195], v[164:167]\n"
"ds_read_b64_tr_b16 v[110:111], v239 offset:4096\n"
"v_add_f32 v169, v169, v137\n"
"v_cvt_pk_bf16_f32 v154, v136, v137\n"
"v_mfma_f32_16x16x32_bf16 v[48:51], v[96:99], v[144:147], v[48:51]\n"
"ds_read_b128 v[92:95], v231 offset:4096\n"
"v_exp_f32 v138, v138\n"
"v_mfma_f32_16x16x32_bf16 v[52:55], v[96:99], v[148:151], v[52:55]\n"
"v_add_f32 v168, v168, v138\n"
"s_waitcnt lgkmcnt(6)\n"
"v_mfma_f32_16x16x32_bf16 v[120:123], v[84:87], v[180:183], v[120:123]\n"
"v_exp_f32 v139, v139\n"
"v_mfma_f32_16x16x32_bf16 v[124:127], v[84:87], v[196:199], v[124:127]\n"
"v_add_f32 v169, v169, v139\n"
"v_cvt_pk_bf16_f32 v155, v138, v139\n"
"v_mfma_f32_16x16x32_bf16 v[56:59], v[100:103], v[144:147], v[56:59]\n"
"v_exp_f32 v140, v140\n"
"v_mfma_f32_16x16x32_bf16 v[60:63], v[100:103], v[148:151], v[60:63]\n"
"s_waitcnt vmcnt(7) lgkmcnt(0)\n"
"s_barrier\n"
"global_load_lds_dwordx4 v243, s[34:35] offset:1024\n"
"s_add_u32 s34, s34, 0x2000\n"
"s_addc_u32 s35, s35, 0\n"
"v_add_f32 v170, v170, v140\n"
"v_mfma_f32_16x16x32_bf16 v[120:123], v[88:91], v[184:187], v[120:123]\n"
"ds_read_b64_tr_b16 v[96:97], v232 offset:32768\n"
"v_exp_f32 v141, v141\n"
"v_mfma_f32_16x16x32_bf16 v[124:127], v[88:91], v[200:203], v[124:127]\n"
"ds_read_b64_tr_b16 v[98:99], v232 offset:36864\n"
"v_add_f32 v171, v171, v141\n"
"v_cvt_pk_bf16_f32 v158, v140, v141\n"
"v_mfma_f32_16x16x32_bf16 v[64:67], v[104:107], v[144:147], v[64:67]\n"
"ds_read_b128 v[80:83], v228 offset:32768\n"
"v_exp_f32 v142, v142\n"
"v_mfma_f32_16x16x32_bf16 v[68:71], v[104:107], v[148:151], v[68:71]\n"
"ds_read_b64_tr_b16 v[100:101], v233 offset:32768\n"
"v_add_f32 v170, v170, v142\n"
"v_mfma_f32_16x16x32_bf16 v[120:123], v[92:95], v[188:191], v[120:123]\n"
"ds_read_b64_tr_b16 v[102:103], v233 offset:36864\n"
"s_sub_u32 s26, s16, 3\ns_cmp_lt_u32 s26, 3\ns_cselect_b64 exec, -1, 0\n"
"global_load_dword v255, v253, s[68:69] offset:0\n"
"s_mov_b64 exec, -1\n"
"v_exp_f32 v143, v143\n"
"v_mfma_f32_16x16x32_bf16 v[124:127], v[92:95], v[204:207], v[124:127]\n"
"ds_read_b128 v[84:87], v229 offset:32768\n"
"v_add_f32 v171, v171, v143\n"
"v_cvt_pk_bf16_f32 v159, v142, v143\n"
"v_mfma_f32_16x16x32_bf16 v[72:75], v[108:111], v[144:147], v[72:75]\n"
"ds_read_b64_tr_b16 v[104:105], v234 offset:32768\n"
"v_mfma_f32_16x16x32_bf16 v[76:79], v[108:111], v[148:151], v[76:79]\n"
"ds_read_b64_tr_b16 v[106:107], v234 offset:36864\n"
"ds_read_b128 v[88:91], v230 offset:32768\n"
"s_waitcnt lgkmcnt(6)\n"
"v_mfma_f32_16x16x32_bf16 v[128:131], v[80:83], v[176:179], v[160:163]\n"
"ds_read_b64_tr_b16 v[108:109], v235 offset:32768\n"
"s_mov_b32 m0, s29\n"
"v_exp_f32 v112, v112\n"
"v_mfma_f32_16x16x32_bf16 v[132:135], v[80:83], v[192:195], v[164:167]\n"
"ds_read_b64_tr_b16 v[110:111], v235 offset:36864\n"
"global_load_lds_dwordx4 v240, s[30:31]\n"
"v_add_f32 v168, v168, v112\n"
"v_exp_f32 v113, v113\n"
"v_mfma_f32_16x16x32_bf16 v[16:19], v[96:99], v[152:155], v[16:19]\n"
"ds_read_b128 v[92:95], v231 offset:32768\n"
"v_add_f32 v169, v169, v113\n"
"v_cvt_pk_bf16_f32 v144, v112, v113\n"
"v_mfma_f32_16x16x32_bf16 v[20:23], v[96:99], v[156:159], v[20:23]\n"
"v_exp_f32 v114, v114\n"
"s_waitcnt lgkmcnt(6)\n"
"v_mfma_f32_16x16x32_bf16 v[128:131], v[84:87], v[180:183], v[128:131]\n"
"ds_read_b64_tr_b16 v[96:97], v236 offset:32768\n"
"v_add_f32 v168, v168, v114\n"
"v_mfma_f32_16x16x32_bf16 v[132:135], v[84:87], v[196:199], v[132:135]\n"
"ds_read_b64_tr_b16 v[98:99], v236 offset:36864\n"
"v_exp_f32 v115, v115\n"
"v_mfma_f32_16x16x32_bf16 v[24:27], v[100:103], v[152:155], v[24:27]\n"
"ds_read_b128 v[80:83], v228 offset:36864\n"
"v_add_f32 v169, v169, v115\n"
"v_cvt_pk_bf16_f32 v145, v114, v115\n"
"v_mfma_f32_16x16x32_bf16 v[28:31], v[100:103], v[156:159], v[28:31]\n"
"v_exp_f32 v116, v116\n"
"s_waitcnt lgkmcnt(6)\n"
"v_mfma_f32_16x16x32_bf16 v[128:131], v[88:91], v[184:187], v[128:131]\n"
"ds_read_b64_tr_b16 v[100:101], v237 offset:32768\n"
"global_load_lds_dwordx4 v241, s[30:31] offset:1024\n"
"s_add_u32 s30, s30, 0x2000\n"
"s_addc_u32 s31, s31, 0\n"
"v_add_f32 v170, v170, v116\n"
"v_mfma_f32_16x16x32_bf16 v[132:135], v[88:91], v[200:203], v[132:135]\n"
"ds_read_b64_tr_b16 v[102:103], v237 offset:36864\n"
"v_exp_f32 v117, v117\n"
"v_mfma_f32_16x16x32_bf16 v[32:35], v[104:107], v[152:155], v[32:35]\n"
"ds_read_b128 v[84:87], v229 offset:36864\n"
"v_add_f32 v171, v171, v117\n"
"v_cvt_pk_bf16_f32 v148, v116, v117\n"
"v_mfma_f32_16x16x32_bf16 v[36:39], v[104:107], v[156:159], v[36:39]\n"
"v_exp_f32 v118, v118\n"
"s_waitcnt lgkmcnt(6)\n"
"v_mfma_f32_16x16x32_bf16 v[128:131], v[92:95], v[188:191], v[128:131]\n"
"ds_read_b64_tr_b16 v[104:105], v238 offset:32768\n"
"v_add_f32 v170, v170, v118\n"
"v_mfma_f32_16x16x32_bf16 v[132:135], v[92:95], v[204:207], v[132:135]\n"
"ds_read_b64_tr_b16 v[106:107], v238 offset:36864\n"
"v_exp_f32 v119, v119\n"
"v_mfma_f32_16x16x32_bf16 v[40:43], v[108:111], v[152:155], v[40:43]\n"
"ds_read_b128 v[88:91], v230 offset:36864\n"
"v_add_f32 v171, v171, v119\n"
"v_cvt_pk_bf16_f32 v149, v118, v119\n"
"v_mfma_f32_16x16x32_bf16 v[44:47], v[108:111], v[156:159], v[44:47]\n"
"s_add_u32 m0, s29, 0x4000\n"
"v_exp_f32 v120, v120\n"
"s_waitcnt lgkmcnt(6)\n"
"v_mfma_f32_16x16x32_bf16 v[136:139], v[80:83], v[176:179], v[160:163]\n"
"ds_read_b64_tr_b16 v[108:109], v239 offset:32768\n"
"global_load_lds_dwordx4 v242, s[34:35]\n"
"v_add_f32 v168, v168, v120\n"
"v_exp_f32 v121, v121\n"
"v_mfma_f32_16x16x32_bf16 v[140:143], v[80:83], v[192:195], v[164:167]\n"
"ds_read_b64_tr_b16 v[110:111], v239 offset:36864\n"
"v_add_f32 v169, v169, v121\n"
"v_cvt_pk_bf16_f32 v146, v120, v121\n"
"v_mfma_f32_16x16x32_bf16 v[48:51], v[96:99], v[152:155], v[48:51]\n"
"ds_read_b128 v[92:95], v231 offset:36864\n"
"v_exp_f32 v122, v122\n"
"v_mfma_f32_16x16x32_bf16 v[52:55], v[96:99], v[156:159], v[52:55]\n"
"v_add_f32 v168, v168, v122\n"
"s_waitcnt lgkmcnt(6)\n"
"v_mfma_f32_16x16x32_bf16 v[136:139], v[84:87], v[180:183], v[136:139]\n"
"v_exp_f32 v123, v123\n"
"v_mfma_f32_16x16x32_bf16 v[140:143], v[84:87], v[196:199], v[140:143]\n"
"v_add_f32 v169, v169, v123\n"
"v_cvt_pk_bf16_f32 v147, v122, v123\n"
"v_mfma_f32_16x16x32_bf16 v[56:59], v[100:103], v[152:155], v[56:59]\n"
"v_exp_f32 v124, v124\n"
"v_mfma_f32_16x16x32_bf16 v[60:63], v[100:103], v[156:159], v[60:63]\n"
"s_waitcnt vmcnt(7) lgkmcnt(0)\n"
"s_barrier\n"
"global_load_lds_dwordx4 v243, s[34:35] offset:1024\n"
"s_add_u32 s34, s34, 0x2000\n"
"s_addc_u32 s35, s35, 0\n"
"v_add_f32 v170, v170, v124\n"
"v_mfma_f32_16x16x32_bf16 v[136:139], v[88:91], v[184:187], v[136:139]\n"
"ds_read_b64_tr_b16 v[96:97], v220 offset:0\n"
"v_exp_f32 v125, v125\n"
"v_mfma_f32_16x16x32_bf16 v[140:143], v[88:91], v[200:203], v[140:143]\n"
"ds_read_b64_tr_b16 v[98:99], v220 offset:4096\n"
"v_add_f32 v171, v171, v125\n"
"v_cvt_pk_bf16_f32 v150, v124, v125\n"
"v_mfma_f32_16x16x32_bf16 v[64:67], v[104:107], v[152:155], v[64:67]\n"
"ds_read_b128 v[80:83], v216 offset:0\n"
"v_exp_f32 v126, v126\n"
"v_mfma_f32_16x16x32_bf16 v[68:71], v[104:107], v[156:159], v[68:71]\n"
"ds_read_b64_tr_b16 v[100:101], v221 offset:0\n"
"v_add_f32 v170, v170, v126\n"
"v_mfma_f32_16x16x32_bf16 v[136:139], v[92:95], v[188:191], v[136:139]\n"
"ds_read_b64_tr_b16 v[102:103], v221 offset:4096\n"
"v_exp_f32 v127, v127\n"
"v_mfma_f32_16x16x32_bf16 v[140:143], v[92:95], v[204:207], v[140:143]\n"
"ds_read_b128 v[84:87], v217 offset:0\n"
"v_add_f32 v171, v171, v127\n"
"v_cvt_pk_bf16_f32 v151, v126, v127\n"
"v_mfma_f32_16x16x32_bf16 v[72:75], v[108:111], v[152:155], v[72:75]\n"
"ds_read_b64_tr_b16 v[104:105], v222 offset:0\n"
"v_mfma_f32_16x16x32_bf16 v[76:79], v[108:111], v[156:159], v[76:79]\n"
"ds_read_b64_tr_b16 v[106:107], v222 offset:4096\n"
"ds_read_b128 v[88:91], v218 offset:0\n"
"s_waitcnt lgkmcnt(6)\n"
"v_mfma_f32_16x16x32_bf16 v[112:115], v[80:83], v[176:179], v[160:163]\n"
"ds_read_b64_tr_b16 v[108:109], v223 offset:0\n"
"s_add_u32 m0, s29, 0x8000\n"
"v_exp_f32 v128, v128\n"
"v_mfma_f32_16x16x32_bf16 v[116:119], v[80:83], v[192:195], v[164:167]\n"
"ds_read_b64_tr_b16 v[110:111], v223 offset:4096\n"
"global_load_lds_dwordx4 v240, s[30:31]\n"
"v_add_f32 v168, v168, v128\n"
"v_exp_f32 v129, v129\n"
"v_mfma_f32_16x16x32_bf16 v[16:19], v[96:99], v[144:147], v[16:19]\n"
"ds_read_b128 v[92:95], v219 offset:0\n"
"v_add_f32 v169, v169, v129\n"
"v_cvt_pk_bf16_f32 v152, v128, v129\n"
"v_mfma_f32_16x16x32_bf16 v[20:23], v[96:99], v[148:151], v[20:23]\n"
"v_exp_f32 v130, v130\n"
"s_waitcnt lgkmcnt(6)\n"
"v_mfma_f32_16x16x32_bf16 v[112:115], v[84:87], v[180:183], v[112:115]\n"
"ds_read_b64_tr_b16 v[96:97], v224 offset:0\n"
"v_add_f32 v168, v168, v130\n"
"v_mfma_f32_16x16x32_bf16 v[116:119], v[84:87], v[196:199], v[116:119]\n"
"ds_read_b64_tr_b16 v[98:99], v224 offset:4096\n"
"v_exp_f32 v131, v131\n"
"v_mfma_f32_16x16x32_bf16 v[24:27], v[100:103], v[144:147], v[24:27]\n"
"ds_read_b128 v[80:83], v216 offset:4096\n"
"v_add_f32 v169, v169, v131\n"
"v_cvt_pk_bf16_f32 v153, v130, v131\n"
"v_mfma_f32_16x16x32_bf16 v[28:31], v[100:103], v[148:151], v[28:31]\n"
"v_exp_f32 v132, v132\n"
"s_waitcnt lgkmcnt(6)\n"
"v_mfma_f32_16x16x32_bf16 v[112:115], v[88:91], v[184:187], v[112:115]\n"
"ds_read_b64_tr_b16 v[100:101], v225 offset:0\n"
"global_load_lds_dwordx4 v241, s[30:31] offset:1024\n"
"s_add_u32 s30, s30, 0x2000\n"
"s_addc_u32 s31, s31, 0\n"
"v_add_f32 v170, v170, v132\n"
"v_mfma_f32_16x16x32_bf16 v[116:119], v[88:91], v[200:203], v[116:119]\n"
"ds_read_b64_tr_b16 v[102:103], v225 offset:4096\n"
"s_add_u32 s38, s39, 0xc000\n"
"v_mov_b32 v174, s38\n"
"v_mov_b32 v175, s15\n"
"s_cmp_eq_u32 s16, 0\n"
"s_cselect_b64 exec, 1, 0\n"
"global_store_dword v174, v175, s[4:5] offset:0 sc1\n"
"s_mov_b64 exec, -1\n"
"v_exp_f32 v133, v133\n"
"v_mfma_f32_16x16x32_bf16 v[32:35], v[104:107], v[144:147], v[32:35]\n"
"ds_read_b128 v[84:87], v217 offset:4096\n"
"v_add_f32 v171, v171, v133\n"
"v_cvt_pk_bf16_f32 v156, v132, v133\n"
"v_mfma_f32_16x16x32_bf16 v[36:39], v[104:107], v[148:151], v[36:39]\n"
"v_exp_f32 v134, v134\n"
"s_waitcnt lgkmcnt(6)\n"
"v_mfma_f32_16x16x32_bf16 v[112:115], v[92:95], v[188:191], v[112:115]\n"
"ds_read_b64_tr_b16 v[104:105], v226 offset:0\n"
"v_add_f32 v170, v170, v134\n"
"v_mfma_f32_16x16x32_bf16 v[116:119], v[92:95], v[204:207], v[116:119]\n"
"ds_read_b64_tr_b16 v[106:107], v226 offset:4096\n"
"v_exp_f32 v135, v135\n"
"v_mfma_f32_16x16x32_bf16 v[40:43], v[108:111], v[144:147], v[40:43]\n"
"ds_read_b128 v[88:91], v218 offset:4096\n"
"v_add_f32 v171, v171, v135\n"
"v_cvt_pk_bf16_f32 v157, v134, v135\n"
"v_mfma_f32_16x16x32_bf16 v[44:47], v[108:111], v[148:151], v[44:47]\n"
"s_add_u32 m0, s29, 0xc000\n"
"v_exp_f32 v136, v136\n"
"s_waitcnt lgkmcnt(6)\n"
"v_mfma_f32_16x16x32_bf16 v[120:123], v[80:83], v[176:179], v[160:163]\n"
"ds_read_b64_tr_b16 v[108:109], v227 offset:0\n"
"global_load_lds_dwordx4 v242, s[34:35]\n"
"v_add_f32 v168, v168, v136\n"
"v_exp_f32 v137, v137\n"
"v_mfma_f32_16x16x32_bf16 v[124:127], v[80:83], v[192:195], v[164:167]\n"
"ds_read_b64_tr_b16 v[110:111], v227 offset:4096\n"
"v_add_f32 v169, v169, v137\n"
"v_cvt_pk_bf16_f32 v154, v136, v137\n"
"v_mfma_f32_16x16x32_bf16 v[48:51], v[96:99], v[144:147], v[48:51]\n"
"ds_read_b128 v[92:95], v219 offset:4096\n"
"v_exp_f32 v138, v138\n"
"v_mfma_f32_16x16x32_bf16 v[52:55], v[96:99], v[148:151], v[52:55]\n"
"v_add_f32 v168, v168, v138\n"
"s_waitcnt lgkmcnt(6)\n"
"v_mfma_f32_16x16x32_bf16 v[120:123], v[84:87], v[180:183], v[120:123]\n"
"v_exp_f32 v139, v139\n"
"v_mfma_f32_16x16x32_bf16 v[124:127], v[84:87], v[196:199], v[124:127]\n"
"v_add_f32 v169, v169, v139\n"
"v_cvt_pk_bf16_f32 v155, v138, v139\n"
"v_mfma_f32_16x16x32_bf16 v[56:59], v[100:103], v[144:147], v[56:59]\n"
"v_exp_f32 v140, v140\n"
"v_mfma_f32_16x16x32_bf16 v[60:63], v[100:103], v[148:151], v[60:63]\n"
"s_waitcnt vmcnt(7) lgkmcnt(0)\n"
"s_barrier\n"
"global_load_lds_dwordx4 v243, s[34:35] offset:1024\n"
"s_add_u32 s34, s34, 0x2000\n"
"s_addc_u32 s35, s35, 0\n"
"v_add_f32 v170, v170, v140\n"
"v_mfma_f32_16x16x32_bf16 v[120:123], v[88:91], v[184:187], v[120:123]\n"
"ds_read_b64_tr_b16 v[96:97], v220 offset:32768\n"
"v_exp_f32 v141, v141\n"
"v_mfma_f32_16x16x32_bf16 v[124:127], v[88:91], v[200:203], v[124:127]\n"
"ds_read_b64_tr_b16 v[98:99], v220 offset:36864\n"
"v_add_f32 v171, v171, v141\n"
"v_cvt_pk_bf16_f32 v158, v140, v141\n"
"v_mfma_f32_16x16x32_bf16 v[64:67], v[104:107], v[144:147], v[64:67]\n"
"ds_read_b128 v[80:83], v216 offset:32768\n"
"v_exp_f32 v142, v142\n"
"v_mfma_f32_16x16x32_bf16 v[68:71], v[104:107], v[148:151], v[68:71]\n"
"ds_read_b64_tr_b16 v[100:101], v221 offset:32768\n"
"v_add_f32 v170, v170, v142\n"
"v_mfma_f32_16x16x32_bf16 v[120:123], v[92:95], v[188:191], v[120:123]\n"
"ds_read_b64_tr_b16 v[102:103], v221 offset:36864\n"
"s_sub_u32 s26, s16, 0\ns_cmp_lt_u32 s26, 3\ns_cselect_b64 exec, -1, 0\n"
"global_load_dword v255, v254, s[64:65] offset:0\n"
"s_mov_b64 exec, -1\n"
"v_exp_f32 v143, v143\n"
"v_mfma_f32_16x16x32_bf16 v[124:127], v[92:95], v[204:207], v[124:127]\n"
"ds_read_b128 v[84:87], v217 offset:32768\n"
"v_add_f32 v171, v171, v143\n"
"v_cvt_pk_bf16_f32 v159, v142, v143\n"
"v_mfma_f32_16x16x32_bf16 v[72:75], v[108:111], v[144:147], v[72:75]\n"
"ds_read_b64_tr_b16 v[104:105], v222 offset:32768\n"
"v_mfma_f32_16x16x32_bf16 v[76:79], v[108:111], v[148:151], v[76:79]\n"
"ds_read_b64_tr_b16 v[106:107], v222 offset:36864\n"
"ds_read_b128 v[88:91], v218 offset:32768\n"
"s_waitcnt lgkmcnt(6)\n"
"v_mfma_f32_16x16x32_bf16 v[128:131], v[80:83], v[176:179], v[160:163]\n"
"ds_read_b64_tr_b16 v[108:109], v223 offset:32768\n"
"s_add_u32 m0, s29, 0x18000\n"
"v_exp_f32 v112, v112\n"
"v_mfma_f32_16x16x32_bf16 v[132:135], v[80:83], v[192:195], v[164:167]\n"
"ds_read_b64_tr_b16 v[110:111], v223 offset:36864\n"
"global_load_lds_dwordx4 v240, s[30:31]\n"
"v_add_f32 v168, v168, v112\n"
"v_exp_f32 v113, v113\n"
"v_mfma_f32_16x16x32_bf16 v[16:19], v[96:99], v[152:155], v[16:19]\n"
"ds_read_b128 v[92:95], v219 offset:32768\n"
"v_add_f32 v169, v169, v113\n"
"v_cvt_pk_bf16_f32 v144, v112, v113\n"
"v_mfma_f32_16x16x32_bf16 v[20:23], v[96:99], v[156:159], v[20:23]\n"
"v_exp_f32 v114, v114\n"
"s_waitcnt lgkmcnt(6)\n"
"v_mfma_f32_16x16x32_bf16 v[128:131], v[84:87], v[180:183], v[128:131]\n"
"ds_read_b64_tr_b16 v[96:97], v224 offset:32768\n"
"v_add_f32 v168, v168, v114\n"
"v_mfma_f32_16x16x32_bf16 v[132:135], v[84:87], v[196:199], v[132:135]\n"
"ds_read_b64_tr_b16 v[98:99], v224 offset:36864\n"
"v_exp_f32 v115, v115\n"
"v_mfma_f32_16x16x32_bf16 v[24:27], v[100:103], v[152:155], v[24:27]\n"
"ds_read_b128 v[80:83], v216 offset:36864\n"
"v_add_f32 v169, v169, v115\n"
"v_cvt_pk_bf16_f32 v145, v114, v115\n"
"v_mfma_f32_16x16x32_bf16 v[28:31], v[100:103], v[156:159], v[28:31]\n"
"v_exp_f32 v116, v116\n"
"s_waitcnt lgkmcnt(6)\n"
"v_mfma_f32_16x16x32_bf16 v[128:131], v[88:91], v[184:187], v[128:131]\n"
"ds_read_b64_tr_b16 v[100:101], v225 offset:32768\n"
"global_load_lds_dwordx4 v241, s[30:31] offset:1024\n"
"s_add_u32 s30, s30, 0x2000\n"
"s_addc_u32 s31, s31, 0\n"
"v_add_f32 v170, v170, v116\n"
"v_mfma_f32_16x16x32_bf16 v[132:135], v[88:91], v[200:203], v[132:135]\n"
"ds_read_b64_tr_b16 v[102:103], v225 offset:36864\n"
"v_exp_f32 v117, v117\n"
"v_mfma_f32_16x16x32_bf16 v[32:35], v[104:107], v[152:155], v[32:35]\n"
"ds_read_b128 v[84:87], v217 offset:36864\n"
"v_add_f32 v171, v171, v117\n"
"v_cvt_pk_bf16_f32 v148, v116, v117\n"
"v_mfma_f32_16x16x32_bf16 v[36:39], v[104:107], v[156:159], v[36:39]\n"
"v_exp_f32 v118, v118\n"
"s_waitcnt lgkmcnt(6)\n"
"v_mfma_f32_16x16x32_bf16 v[128:131], v[92:95], v[188:191], v[128:131]\n"
"ds_read_b64_tr_b16 v[104:105], v226 offset:32768\n"
"v_add_f32 v170, v170, v118\n"
"v_mfma_f32_16x16x32_bf16 v[132:135], v[92:95], v[204:207], v[132:135]\n"
"ds_read_b64_tr_b16 v[106:107], v226 offset:36864\n"
"v_exp_f32 v119, v119\n"
"v_mfma_f32_16x16x32_bf16 v[40:43], v[108:111], v[152:155], v[40:43]\n"
"ds_read_b128 v[88:91], v218 offset:36864\n"
"v_add_f32 v171, v171, v119\n"
"v_cvt_pk_bf16_f32 v149, v118, v119\n"
"v_mfma_f32_16x16x32_bf16 v[44:47], v[108:111], v[156:159], v[44:47]\n"
"s_add_u32 m0, s29, 0x1c000\n"
"v_exp_f32 v120, v120\n"
"s_waitcnt lgkmcnt(6)\n"
"v_mfma_f32_16x16x32_bf16 v[136:139], v[80:83], v[176:179], v[160:163]\n"
"ds_read_b64_tr_b16 v[108:109], v227 offset:32768\n"
"global_load_lds_dwordx4 v242, s[34:35]\n"
"v_add_f32 v168, v168, v120\n"
"v_exp_f32 v121, v121\n"
"v_mfma_f32_16x16x32_bf16 v[140:143], v[80:83], v[192:195], v[164:167]\n"
"ds_read_b64_tr_b16 v[110:111], v227 offset:36864\n"
"v_add_f32 v169, v169, v121\n"
"v_cvt_pk_bf16_f32 v146, v120, v121\n"
"v_mfma_f32_16x16x32_bf16 v[48:51], v[96:99], v[152:155], v[48:51]\n"
"ds_read_b128 v[92:95], v219 offset:36864\n"
"v_exp_f32 v122, v122\n"
"v_mfma_f32_16x16x32_bf16 v[52:55], v[96:99], v[156:159], v[52:55]\n"
"v_add_f32 v168, v168, v122\n"
"s_waitcnt lgkmcnt(6)\n"
"v_mfma_f32_16x16x32_bf16 v[136:139], v[84:87], v[180:183], v[136:139]\n"
"v_exp_f32 v123, v123\n"
"v_mfma_f32_16x16x32_bf16 v[140:143], v[84:87], v[196:199], v[140:143]\n"
"v_add_f32 v169, v169, v123\n"
"v_cvt_pk_bf16_f32 v147, v122, v123\n"
"v_mfma_f32_16x16x32_bf16 v[56:59], v[100:103], v[152:155], v[56:59]\n"
"v_exp_f32 v124, v124\n"
"v_mfma_f32_16x16x32_bf16 v[60:63], v[100:103], v[156:159], v[60:63]\n"
"s_waitcnt vmcnt(7) lgkmcnt(0)\n"
"s_barrier\n"
"global_load_lds_dwordx4 v243, s[34:35] offset:1024\n"
"s_add_u32 s34, s34, 0x2000\n"
"s_addc_u32 s35, s35, 0\n"
"v_add_f32 v170, v170, v124\n"
"v_mfma_f32_16x16x32_bf16 v[136:139], v[88:91], v[184:187], v[136:139]\n"
"ds_read_b64_tr_b16 v[96:97], v232 offset:0\n"
"v_exp_f32 v125, v125\n"
"v_mfma_f32_16x16x32_bf16 v[140:143], v[88:91], v[200:203], v[140:143]\n"
"ds_read_b64_tr_b16 v[98:99], v232 offset:4096\n"
"v_add_f32 v171, v171, v125\n"
"v_cvt_pk_bf16_f32 v150, v124, v125\n"
"v_mfma_f32_16x16x32_bf16 v[64:67], v[104:107], v[152:155], v[64:67]\n"
"ds_read_b128 v[80:83], v228 offset:0\n"
"v_exp_f32 v126, v126\n"
"v_mfma_f32_16x16x32_bf16 v[68:71], v[104:107], v[156:159], v[68:71]\n"
"ds_read_b64_tr_b16 v[100:101], v233 offset:0\n"
"v_add_f32 v170, v170, v126\n"
"v_mfma_f32_16x16x32_bf16 v[136:139], v[92:95], v[188:191], v[136:139]\n"
"ds_read_b64_tr_b16 v[102:103], v233 offset:4096\n"
"v_exp_f32 v127, v127\n"
"v_mfma_f32_16x16x32_bf16 v[140:143], v[92:95], v[204:207], v[140:143]\n"
"ds_read_b128 v[84:87], v229 offset:0\n"
"v_add_f32 v171, v171, v127\n"
"v_cvt_pk_bf16_f32 v151, v126, v127\n"
"v_mfma_f32_16x16x32_bf16 v[72:75], v[108:111], v[152:155], v[72:75]\n"
"ds_read_b64_tr_b16 v[104:105], v234 offset:0\n"
"v_mfma_f32_16x16x32_bf16 v[76:79], v[108:111], v[156:159], v[76:79]\n"
"ds_read_b64_tr_b16 v[106:107], v234 offset:4096\n"
"ds_read_b128 v[88:91], v230 offset:0\n"
"s_waitcnt lgkmcnt(6)\n"
"v_mfma_f32_16x16x32_bf16 v[112:115], v[80:83], v[176:179], v[160:163]\n"
"ds_read_b64_tr_b16 v[108:109], v235 offset:0\n"
"s_add_u32 m0, s29, 0x20000\n"
"v_exp_f32 v128, v128\n"
"v_mfma_f32_16x16x32_bf16 v[116:119], v[80:83], v[192:195], v[164:167]\n"
"ds_read_b64_tr_b16 v[110:111], v235 offset:4096\n"
"global_load_lds_dwordx4 v240, s[30:31]\n"
"v_add_f32 v168, v168, v128\n"
"v_exp_f32 v129, v129\n"
"v_mfma_f32_16x16x32_bf16 v[16:19], v[96:99], v[144:147], v[16:19]\n"
"ds_read_b128 v[92:95], v231 offset:0\n"
"v_add_f32 v169, v169, v129\n"
"v_cvt_pk_bf16_f32 v152, v128, v129\n"
"v_mfma_f32_16x16x32_bf16 v[20:23], v[96:99], v[148:151], v[20:23]\n"
"v_exp_f32 v130, v130\n"
"s_waitcnt lgkmcnt(6)\n"
"v_mfma_f32_16x16x32_bf16 v[112:115], v[84:87], v[180:183], v[112:115]\n"
"ds_read_b64_tr_b16 v[96:97], v236 offset:0\n"
"v_add_f32 v168, v168, v130\n"
"v_mfma_f32_16x16x32_bf16 v[116:119], v[84:87], v[196:199], v[116:119]\n"
"ds_read_b64_tr_b16 v[98:99], v236 offset:4096\n"
"v_exp_f32 v131, v131\n"
"v_mfma_f32_16x16x32_bf16 v[24:27], v[100:103], v[144:147], v[24:27]\n"
"ds_read_b128 v[80:83], v228 offset:4096\n"
"v_add_f32 v169, v169, v131\n"
"v_cvt_pk_bf16_f32 v153, v130, v131\n"
"v_mfma_f32_16x16x32_bf16 v[28:31], v[100:103], v[148:151], v[28:31]\n"
"v_exp_f32 v132, v132\n"
"s_waitcnt lgkmcnt(6)\n"
"v_mfma_f32_16x16x32_bf16 v[112:115], v[88:91], v[184:187], v[112:115]\n"
"ds_read_b64_tr_b16 v[100:101], v237 offset:0\n"
"global_load_lds_dwordx4 v241, s[30:31] offset:1024\n"
"s_add_u32 s30, s30, 0x2000\n"
"s_addc_u32 s31, s31, 0\n"
"v_add_f32 v170, v170, v132\n"
"v_mfma_f32_16x16x32_bf16 v[116:119], v[88:91], v[200:203], v[116:119]\n"
"ds_read_b64_tr_b16 v[102:103], v237 offset:4096\n"
"v_exp_f32 v133, v133\n"
"v_mfma_f32_16x16x32_bf16 v[32:35], v[104:107], v[144:147], v[32:35]\n"
"ds_read_b128 v[84:87], v229 offset:4096\n"
"v_add_f32 v171, v171, v133\n"
"v_cvt_pk_bf16_f32 v156, v132, v133\n"
"v_mfma_f32_16x16x32_bf16 v[36:39], v[104:107], v[148:151], v[36:39]\n"
"v_exp_f32 v134, v134\n"
"s_waitcnt lgkmcnt(6)\n"
"v_mfma_f32_16x16x32_bf16 v[112:115], v[92:95], v[188:191], v[112:115]\n"
"ds_read_b64_tr_b16 v[104:105], v238 offset:0\n"
"v_add_f32 v170, v170, v134\n"
"v_mfma_f32_16x16x32_bf16 v[116:119], v[92:95], v[204:207], v[116:119]\n"
"ds_read_b64_tr_b16 v[106:107], v238 offset:4096\n"
"v_exp_f32 v135, v135\n"
"v_mfma_f32_16x16x32_bf16 v[40:43], v[108:111], v[144:147], v[40:43]\n"
"ds_read_b128 v[88:91], v230 offset:4096\n"
"v_add_f32 v171, v171, v135\n"
"v_cvt_pk_bf16_f32 v157, v134, v135\n"
"v_mfma_f32_16x16x32_bf16 v[44:47], v[108:111], v[148:151], v[44:47]\n"
"s_add_u32 m0, s29, 0x24000\n"
"v_exp_f32 v136, v136\n"
"s_waitcnt lgkmcnt(6)\n"
"v_mfma_f32_16x16x32_bf16 v[120:123], v[80:83], v[176:179], v[160:163]\n"
"ds_read_b64_tr_b16 v[108:109], v239 offset:0\n"
"global_load_lds_dwordx4 v242, s[34:35]\n"
"v_add_f32 v168, v168, v136\n"
"v_exp_f32 v137, v137\n"
"v_mfma_f32_16x16x32_bf16 v[124:127], v[80:83], v[192:195], v[164:167]\n"
"ds_read_b64_tr_b16 v[110:111], v239 offset:4096\n"
"v_add_f32 v169, v169, v137\n"
"v_cvt_pk_bf16_f32 v154, v136, v137\n"
"v_mfma_f32_16x16x32_bf16 v[48:51], v[96:99], v[144:147], v[48:51]\n"
"ds_read_b128 v[92:95], v231 offset:4096\n"
"v_exp_f32 v138, v138\n"
"v_mfma_f32_16x16x32_bf16 v[52:55], v[96:99], v[148:151], v[52:55]\n"
"v_add_f32 v168, v168, v138\n"
"s_waitcnt lgkmcnt(6)\n"
"v_mfma_f32_16x16x32_bf16 v[120:123], v[84:87], v[180:183], v[120:123]\n"
"v_exp_f32 v139, v139\n"
"v_mfma_f32_16x16x32_bf16 v[124:127], v[84:87], v[196:199], v[124:127]\n"
"v_add_f32 v169, v169, v139\n"
"v_cvt_pk_bf16_f32 v155, v138, v139\n"
"v_mfma_f32_16x16x32_bf16 v[56:59], v[100:103], v[144:147], v[56:59]\n"
"v_exp_f32 v140, v140\n"
"v_mfma_f32_16x16x32_bf16 v[60:63], v[100:103], v[148:151], v[60:63]\n"
"s_waitcnt vmcnt(7) lgkmcnt(0)\n"
"s_barrier\n"
"global_load_lds_dwordx4 v243, s[34:35] offset:1024\n"
"s_add_u32 s34, s34, 0x2000\n"
"s_addc_u32 s35, s35, 0\n"
"v_add_f32 v170, v170, v140\n"
"v_mfma_f32_16x16x32_bf16 v[120:123], v[88:91], v[184:187], v[120:123]\n"
"ds_read_b64_tr_b16 v[96:97], v232 offset:32768\n"
"v_exp_f32 v141, v141\n"
"v_mfma_f32_16x16x32_bf16 v[124:127], v[88:91], v[200:203], v[124:127]\n"
"ds_read_b64_tr_b16 v[98:99], v232 offset:36864\n"
"v_add_f32 v171, v171, v141\n"
"v_cvt_pk_bf16_f32 v158, v140, v141\n"
"v_mfma_f32_16x16x32_bf16 v[64:67], v[104:107], v[144:147], v[64:67]\n"
"ds_read_b128 v[80:83], v228 offset:32768\n"
"v_exp_f32 v142, v142\n"
"v_mfma_f32_16x16x32_bf16 v[68:71], v[104:107], v[148:151], v[68:71]\n"
"ds_read_b64_tr_b16 v[100:101], v233 offset:32768\n"
"v_add_f32 v170, v170, v142\n"
"v_mfma_f32_16x16x32_bf16 v[120:123], v[92:95], v[188:191], v[120:123]\n"
"ds_read_b64_tr_b16 v[102:103], v233 offset:36864\n"
"s_sub_u32 s26, s16, 3\ns_cmp_lt_u32 s26, 3\ns_cselect_b64 exec, -1, 0\n"
"global_load_dword v255, v254, s[66:67] offset:0\n"
"s_mov_b64 exec, -1\n"
"v_exp_f32 v143, v143\n"
"v_mfma_f32_16x16x32_bf16 v[124:127], v[92:95], v[204:207], v[124:127]\n"
"ds_read_b128 v[84:87], v229 offset:32768\n"
"v_add_f32 v171, v171, v143\n"
"v_cvt_pk_bf16_f32 v159, v142, v143\n"
"v_mfma_f32_16x16x32_bf16 v[72:75], v[108:111], v[144:147], v[72:75]\n"
"ds_read_b64_tr_b16 v[104:105], v234 offset:32768\n"
"v_mfma_f32_16x16x32_bf16 v[76:79], v[108:111], v[148:151], v[76:79]\n"
"ds_read_b64_tr_b16 v[106:107], v234 offset:36864\n"
"ds_read_b128 v[88:91], v230 offset:32768\n"
"s_waitcnt lgkmcnt(6)\n"
"v_mfma_f32_16x16x32_bf16 v[128:131], v[80:83], v[176:179], v[160:163]\n"
"ds_read_b64_tr_b16 v[108:109], v235 offset:32768\n"
"s_add_u32 m0, s29, 0x4000\n"
"v_exp_f32 v112, v112\n"
"v_mfma_f32_16x16x32_bf16 v[132:135], v[80:83], v[192:195], v[164:167]\n"
"ds_read_b64_tr_b16 v[110:111], v235 offset:36864\n"
"global_load_lds_dwordx4 v242, s[34:35]\n"
"v_add_f32 v168, v168, v112\n"
"v_exp_f32 v113, v113\n"
"v_mfma_f32_16x16x32_bf16 v[16:19], v[96:99], v[152:155], v[16:19]\n"
"ds_read_b128 v[92:95], v231 offset:32768\n"
"v_add_f32 v169, v169, v113\n"
"v_cvt_pk_bf16_f32 v144, v112, v113\n"
"v_mfma_f32_16x16x32_bf16 v[20:23], v[96:99], v[156:159], v[20:23]\n"
"v_exp_f32 v114, v114\n"
"s_waitcnt lgkmcnt(6)\n"
"v_mfma_f32_16x16x32_bf16 v[128:131], v[84:87], v[180:183], v[128:131]\n"
"ds_read_b64_tr_b16 v[96:97], v236 offset:32768\n"
"v_add_f32 v168, v168, v114\n"
"v_mfma_f32_16x16x32_bf16 v[132:135], v[84:87], v[196:199], v[132:135]\n"
"ds_read_b64_tr_b16 v[98:99], v236 offset:36864\n"
"v_exp_f32 v115, v115\n"
"v_mfma_f32_16x16x32_bf16 v[24:27], v[100:103], v[152:155], v[24:27]\n"
"ds_read_b128 v[80:83], v228 offset:36864\n"
"v_add_f32 v169, v169, v115\n"
"v_cvt_pk_bf16_f32 v145, v114, v115\n"
"v_mfma_f32_16x16x32_bf16 v[28:31], v[100:103], v[156:159], v[28:31]\n"
"v_exp_f32 v116, v116\n"
"s_waitcnt lgkmcnt(6)\n"
"v_mfma_f32_16x16x32_bf16 v[128:131], v[88:91], v[184:187], v[128:131]\n"
"ds_read_b64_tr_b16 v[100:101], v237 offset:32768\n"
"v_add_f32 v170, v170, v116\n"
"v_mfma_f32_16x16x32_bf16 v[132:135], v[88:91], v[200:203], v[132:135]\n"
"ds_read_b64_tr_b16 v[102:103], v237 offset:36864\n"
"v_exp_f32 v117, v117\n"
"v_mfma_f32_16x16x32_bf16 v[32:35], v[104:107], v[152:155], v[32:35]\n"
"ds_read_b128 v[84:87], v229 offset:36864\n"
"v_add_f32 v171, v171, v117\n"
"v_cvt_pk_bf16_f32 v148, v116, v117\n"
"v_mfma_f32_16x16x32_bf16 v[36:39], v[104:107], v[156:159], v[36:39]\n"
"v_exp_f32 v118, v118\n"
"s_waitcnt lgkmcnt(6)\n"
"v_mfma_f32_16x16x32_bf16 v[128:131], v[92:95], v[188:191], v[128:131]\n"
"ds_read_b64_tr_b16 v[104:105], v238 offset:32768\n"
"v_add_f32 v170, v170, v118\n"
"v_mfma_f32_16x16x32_bf16 v[132:135], v[92:95], v[204:207], v[132:135]\n"
"ds_read_b64_tr_b16 v[106:107], v238 offset:36864\n"
"v_exp_f32 v119, v119\n"
"v_mfma_f32_16x16x32_bf16 v[40:43], v[108:111], v[152:155], v[40:43]\n"
"ds_read_b128 v[88:91], v230 offset:36864\n"
"v_add_f32 v171, v171, v119\n"
"v_cvt_pk_bf16_f32 v149, v118, v119\n"
"v_mfma_f32_16x16x32_bf16 v[44:47], v[108:111], v[156:159], v[44:47]\n"
"v_exp_f32 v120, v120\n"
"s_waitcnt lgkmcnt(6)\n"
"v_mfma_f32_16x16x32_bf16 v[136:139], v[80:83], v[176:179], v[160:163]\n"
"ds_read_b64_tr_b16 v[108:109], v239 offset:32768\n"
"global_load_lds_dwordx4 v243, s[34:35] offset:1024\n"
"s_add_u32 s34, s34, 0x2000\n"
"s_addc_u32 s35, s35, 0\n"
"v_add_f32 v168, v168, v120\n"
"v_exp_f32 v121, v121\n"
"v_mfma_f32_16x16x32_bf16 v[140:143], v[80:83], v[192:195], v[164:167]\n"
"ds_read_b64_tr_b16 v[110:111], v239 offset:36864\n"
"v_add_f32 v169, v169, v121\n"
"v_cvt_pk_bf16_f32 v146, v120, v121\n"
"v_mfma_f32_16x16x32_bf16 v[48:51], v[96:99], v[152:155], v[48:51]\n"
"ds_read_b128 v[92:95], v231 offset:36864\n"
"v_exp_f32 v122, v122\n"
"v_mfma_f32_16x16x32_bf16 v[52:55], v[96:99], v[156:159], v[52:55]\n"
"v_add_f32 v168, v168, v122\n"
"s_waitcnt lgkmcnt(6)\n"
"v_mfma_f32_16x16x32_bf16 v[136:139], v[84:87], v[180:183], v[136:139]\n"
"v_exp_f32 v123, v123\n"
"v_mfma_f32_16x16x32_bf16 v[140:143], v[84:87], v[196:199], v[140:143]\n"
"v_add_f32 v169, v169, v123\n"
"v_cvt_pk_bf16_f32 v147, v122, v123\n"
"v_mfma_f32_16x16x32_bf16 v[56:59], v[100:103], v[152:155], v[56:59]\n"
"v_exp_f32 v124, v124\n"
"v_mfma_f32_16x16x32_bf16 v[60:63], v[100:103], v[156:159], v[60:63]\n"
"s_waitcnt vmcnt(6) lgkmcnt(0)\n"
"s_barrier\n"
"v_add_f32 v170, v170, v124\n"
"v_mfma_f32_16x16x32_bf16 v[136:139], v[88:91], v[184:187], v[136:139]\n"
"ds_read_b64_tr_b16 v[96:97], v220 offset:0\n"
"v_exp_f32 v125, v125\n"
"v_mfma_f32_16x16x32_bf16 v[140:143], v[88:91], v[200:203], v[140:143]\n"
"ds_read_b64_tr_b16 v[98:99], v220 offset:4096\n"
"v_add_f32 v171, v171, v125\n"
"v_cvt_pk_bf16_f32 v150, v124, v125\n"
"v_mfma_f32_16x16x32_bf16 v[64:67], v[104:107], v[152:155], v[64:67]\n"
"ds_read_b128 v[80:83], v216 offset:0\n"
"v_exp_f32 v126, v126\n"
"v_mfma_f32_16x16x32_bf16 v[68:71], v[104:107], v[156:159], v[68:71]\n"
"ds_read_b64_tr_b16 v[100:101], v221 offset:0\n"
"v_add_f32 v170, v170, v126\n"
"v_mfma_f32_16x16x32_bf16 v[136:139], v[92:95], v[188:191], v[136:139]\n"
"ds_read_b64_tr_b16 v[102:103], v221 offset:4096\n"
"v_exp_f32 v127, v127\n"
"v_mfma_f32_16x16x32_bf16 v[140:143], v[92:95], v[204:207], v[140:143]\n"
"ds_read_b128 v[84:87], v217 offset:0\n"
"v_add_f32 v171, v171, v127\n"
"v_cvt_pk_bf16_f32 v151, v126, v127\n"
"v_mfma_f32_16x16x32_bf16 v[72:75], v[108:111], v[152:155], v[72:75]\n"
"ds_read_b64_tr_b16 v[104:105], v222 offset:0\n"
"v_mfma_f32_16x16x32_bf16 v[76:79], v[108:111], v[156:159], v[76:79]\n"
"ds_read_b64_tr_b16 v[106:107], v222 offset:4096\n"
"ds_read_b128 v[88:91], v218 offset:0\n"
"s_waitcnt lgkmcnt(6)\n"
"v_mfma_f32_16x16x32_bf16 v[112:115], v[80:83], v[176:179], v[160:163]\n"
"ds_read_b64_tr_b16 v[108:109], v223 offset:0\n"
"s_add_u32 m0, s29, 0xc000\n"
"v_exp_f32 v128, v128\n"
"v_mfma_f32_16x16x32_bf16 v[116:119], v[80:83], v[192:195], v[164:167]\n"
"ds_read_b64_tr_b16 v[110:111], v223 offset:4096\n"
"global_load_lds_dwordx4 v242, s[34:35]\n"
"v_add_f32 v168, v168, v128\n"
"v_exp_f32 v129, v129\n"
"v_mfma_f32_16x16x32_bf16 v[16:19], v[96:99], v[144:147], v[16:19]\n"
"ds_read_b128 v[92:95], v219 offset:0\n"
"v_add_f32 v169, v169, v129\n"
"v_cvt_pk_bf16_f32 v152, v128, v129\n"
"v_mfma_f32_16x16x32_bf16 v[20:23], v[96:99], v[148:151], v[20:23]\n"
"v_exp_f32 v130, v130\n"
"s_waitcnt lgkmcnt(6)\n"
"v_mfma_f32_16x16x32_bf16 v[112:115], v[84:87], v[180:183], v[112:115]\n"
"ds_read_b64_tr_b16 v[96:97], v224 offset:0\n"
"v_add_f32 v168, v168, v130\n"
"v_mfma_f32_16x16x32_bf16 v[116:119], v[84:87], v[196:199], v[116:119]\n"
"ds_read_b64_tr_b16 v[98:99], v224 offset:4096\n"
"v_exp_f32 v131, v131\n"
"v_mfma_f32_16x16x32_bf16 v[24:27], v[100:103], v[144:147], v[24:27]\n"
"ds_read_b128 v[80:83], v216 offset:4096\n"
"v_add_f32 v169, v169, v131\n"
"v_cvt_pk_bf16_f32 v153, v130, v131\n"
"v_mfma_f32_16x16x32_bf16 v[28:31], v[100:103], v[148:151], v[28:31]\n"
"v_exp_f32 v132, v132\n"
"s_waitcnt lgkmcnt(6)\n"
"v_mfma_f32_16x16x32_bf16 v[112:115], v[88:91], v[184:187], v[112:115]\n"
"ds_read_b64_tr_b16 v[100:101], v225 offset:0\n"
"v_add_f32 v170, v170, v132\n"
"v_mfma_f32_16x16x32_bf16 v[116:119], v[88:91], v[200:203], v[116:119]\n"
"ds_read_b64_tr_b16 v[102:103], v225 offset:4096\n"
"v_exp_f32 v133, v133\n"
"v_mfma_f32_16x16x32_bf16 v[32:35], v[104:107], v[144:147], v[32:35]\n"
"ds_read_b128 v[84:87], v217 offset:4096\n"
"v_add_f32 v171, v171, v133\n"
"v_cvt_pk_bf16_f32 v156, v132, v133\n"
"v_mfma_f32_16x16x32_bf16 v[36:39], v[104:107], v[148:151], v[36:39]\n"
"v_exp_f32 v134, v134\n"
"s_waitcnt lgkmcnt(6)\n"
"v_mfma_f32_16x16x32_bf16 v[112:115], v[92:95], v[188:191], v[112:115]\n"
"ds_read_b64_tr_b16 v[104:105], v226 offset:0\n"
"v_add_f32 v170, v170, v134\n"
"v_mfma_f32_16x16x32_bf16 v[116:119], v[92:95], v[204:207], v[116:119]\n"
"ds_read_b64_tr_b16 v[106:107], v226 offset:4096\n"
"v_exp_f32 v135, v135\n"
"v_mfma_f32_16x16x32_bf16 v[40:43], v[108:111], v[144:147], v[40:43]\n"
"ds_read_b128 v[88:91], v218 offset:4096\n"
"v_add_f32 v171, v171, v135\n"
"v_cvt_pk_bf16_f32 v157, v134, v135\n"
"v_mfma_f32_16x16x32_bf16 v[44:47], v[108:111], v[148:151], v[44:47]\n"
"v_exp_f32 v136, v136\n"
"s_waitcnt lgkmcnt(6)\n"
"v_mfma_f32_16x16x32_bf16 v[120:123], v[80:83], v[176:179], v[160:163]\n"
"ds_read_b64_tr_b16 v[108:109], v227 offset:0\n"
"global_load_lds_dwordx4 v243, s[34:35] offset:1024\n"
"s_add_u32 s34, s34, 0x2000\n"
"s_addc_u32 s35, s35, 0\n"
"v_add_f32 v168, v168, v136\n"
"v_exp_f32 v137, v137\n"
"v_mfma_f32_16x16x32_bf16 v[124:127], v[80:83], v[192:195], v[164:167]\n"
"ds_read_b64_tr_b16 v[110:111], v227 offset:4096\n"
"v_add_f32 v169, v169, v137\n"
"v_cvt_pk_bf16_f32 v154, v136, v137\n"
"v_mfma_f32_16x16x32_bf16 v[48:51], v[96:99], v[144:147], v[48:51]\n"
"ds_read_b128 v[92:95], v219 offset:4096\n"
"v_exp_f32 v138, v138\n"
"v_mfma_f32_16x16x32_bf16 v[52:55], v[96:99], v[148:151], v[52:55]\n"
"v_add_f32 v168, v168, v138\n"
"s_waitcnt lgkmcnt(6)\n"
"v_mfma_f32_16x16x32_bf16 v[120:123], v[84:87], v[180:183], v[120:123]\n"
"v_exp_f32 v139, v139\n"
"v_mfma_f32_16x16x32_bf16 v[124:127], v[84:87], v[196:199], v[124:127]\n"
"v_add_f32 v169, v169, v139\n"
"v_cvt_pk_bf16_f32 v155, v138, v139\n"
"v_mfma_f32_16x16x32_bf16 v[56:59], v[100:103], v[144:147], v[56:59]\n"
"v_exp_f32 v140, v140\n"
"v_mfma_f32_16x16x32_bf16 v[60:63], v[100:103], v[148:151], v[60:63]\n"
"s_waitcnt vmcnt(4) lgkmcnt(0)\n"
"s_barrier\n"
"v_add_f32 v170, v170, v140\n"
"v_mfma_f32_16x16x32_bf16 v[120:123], v[88:91], v[184:187], v[120:123]\n"
"ds_read_b64_tr_b16 v[96:97], v220 offset:32768\n"
"v_mov_b32 v213, 0\n"
"ds_write_b32 v213, v213 offset:0\n"
"v_exp_f32 v141, v141\n"
"v_mfma_f32_16x16x32_bf16 v[124:127], v[88:91], v[200:203], v[124:127]\n"
"ds_read_b64_tr_b16 v[98:99], v220 offset:36864\n"
"v_add_f32 v171, v171, v141\n"
"v_cvt_pk_bf16_f32 v158, v140, v141\n"
"v_mfma_f32_16x16x32_bf16 v[64:67], v[104:107], v[144:147], v[64:67]\n"
"ds_read_b128 v[80:83], v216 offset:32768\n"
"v_exp_f32 v142, v142\n"
"v_mfma_f32_16x16x32_bf16 v[68:71], v[104:107], v[148:151], v[68:71]\n"
"ds_read_b64_tr_b16 v[100:101], v221 offset:32768\n"
"v_add_f32 v170, v170, v142\n"
"v_mfma_f32_16x16x32_bf16 v[120:123], v[92:95], v[188:191], v[120:123]\n"
"ds_read_b64_tr_b16 v[102:103], v221 offset:36864\n"
"v_exp_f32 v143, v143\n"
"v_mfma_f32_16x16x32_bf16 v[124:127], v[92:95], v[204:207], v[124:127]\n"
"ds_read_b128 v[84:87], v217 offset:32768\n"
"v_add_f32 v171, v171, v143\n"
"v_cvt_pk_bf16_f32 v159, v142, v143\n"
"v_mfma_f32_16x16x32_bf16 v[72:75], v[108:111], v[144:147], v[72:75]\n"
"ds_read_b64_tr_b16 v[104:105], v222 offset:32768\n"
"v_mfma_f32_16x16x32_bf16 v[76:79], v[108:111], v[148:151], v[76:79]\n"
"ds_read_b64_tr_b16 v[106:107], v222 offset:36864\n"
"ds_read_b128 v[88:91], v218 offset:32768\n"
"s_waitcnt lgkmcnt(6)\n"
"v_mfma_f32_16x16x32_bf16 v[128:131], v[80:83], v[176:179], v[160:163]\n"
"ds_read_b64_tr_b16 v[108:109], v223 offset:32768\n"
"s_add_u32 s48, s50, 0\n"
"s_addc_u32 s49, s51, 0\n"
"s_add_u32 m0, s44, 0x10000\n"
"v_exp_f32 v112, v112\n"
"v_mfma_f32_16x16x32_bf16 v[132:135], v[80:83], v[192:195], v[164:167]\n"
"ds_read_b64_tr_b16 v[110:111], v223 offset:36864\n"
"global_load_lds_dwordx4 v6, s[48:49]\n"
"v_add_f32 v168, v168, v112\n"
"v_exp_f32 v113, v113\n"
"v_mfma_f32_16x16x32_bf16 v[16:19], v[96:99], v[152:155], v[16:19]\n"
"ds_read_b128 v[92:95], v219 offset:32768\n"
"v_add_f32 v169, v169, v113\n"
"v_cvt_pk_bf16_f32 v144, v112, v113\n"
"v_mfma_f32_16x16x32_bf16 v[20:23], v[96:99], v[156:159], v[20:23]\n"
"v_exp_f32 v114, v114\n"
"s_waitcnt lgkmcnt(6)\n"
"v_mfma_f32_16x16x32_bf16 v[128:131], v[84:87], v[180:183], v[128:131]\n"
"ds_read_b64_tr_b16 v[96:97], v224 offset:32768\n"
"v_add_f32 v168, v168, v114\n"
"v_mfma_f32_16x16x32_bf16 v[132:135], v[84:87], v[196:199], v[132:135]\n"
"ds_read_b64_tr_b16 v[98:99], v224 offset:36864\n"
"v_exp_f32 v115, v115\n"
"v_mfma_f32_16x16x32_bf16 v[24:27], v[100:103], v[152:155], v[24:27]\n"
"ds_read_b128 v[80:83], v216 offset:36864\n"
"global_load_lds_dwordx4 v7, s[48:49] offset:1024\n"
"v_add_f32 v169, v169, v115\n"
"v_cvt_pk_bf16_f32 v145, v114, v115\n"
"v_mfma_f32_16x16x32_bf16 v[28:31], v[100:103], v[156:159], v[28:31]\n"
"v_exp_f32 v116, v116\n"
"s_waitcnt lgkmcnt(6)\n"
"v_mfma_f32_16x16x32_bf16 v[128:131], v[88:91], v[184:187], v[128:131]\n"
"ds_read_b64_tr_b16 v[100:101], v225 offset:32768\n"
"v_add_f32 v170, v170, v116\n"
"v_mfma_f32_16x16x32_bf16 v[132:135], v[88:91], v[200:203], v[132:135]\n"
"ds_read_b64_tr_b16 v[102:103], v225 offset:36864\n"
"v_exp_f32 v117, v117\n"
"v_mfma_f32_16x16x32_bf16 v[32:35], v[104:107], v[152:155], v[32:35]\n"
"ds_read_b128 v[84:87], v217 offset:36864\n"
"v_add_f32 v171, v171, v117\n"
"v_cvt_pk_bf16_f32 v148, v116, v117\n"
"v_mfma_f32_16x16x32_bf16 v[36:39], v[104:107], v[156:159], v[36:39]\n"
"global_load_lds_dwordx4 v8, s[48:49] offset:2048\n"
"v_exp_f32 v118, v118\n"
"s_waitcnt lgkmcnt(6)\n"
"v_mfma_f32_16x16x32_bf16 v[128:131], v[92:95], v[188:191], v[128:131]\n"
"ds_read_b64_tr_b16 v[104:105], v226 offset:32768\n"
"v_add_f32 v170, v170, v118\n"
"v_mfma_f32_16x16x32_bf16 v[132:135], v[92:95], v[204:207], v[132:135]\n"
"ds_read_b64_tr_b16 v[106:107], v226 offset:36864\n"
"v_exp_f32 v119, v119\n"
"v_mfma_f32_16x16x32_bf16 v[40:43], v[108:111], v[152:155], v[40:43]\n"
"ds_read_b128 v[88:91], v218 offset:36864\n"
"v_add_f32 v171, v171, v119\n"
"v_cvt_pk_bf16_f32 v149, v118, v119\n"
"v_mfma_f32_16x16x32_bf16 v[44:47], v[108:111], v[156:159], v[44:47]\n"
"s_add_u32 m0, s44, 0x10c00\n"
"v_exp_f32 v120, v120\n"
"s_waitcnt lgkmcnt(6)\n"
"v_mfma_f32_16x16x32_bf16 v[136:139], v[80:83], v[176:179], v[160:163]\n"
"ds_read_b64_tr_b16 v[108:109], v227 offset:32768\n"
"global_load_lds_dwordx4 v9, s[48:49]\n"
"v_add_f32 v168, v168, v120\n"
"v_exp_f32 v121, v121\n"
"v_mfma_f32_16x16x32_bf16 v[140:143], v[80:83], v[192:195], v[164:167]\n"
"ds_read_b64_tr_b16 v[110:111], v227 offset:36864\n"
"v_add_f32 v169, v169, v121\n"
"v_cvt_pk_bf16_f32 v146, v120, v121\n"
"v_mfma_f32_16x16x32_bf16 v[48:51], v[96:99], v[152:155], v[48:51]\n"
"ds_read_b128 v[92:95], v219 offset:36864\n"
"v_exp_f32 v122, v122\n"
"v_mfma_f32_16x16x32_bf16 v[52:55], v[96:99], v[156:159], v[52:55]\n"
"v_add_f32 v168, v168, v122\n"
"s_waitcnt lgkmcnt(6)\n"
"v_mfma_f32_16x16x32_bf16 v[136:139], v[84:87], v[180:183], v[136:139]\n"
"v_exp_f32 v123, v123\n"
"v_mfma_f32_16x16x32_bf16 v[140:143], v[84:87], v[196:199], v[140:143]\n"
"global_load_lds_dwordx4 v10, s[48:49] offset:1024\n"
"v_add_f32 v169, v169, v123\n"
"v_cvt_pk_bf16_f32 v147, v122, v123\n"
"v_mfma_f32_16x16x32_bf16 v[56:59], v[100:103], v[152:155], v[56:59]\n"
"v_exp_f32 v124, v124\n"
"v_mfma_f32_16x16x32_bf16 v[60:63], v[100:103], v[156:159], v[60:63]\n"
"s_waitcnt vmcnt(7) lgkmcnt(0)\n"
"s_barrier\n"
"v_add_f32 v170, v170, v124\n"
"v_mfma_f32_16x16x32_bf16 v[136:139], v[88:91], v[184:187], v[136:139]\n"
"ds_read_b64_tr_b16 v[96:97], v232 offset:0\n"
"v_exp_f32 v125, v125\n"
"v_mfma_f32_16x16x32_bf16 v[140:143], v[88:91], v[200:203], v[140:143]\n"
"ds_read_b64_tr_b16 v[98:99], v232 offset:4096\n"
"v_add_f32 v171, v171, v125\n"
"v_cvt_pk_bf16_f32 v150, v124, v125\n"
"v_mfma_f32_16x16x32_bf16 v[64:67], v[104:107], v[152:155], v[64:67]\n"
"global_load_lds_dwordx4 v11, s[48:49] offset:2048\n"
"v_exp_f32 v126, v126\n"
"v_mfma_f32_16x16x32_bf16 v[68:71], v[104:107], v[156:159], v[68:71]\n"
"ds_read_b64_tr_b16 v[100:101], v233 offset:0\n"
"v_add_f32 v170, v170, v126\n"
"v_mfma_f32_16x16x32_bf16 v[136:139], v[92:95], v[188:191], v[136:139]\n"
"ds_read_b64_tr_b16 v[102:103], v233 offset:4096\n"
"v_exp_f32 v127, v127\n"
"v_mfma_f32_16x16x32_bf16 v[140:143], v[92:95], v[204:207], v[140:143]\n"
"v_add_f32 v171, v171, v127\n"
"v_cvt_pk_bf16_f32 v151, v126, v127\n"
"v_mfma_f32_16x16x32_bf16 v[72:75], v[108:111], v[152:155], v[72:75]\n"
"ds_read_b64_tr_b16 v[104:105], v234 offset:0\n"
"v_mfma_f32_16x16x32_bf16 v[76:79], v[108:111], v[156:159], v[76:79]\n"
"ds_read_b64_tr_b16 v[106:107], v234 offset:4096\n"
"s_waitcnt lgkmcnt(4)\n"
"v_mfma_f32_16x16x32_bf16 v[16:19], v[96:99], v[144:147], v[16:19]\n"
"ds_read_b64_tr_b16 v[108:109], v235 offset:0\n"
"s_add_u32 s48, s50, 0x24000\n"
"s_addc_u32 s49, s51, 0\n"
"s_add_u32 m0, s44, 0x1c000\n"
"v_exp_f32 v128, v128\n"
"v_exp_f32 v129, v129\n"
"v_mfma_f32_16x16x32_bf16 v[20:23], v[96:99], v[148:151], v[20:23]\n"
"ds_read_b64_tr_b16 v[110:111], v235 offset:4096\n"
"global_load_lds_dwordx4 v6, s[48:49]\n"
"v_add_f32 v168, v168, v128\n"
"v_add_f32 v169, v169, v129\n"
"v_cvt_pk_bf16_f32 v152, v128, v129\n"
"v_exp_f32 v130, v130\n"
"s_waitcnt lgkmcnt(4)\n"
"v_mfma_f32_16x16x32_bf16 v[24:27], v[100:103], v[144:147], v[24:27]\n"
"ds_read_b64_tr_b16 v[96:97], v236 offset:0\n"
"v_add_f32 v168, v168, v130\n"
"v_exp_f32 v131, v131\n"
"v_mfma_f32_16x16x32_bf16 v[28:31], v[100:103], v[148:151], v[28:31]\n"
"ds_read_b64_tr_b16 v[98:99], v236 offset:4096\n"
"global_load_lds_dwordx4 v7, s[48:49] offset:1024\n"
"v_add_f32 v169, v169, v131\n"
"v_cvt_pk_bf16_f32 v153, v130, v131\n"
"v_exp_f32 v132, v132\n"
"s_waitcnt lgkmcnt(4)\n"
"v_mfma_f32_16x16x32_bf16 v[32:35], v[104:107], v[144:147], v[32:35]\n"
"ds_read_b64_tr_b16 v[100:101], v237 offset:0\n"
"v_add_f32 v170, v170, v132\n"
"v_exp_f32 v133, v133\n"
"v_mfma_f32_16x16x32_bf16 v[36:39], v[104:107], v[148:151], v[36:39]\n"
"ds_read_b64_tr_b16 v[102:103], v237 offset:4096\n"
"global_load_lds_dwordx4 v8, s[48:49] offset:2048\n"
"v_add_f32 v171, v171, v133\n"
"v_cvt_pk_bf16_f32 v156, v132, v133\n"
"v_exp_f32 v134, v134\n"
"s_waitcnt lgkmcnt(4)\n"
"v_mfma_f32_16x16x32_bf16 v[40:43], v[108:111], v[144:147], v[40:43]\n"
"ds_read_b64_tr_b16 v[104:105], v238 offset:0\n"
"v_add_f32 v170, v170, v134\n"
"v_exp_f32 v135, v135\n"
"v_mfma_f32_16x16x32_bf16 v[44:47], v[108:111], v[148:151], v[44:47]\n"
"ds_read_b64_tr_b16 v[106:107], v238 offset:4096\n"
"s_add_u32 m0, s44, 0x1cc00\n"
"v_add_f32 v171, v171, v135\n"
"v_cvt_pk_bf16_f32 v157, v134, v135\n"
"v_exp_f32 v136, v136\n"
"v_exp_f32 v137, v137\n"
"s_waitcnt lgkmcnt(4)\n"
"v_mfma_f32_16x16x32_bf16 v[48:51], v[96:99], v[144:147], v[48:51]\n"
"ds_read_b64_tr_b16 v[108:109], v239 offset:0\n"
"global_load_lds_dwordx4 v9, s[48:49]\n"
"v_add_f32 v168, v168, v136\n"
"v_add_f32 v169, v169, v137\n"
"v_cvt_pk_bf16_f32 v154, v136, v137\n"
"v_exp_f32 v138, v138\n"
"v_mfma_f32_16x16x32_bf16 v[52:55], v[96:99], v[148:151], v[52:55]\n"
"ds_read_b64_tr_b16 v[110:111], v239 offset:4096\n"
"v_add_f32 v168, v168, v138\n"
"v_exp_f32 v139, v139\n"
"s_waitcnt lgkmcnt(4)\n"
"v_mfma_f32_16x16x32_bf16 v[56:59], v[100:103], v[144:147], v[56:59]\n"
"global_load_lds_dwordx4 v10, s[48:49] offset:1024\n"
"v_add_f32 v169, v169, v139\n"
"v_cvt_pk_bf16_f32 v155, v138, v139\n"
"v_exp_f32 v140, v140\n"
"v_mfma_f32_16x16x32_bf16 v[60:63], v[100:103], v[148:151], v[60:63]\n"
"s_waitcnt vmcnt(11) lgkmcnt(0)\n"
"s_barrier\n"
"v_add_f32 v170, v170, v140\n"
"v_exp_f32 v141, v141\n"
"v_mfma_f32_16x16x32_bf16 v[64:67], v[104:107], v[144:147], v[64:67]\n"
"ds_read_b64_tr_b16 v[96:97], v232 offset:32768\n"
"ds_read_b64_tr_b16 v[100:101], v233 offset:32768\n"
"global_load_lds_dwordx4 v11, s[48:49] offset:2048\n"
"v_add_f32 v171, v171, v141\n"
"v_cvt_pk_bf16_f32 v158, v140, v141\n"
"v_exp_f32 v142, v142\n"
"v_mfma_f32_16x16x32_bf16 v[68:71], v[104:107], v[148:151], v[68:71]\n"
"ds_read_b64_tr_b16 v[98:99], v232 offset:36864\n"
"ds_read_b64_tr_b16 v[102:103], v233 offset:36864\n"
"ds_read_b64_tr_b16 v[104:105], v234 offset:32768\n"
"v_add_f32 v170, v170, v142\n"
"v_exp_f32 v143, v143\n"
"v_mfma_f32_16x16x32_bf16 v[72:75], v[108:111], v[144:147], v[72:75]\n"
"ds_read_b64_tr_b16 v[106:107], v234 offset:36864\n"
"v_add_f32 v171, v171, v143\n"
"v_cvt_pk_bf16_f32 v159, v142, v143\n"
"v_mfma_f32_16x16x32_bf16 v[76:79], v[108:111], v[148:151], v[76:79]\n"
"s_waitcnt lgkmcnt(3)\n"
"v_mfma_f32_16x16x32_bf16 v[16:19], v[96:99], v[152:155], v[16:19]\n"
"ds_read_b64_tr_b16 v[108:109], v235 offset:32768\n"
"v_add_f32 v208, v168, v169\n"
"v_add_f32 v210, v170, v171\n"
"v_mfma_f32_16x16x32_bf16 v[20:23], v[96:99], v[156:159], v[20:23]\n"
"ds_read_b64_tr_b16 v[110:111], v235 offset:36864\n"
"v_mov_b32 v209, v208\n"
"v_mov_b32 v211, v210\n"
"s_waitcnt lgkmcnt(4)\n"
"v_mfma_f32_16x16x32_bf16 v[24:27], v[100:103], v[152:155], v[24:27]\n"
"ds_read_b64_tr_b16 v[96:97], v236 offset:32768\n"
"v_permlane32_swap_b32 v208, v209\n"
"v_permlane32_swap_b32 v210, v211\n"
"v_mfma_f32_16x16x32_bf16 v[28:31], v[100:103], v[156:159], v[28:31]\n"
"ds_read_b64_tr_b16 v[98:99], v236 offset:36864\n"
"v_add_f32 v208, v208, v209\n"
"v_add_f32 v210, v210, v211\n"
"s_waitcnt lgkmcnt(4)\n"
"v_mfma_f32_16x16x32_bf16 v[32:35], v[104:107], v[152:155], v[32:35]\n"
"ds_read_b64_tr_b16 v[100:101], v237 offset:32768\n"
"v_mov_b32 v209, v208\n"
"v_mfma_f32_16x16x32_bf16 v[36:39], v[104:107], v[156:159], v[36:39]\n"
"ds_read_b64_tr_b16 v[102:103], v237 offset:36864\n"
"v_mov_b32 v211, v210\n"
"v_permlane16_swap_b32 v208, v209\n"
"s_waitcnt lgkmcnt(4)\n"
"v_mfma_f32_16x16x32_bf16 v[40:43], v[108:111], v[152:155], v[40:43]\n"
"ds_read_b64_tr_b16 v[104:105], v238 offset:32768\n"
"v_permlane16_swap_b32 v210, v211\n"
"v_add_f32 v208, v208, v209\n"
"v_mfma_f32_16x16x32_bf16 v[44:47], v[108:111], v[156:159], v[44:47]\n"
"ds_read_b64_tr_b16 v[106:107], v238 offset:36864\n"
"v_add_f32 v210, v210, v211\n"
"v_mov_b32 v174, 0x5d800000\n"
"s_waitcnt lgkmcnt(4)\n"
"v_mfma_f32_16x16x32_bf16 v[48:51], v[96:99], v[152:155], v[48:51]\n"
"ds_read_b64_tr_b16 v[108:109], v239 offset:32768\n"
"v_add_f32 v212, v208, v210\n"
"v_cmp_nlt_f32 vcc, v212, v174\n"
"v_mfma_f32_16x16x32_bf16 v[52:55], v[96:99], v[156:159], v[52:55]\n"
"ds_read_b64_tr_b16 v[110:111], v239 offset:36864\n"
"v_mov_b32 v212, 1\n"
"s_waitcnt lgkmcnt(4)\n"
"v_mfma_f32_16x16x32_bf16 v[56:59], v[100:103], v[152:155], v[56:59]\n"
"s_mov_b64 exec, vcc\n"
"ds_write_b32 v213, v212 offset:0\n"
"s_mov_b64 exec, -1\n"
"v_mfma_f32_16x16x32_bf16 v[60:63], v[100:103], v[156:159], v[60:63]\n"
"s_waitcnt lgkmcnt(0)\n"
"s_barrier\n"
"ds_read_b32 v212, v213 offset:0\n"
"v_rcp_f32 v209, v208\n"
"v_rcp_f32 v211, v210\n"
"v_mfma_f32_16x16x32_bf16 v[64:67], v[104:107], v[152:155], v[64:67]\n"
"v_add_u32 v244, 0xc000, v2\n"
"v_add_u32 v245, 0xc000, v3\n"
"v_add_u32 v246, 0xc000, v4\n"
"v_add_u32 v247, 0xffff0000, v2\n"
"v_add_u32 v248, 0xffff0000, v3\n"
"v_add_u32 v249, 0xffff0000, v4\n"
"v_add_u32 v250, 0x9000, v5\n"
"v_mfma_f32_16x16x32_bf16 v[68:71], v[104:107], v[156:159], v[68:71]\n"
"v_mfma_f32_16x16x32_bf16 v[72:75], v[108:111], v[152:155], v[72:75]\n"
"v_mfma_f32_16x16x32_bf16 v[76:79], v[108:111], v[156:159], v[76:79]\n"
"s_waitcnt lgkmcnt(0)\n"
"s_barrier\n"
"v_readfirstlane_b32 s40, v212\n"
"s_nop 1\n"
"s_andn2_b32 s40, s40, s27\n"
"s_cbranch_scc1 F1_FALLBACK\n"
"v_xor_b32 v174, 32, v14\n"
"v_mul_f32 v16, v16, v209\n"
"v_mul_f32 v17, v17, v209\n"
"v_mul_f32 v18, v18, v209\n"
"v_mul_f32 v19, v19, v209\n"
"v_cvt_pk_bf16_f32 v112, v16, v17\n"
"v_cvt_pk_bf16_f32 v113, v18, v19\n"
"ds_write_b64 v14, v[112:113] offset:0\n"
"v_mul_f32 v20, v20, v211\n"
"v_mul_f32 v21, v21, v211\n"
"v_mul_f32 v22, v22, v211\n"
"v_mul_f32 v23, v23, v211\n"
"v_cvt_pk_bf16_f32 v114, v20, v21\n"
"v_cvt_pk_bf16_f32 v115, v22, v23\n"
"ds_write_b64 v14, v[114:115] offset:1024\n"
"v_mul_f32 v24, v24, v209\n"
"v_mul_f32 v25, v25, v209\n"
"v_mul_f32 v26, v26, v209\n"
"v_mul_f32 v27, v27, v209\n"
"v_cvt_pk_bf16_f32 v116, v24, v25\n"
"v_cvt_pk_bf16_f32 v117, v26, v27\n"
"ds_write_b64 v174, v[116:117] offset:0\n"
"v_mul_f32 v28, v28, v211\n"
"v_mul_f32 v29, v29, v211\n"
"v_mul_f32 v30, v30, v211\n"
"v_mul_f32 v31, v31, v211\n"
"v_cvt_pk_bf16_f32 v118, v28, v29\n"
"v_cvt_pk_bf16_f32 v119, v30, v31\n"
"ds_write_b64 v174, v[118:119] offset:1024\n"
"v_mul_f32 v32, v32, v209\n"
"v_mul_f32 v33, v33, v209\n"
"v_mul_f32 v34, v34, v209\n"
"v_mul_f32 v35, v35, v209\n"
"v_cvt_pk_bf16_f32 v120, v32, v33\n"
"v_cvt_pk_bf16_f32 v121, v34, v35\n"
"ds_write_b64 v14, v[120:121] offset:4096\n"
"v_mul_f32 v36, v36, v211\n"
"v_mul_f32 v37, v37, v211\n"
"v_mul_f32 v38, v38, v211\n"
"v_mul_f32 v39, v39, v211\n"
"v_cvt_pk_bf16_f32 v122, v36, v37\n"
"v_cvt_pk_bf16_f32 v123, v38, v39\n"
"ds_write_b64 v14, v[122:123] offset:5120\n"
"v_mul_f32 v40, v40, v209\n"
"v_mul_f32 v41, v41, v209\n"
"v_mul_f32 v42, v42, v209\n"
"v_mul_f32 v43, v43, v209\n"
"v_cvt_pk_bf16_f32 v124, v40, v41\n"
"v_cvt_pk_bf16_f32 v125, v42, v43\n"
"ds_write_b64 v174, v[124:125] offset:4096\n"
"v_mul_f32 v44, v44, v211\n"
"v_mul_f32 v45, v45, v211\n"
"v_mul_f32 v46, v46, v211\n"
"v_mul_f32 v47, v47, v211\n"
"v_cvt_pk_bf16_f32 v126, v44, v45\n"
"v_cvt_pk_bf16_f32 v127, v46, v47\n"
"ds_write_b64 v174, v[126:127] offset:5120\n"
"v_mul_f32 v48, v48, v209\n"
"v_mul_f32 v49, v49, v209\n"
"v_mul_f32 v50, v50, v209\n"
"v_mul_f32 v51, v51, v209\n"
"v_cvt_pk_bf16_f32 v112, v48, v49\n"
"v_cvt_pk_bf16_f32 v113, v50, v51\n"
"ds_write_b64 v14, v[112:113] offset:8192\n"
"v_mul_f32 v52, v52, v211\n"
"v_mul_f32 v53, v53, v211\n"
"v_mul_f32 v54, v54, v211\n"
"v_mul_f32 v55, v55, v211\n"
"v_cvt_pk_bf16_f32 v114, v52, v53\n"
"v_cvt_pk_bf16_f32 v115, v54, v55\n"
"ds_write_b64 v14, v[114:115] offset:9216\n"
"v_mul_f32 v56, v56, v209\n"
"v_mul_f32 v57, v57, v209\n"
"v_mul_f32 v58, v58, v209\n"
"v_mul_f32 v59, v59, v209\n"
"v_cvt_pk_bf16_f32 v116, v56, v57\n"
"v_cvt_pk_bf16_f32 v117, v58, v59\n"
"ds_write_b64 v174, v[116:117] offset:8192\n"
"v_mul_f32 v60, v60, v211\n"
"v_mul_f32 v61, v61, v211\n"
"v_mul_f32 v62, v62, v211\n"
"v_mul_f32 v63, v63, v211\n"
"v_cvt_pk_bf16_f32 v118, v60, v61\n"
"v_cvt_pk_bf16_f32 v119, v62, v63\n"
"ds_write_b64 v174, v[118:119] offset:9216\n"
"v_mul_f32 v64, v64, v209\n"
"v_mul_f32 v65, v65, v209\n"
"v_mul_f32 v66, v66, v209\n"
"v_mul_f32 v67, v67, v209\n"
"v_cvt_pk_bf16_f32 v120, v64, v65\n"
"v_cvt_pk_bf16_f32 v121, v66, v67\n"
"ds_write_b64 v14, v[120:121] offset:12288\n"
"v_mul_f32 v68, v68, v211\n"
"v_mul_f32 v69, v69, v211\n"
"v_mul_f32 v70, v70, v211\n"
"v_mul_f32 v71, v71, v211\n"
"v_cvt_pk_bf16_f32 v122, v68, v69\n"
"v_cvt_pk_bf16_f32 v123, v70, v71\n"
"ds_write_b64 v14, v[122:123] offset:13312\n"
"v_mul_f32 v72, v72, v209\n"
"v_mul_f32 v73, v73, v209\n"
"v_mul_f32 v74, v74, v209\n"
"v_mul_f32 v75, v75, v209\n"
"v_cvt_pk_bf16_f32 v124, v72, v73\n"
"v_cvt_pk_bf16_f32 v125, v74, v75\n"
"ds_write_b64 v174, v[124:125] offset:12288\n"
"v_mul_f32 v76, v76, v211\n"
"v_mul_f32 v77, v77, v211\n"
"v_mul_f32 v78, v78, v211\n"
"v_mul_f32 v79, v79, v211\n"
"v_cvt_pk_bf16_f32 v126, v76, v77\n"
"v_cvt_pk_bf16_f32 v127, v78, v79\n"
"ds_write_b64 v174, v[126:127] offset:13312\n"
"s_waitcnt vmcnt(6) lgkmcnt(0)\n"
"s_barrier\n"
"ds_read_b128 v[88:91], v5 offset:0\n"
"ds_read_b64_tr_b16 v[52:53], v2 offset:0\n"
"ds_read_b64_tr_b16 v[54:55], v2 offset:512\n"
"ds_read_b128 v[92:95], v5 offset:1024\n"
"ds_read_b64_tr_b16 v[56:57], v3 offset:0\n"
"ds_read_b64_tr_b16 v[58:59], v3 offset:512\n"
"ds_read_b64_tr_b16 v[60:61], v4 offset:0\n"
"ds_read_b64_tr_b16 v[62:63], v4 offset:512\n"
"ds_read_b128 v[96:99], v5 offset:4096\n"
"ds_read_b128 v[100:103], v5 offset:5120\n"
"ds_read_b64_tr_b16 v[64:65], v2 offset:12288\n"
"ds_read_b64_tr_b16 v[66:67], v2 offset:12800\n"
"ds_read_b64_tr_b16 v[68:69], v3 offset:12288\n"
"ds_read_b64_tr_b16 v[70:71], v3 offset:12800\n"
"ds_read_b64_tr_b16 v[72:73], v4 offset:12288\n"
"ds_read_b64_tr_b16 v[74:75], v4 offset:12800\n"
"ds_read_b128 v[104:107], v5 offset:8192\n"
"ds_read_b128 v[108:111], v5 offset:9216\n"
"s_waitcnt lgkmcnt(15)\n"
"v_mfma_f32_16x16x32_bf16 v[16:19], v[52:55], v[88:91], 0\n"
"ds_read_b64_tr_b16 v[76:77], v2 offset:24576\n"
"s_waitcnt lgkmcnt(15)\n"
"v_mfma_f32_16x16x32_bf16 v[20:23], v[52:55], v[92:95], 0\n"
"ds_read_b64_tr_b16 v[78:79], v2 offset:25088\n"
"ds_read_b128 v[112:115], v5 offset:12288\n"
"ds_read_b128 v[116:119], v5 offset:13312\n"
"s_waitcnt lgkmcnt(15)\n"
"v_mfma_f32_16x16x32_bf16 v[24:27], v[56:59], v[88:91], 0\n"
"ds_read_b64_tr_b16 v[80:81], v3 offset:24576\n"
"ds_read_b128 v[120:123], v5 offset:16384\n"
"ds_read_b128 v[124:127], v5 offset:17408\n"
"v_mfma_f32_16x16x32_bf16 v[28:31], v[56:59], v[92:95], 0\n"
"ds_read_b64_tr_b16 v[82:83], v3 offset:25088\n"
"ds_read_b128 v[128:131], v5 offset:20480\n"
"ds_read_b128 v[132:135], v5 offset:21504\n"
"s_waitcnt lgkmcnt(15)\n"
"v_mfma_f32_16x16x32_bf16 v[32:35], v[60:63], v[88:91], 0\n"
"ds_read_b64_tr_b16 v[84:85], v4 offset:24576\n"
"ds_read_b128 v[136:139], v5 offset:24576\n"
"ds_read_b128 v[140:143], v5 offset:25600\n"
"v_mfma_f32_16x16x32_bf16 v[36:39], v[60:63], v[92:95], 0\n"
"ds_read_b64_tr_b16 v[86:87], v4 offset:25088\n"
"v_mfma_f32_16x16x32_bf16 v[16:19], v[64:67], v[96:99], v[16:19]\n"
"ds_read_b64_tr_b16 v[52:53], v2 offset:36864\n"
"v_mfma_f32_16x16x32_bf16 v[20:23], v[64:67], v[100:103], v[20:23]\n"
"ds_read_b64_tr_b16 v[54:55], v2 offset:37376\n"
"ds_read_b128 v[144:147], v5 offset:28672\n"
"ds_read_b128 v[148:151], v5 offset:29696\n"
"s_waitcnt lgkmcnt(15)\n"
"v_mfma_f32_16x16x32_bf16 v[24:27], v[68:71], v[96:99], v[24:27]\n"
"ds_read_b64_tr_b16 v[56:57], v3 offset:36864\n"
"ds_read_b128 v[152:155], v5 offset:32768\n"
"ds_read_b128 v[156:159], v5 offset:33792\n"
"v_mfma_f32_16x16x32_bf16 v[28:31], v[68:71], v[100:103], v[28:31]\n"
"ds_read_b64_tr_b16 v[58:59], v3 offset:37376\n"
"v_mfma_f32_16x16x32_bf16 v[32:35], v[72:75], v[96:99], v[32:35]\n"
"ds_read_b64_tr_b16 v[60:61], v4 offset:36864\n"
"ds_read_b128 v[160:163], v250 offset:0\n"
"ds_read_b128 v[164:167], v250 offset:1024\n"
"v_mfma_f32_16x16x32_bf16 v[36:39], v[72:75], v[100:103], v[36:39]\n"
"ds_read_b64_tr_b16 v[62:63], v4 offset:37376\n"
"v_mfma_f32_16x16x32_bf16 v[16:19], v[76:79], v[104:107], v[16:19]\n"
"s_waitcnt vmcnt(0) lgkmcnt(0)\n"
"s_barrier\n"
"ds_read_b64_tr_b16 v[64:65], v244 offset:0\n"
"v_mfma_f32_16x16x32_bf16 v[20:23], v[76:79], v[108:111], v[20:23]\n"
"ds_read_b64_tr_b16 v[66:67], v244 offset:512\n"
"ds_read_b128 v[168:171], v250 offset:4096\n"
"ds_read_b128 v[172:175], v250 offset:5120\n"
"s_add_u32 s48, s50, 0x48000\n"
"s_addc_u32 s49, s51, 0\n"
"s_add_u32 m0, s44, 0x10000\n"
"v_mfma_f32_16x16x32_bf16 v[24:27], v[80:83], v[104:107], v[24:27]\n"
"ds_read_b64_tr_b16 v[68:69], v245 offset:0\n"
"ds_read_b128 v[176:179], v250 offset:8192\n"
"ds_read_b128 v[180:183], v250 offset:9216\n"
"global_load_lds_dwordx4 v6, s[48:49]\n"
"global_load_lds_dwordx4 v7, s[48:49] offset:1024\n"
"v_mfma_f32_16x16x32_bf16 v[28:31], v[80:83], v[108:111], v[28:31]\n"
"ds_read_b64_tr_b16 v[70:71], v245 offset:512\n"
"v_mfma_f32_16x16x32_bf16 v[32:35], v[84:87], v[104:107], v[32:35]\n"
"ds_read_b64_tr_b16 v[72:73], v246 offset:0\n"
"ds_read_b128 v[184:187], v250 offset:12288\n"
"ds_read_b128 v[188:191], v250 offset:13312\n"
"global_load_lds_dwordx4 v8, s[48:49] offset:2048\n"
"v_mfma_f32_16x16x32_bf16 v[36:39], v[84:87], v[108:111], v[36:39]\n"
"ds_read_b64_tr_b16 v[74:75], v246 offset:512\n"
"v_mfma_f32_16x16x32_bf16 v[16:19], v[52:55], v[112:115], v[16:19]\n"
"ds_read_b64_tr_b16 v[76:77], v244 offset:12288\n"
"v_mfma_f32_16x16x32_bf16 v[20:23], v[52:55], v[116:119], v[20:23]\n"
"ds_read_b64_tr_b16 v[78:79], v244 offset:12800\n"
"ds_read_b128 v[192:195], v250 offset:16384\n"
"ds_read_b128 v[196:199], v250 offset:17408\n"
"s_add_u32 m0, s44, 0x10c00\n"
"v_mfma_f32_16x16x32_bf16 v[24:27], v[56:59], v[112:115], v[24:27]\n"
"ds_read_b64_tr_b16 v[80:81], v245 offset:12288\n"
"ds_read_b128 v[200:203], v250 offset:20480\n"
"ds_read_b128 v[204:207], v250 offset:21504\n"
"global_load_lds_dwordx4 v9, s[48:49]\n"
"global_load_lds_dwordx4 v10, s[48:49] offset:1024\n"
"v_mfma_f32_16x16x32_bf16 v[28:31], v[56:59], v[116:119], v[28:31]\n"
"ds_read_b64_tr_b16 v[82:83], v245 offset:12800\n"
"v_mfma_f32_16x16x32_bf16 v[32:35], v[60:63], v[112:115], v[32:35]\n"
"ds_read_b64_tr_b16 v[84:85], v246 offset:12288\n"
"ds_read_b128 v[208:211], v250 offset:24576\n"
"ds_read_b128 v[212:215], v250 offset:25600\n"
"global_load_lds_dwordx4 v11, s[48:49] offset:2048\n"
"v_mfma_f32_16x16x32_bf16 v[36:39], v[60:63], v[116:119], v[36:39]\n"
"ds_read_b64_tr_b16 v[86:87], v246 offset:12800\n"
"s_waitcnt lgkmcnt(15)\n"
"v_mfma_f32_16x16x32_bf16 v[16:19], v[64:67], v[120:123], v[16:19]\n"
"s_waitcnt lgkmcnt(0)\n"
"s_barrier\n"
"ds_read_b64_tr_b16 v[52:53], v244 offset:24576\n"
"v_mfma_f32_16x16x32_bf16 v[20:23], v[64:67], v[124:127], v[20:23]\n"
"ds_read_b64_tr_b16 v[54:55], v244 offset:25088\n"
"s_add_u32 s48, s50, 0x6c000\n"
"s_addc_u32 s49, s51, 0\n"
"s_mov_b32 m0, s44\n"
"v_mfma_f32_16x16x32_bf16 v[24:27], v[68:71], v[120:123], v[24:27]\n"
"ds_read_b64_tr_b16 v[56:57], v245 offset:24576\n"
"global_load_lds_dwordx4 v6, s[48:49]\n"
"global_load_lds_dwordx4 v7, s[48:49] offset:1024\n"
"v_mfma_f32_16x16x32_bf16 v[28:31], v[68:71], v[124:127], v[28:31]\n"
"ds_read_b64_tr_b16 v[58:59], v245 offset:25088\n"
"v_mfma_f32_16x16x32_bf16 v[32:35], v[72:75], v[120:123], v[32:35]\n"
"ds_read_b64_tr_b16 v[60:61], v246 offset:24576\n"
"global_load_lds_dwordx4 v8, s[48:49] offset:2048\n"
"v_mfma_f32_16x16x32_bf16 v[36:39], v[72:75], v[124:127], v[36:39]\n"
"ds_read_b64_tr_b16 v[62:63], v246 offset:25088\n"
"v_mfma_f32_16x16x32_bf16 v[16:19], v[76:79], v[128:131], v[16:19]\n"
"ds_read_b64_tr_b16 v[64:65], v244 offset:36864\n"
"v_mfma_f32_16x16x32_bf16 v[20:23], v[76:79], v[132:135], v[20:23]\n"
"ds_read_b64_tr_b16 v[66:67], v244 offset:37376\n"
"s_add_u32 m0, s44, 0xc00\n"
"v_mfma_f32_16x16x32_bf16 v[24:27], v[80:83], v[128:131], v[24:27]\n"
"ds_read_b64_tr_b16 v[68:69], v245 offset:36864\n"
"global_load_lds_dwordx4 v9, s[48:49]\n"
"global_load_lds_dwordx4 v10, s[48:49] offset:1024\n"
"v_mfma_f32_16x16x32_bf16 v[28:31], v[80:83], v[132:135], v[28:31]\n"
"ds_read_b64_tr_b16 v[70:71], v245 offset:37376\n"
"v_mfma_f32_16x16x32_bf16 v[32:35], v[84:87], v[128:131], v[32:35]\n"
"ds_read_b64_tr_b16 v[72:73], v246 offset:36864\n"
"global_load_lds_dwordx4 v11, s[48:49] offset:2048\n"
"v_mfma_f32_16x16x32_bf16 v[36:39], v[84:87], v[132:135], v[36:39]\n"
"ds_read_b64_tr_b16 v[74:75], v246 offset:37376\n"
"s_waitcnt lgkmcnt(10)\n"
"v_mfma_f32_16x16x32_bf16 v[16:19], v[52:55], v[136:139], v[16:19]\n"
"s_waitcnt vmcnt(6) lgkmcnt(0)\n"
"s_barrier\n"
"ds_read_b64_tr_b16 v[76:77], v2 offset:0\n"
"v_mfma_f32_16x16x32_bf16 v[20:23], v[52:55], v[140:143], v[20:23]\n"
"ds_read_b64_tr_b16 v[78:79], v2 offset:512\n"
"s_add_u32 s48, s50, 0x180\n"
"s_addc_u32 s49, s51, 0\n"
"s_add_u32 m0, s44, 0x1c000\n"
"v_mfma_f32_16x16x32_bf16 v[24:27], v[56:59], v[136:139], v[24:27]\n"
"ds_read_b64_tr_b16 v[80:81], v3 offset:0\n"
"global_load_lds_dwordx4 v6, s[48:49]\n"
"v_mfma_f32_16x16x32_bf16 v[28:31], v[56:59], v[140:143], v[28:31]\n"
"ds_read_b64_tr_b16 v[82:83], v3 offset:512\n"
"global_load_lds_dwordx4 v7, s[48:49] offset:1024\n"
"v_mfma_f32_16x16x32_bf16 v[32:35], v[60:63], v[136:139], v[32:35]\n"
"ds_read_b64_tr_b16 v[84:85], v4 offset:0\n"
"v_mfma_f32_16x16x32_bf16 v[36:39], v[60:63], v[140:143], v[36:39]\n"
"ds_read_b64_tr_b16 v[86:87], v4 offset:512\n"
"v_mfma_f32_16x16x32_bf16 v[16:19], v[64:67], v[144:147], v[16:19]\n"
"ds_read_b64_tr_b16 v[52:53], v2 offset:12288\n"
"v_mfma_f32_16x16x32_bf16 v[20:23], v[64:67], v[148:151], v[20:23]\n"
"ds_read_b64_tr_b16 v[54:55], v2 offset:12800\n"
"global_load_lds_dwordx4 v8, s[48:49] offset:2048\n"
"v_mfma_f32_16x16x32_bf16 v[24:27], v[68:71], v[144:147], v[24:27]\n"
"ds_read_b64_tr_b16 v[56:57], v3 offset:12288\n"
"s_add_u32 m0, s44, 0x1cc00\n"
"v_mfma_f32_16x16x32_bf16 v[28:31], v[68:71], v[148:151], v[28:31]\n"
"ds_read_b64_tr_b16 v[58:59], v3 offset:12800\n"
"global_load_lds_dwordx4 v9, s[48:49]\n"
"v_mfma_f32_16x16x32_bf16 v[32:35], v[72:75], v[144:147], v[32:35]\n"
"ds_read_b64_tr_b16 v[60:61], v4 offset:12288\n"
"v_mfma_f32_16x16x32_bf16 v[36:39], v[72:75], v[148:151], v[36:39]\n"
"ds_read_b64_tr_b16 v[62:63], v4 offset:12800\n"
"s_waitcnt lgkmcnt(10)\n"
"v_mfma_f32_16x16x32_bf16 v[16:19], v[76:79], v[152:155], v[16:19]\n"
"ds_read_b64_tr_b16 v[64:65], v2 offset:24576\n"
"v_mfma_f32_16x16x32_bf16 v[20:23], v[76:79], v[156:159], v[20:23]\n"
"ds_read_b64_tr_b16 v[66:67], v2 offset:25088\n"
"global_load_lds_dwordx4 v10, s[48:49] offset:1024\n"
"s_waitcnt lgkmcnt(10)\n"
"v_mfma_f32_16x16x32_bf16 v[24:27], v[80:83], v[152:155], v[24:27]\n"
"ds_read_b64_tr_b16 v[68:69], v3 offset:24576\n"
"v_mfma_f32_16x16x32_bf16 v[28:31], v[80:83], v[156:159], v[28:31]\n"
"ds_read_b64_tr_b16 v[70:71], v3 offset:25088\n"
"s_waitcnt lgkmcnt(10)\n"
"v_mfma_f32_16x16x32_bf16 v[32:35], v[84:87], v[152:155], v[32:35]\n"
"ds_read_b64_tr_b16 v[72:73], v4 offset:24576\n"
"v_mfma_f32_16x16x32_bf16 v[36:39], v[84:87], v[156:159], v[36:39]\n"
"ds_read_b64_tr_b16 v[74:75], v4 offset:25088\n"
"s_waitcnt lgkmcnt(10)\n"
"v_mfma_f32_16x16x32_bf16 v[16:19], v[52:55], v[160:163], v[16:19]\n"
"ds_read_b64_tr_b16 v[76:77], v2 offset:36864\n"
"v_mfma_f32_16x16x32_bf16 v[20:23], v[52:55], v[164:167], v[20:23]\n"
"ds_read_b64_tr_b16 v[78:79], v2 offset:37376\n"
"global_load_lds_dwordx4 v11, s[48:49] offset:2048\n"
"s_waitcnt lgkmcnt(10)\n"
"v_mfma_f32_16x16x32_bf16 v[24:27], v[56:59], v[160:163], v[24:27]\n"
"ds_read_b64_tr_b16 v[80:81], v3 offset:36864\n"
"v_mfma_f32_16x16x32_bf16 v[28:31], v[56:59], v[164:167], v[28:31]\n"
"ds_read_b64_tr_b16 v[82:83], v3 offset:37376\n"
"s_waitcnt lgkmcnt(10)\n"
"v_mfma_f32_16x16x32_bf16 v[32:35], v[60:63], v[160:163], v[32:35]\n"
"ds_read_b64_tr_b16 v[84:85], v4 offset:36864\n"
"v_mfma_f32_16x16x32_bf16 v[36:39], v[60:63], v[164:167], v[36:39]\n"
"ds_read_b64_tr_b16 v[86:87], v4 offset:37376\n"
"s_waitcnt lgkmcnt(10)\n"
"v_mfma_f32_16x16x32_bf16 v[16:19], v[64:67], v[168:171], v[16:19]\n"
"s_waitcnt vmcnt(6) lgkmcnt(0)\n"
"s_barrier\n"
"ds_read_b64_tr_b16 v[52:53], v247 offset:0\n"
"v_mfma_f32_16x16x32_bf16 v[20:23], v[64:67], v[172:175], v[20:23]\n"
"ds_read_b64_tr_b16 v[54:55], v247 offset:512\n"
"s_add_u32 s48, s50, 0x24180\n"
"s_addc_u32 s49, s51, 0\n"
"s_add_u32 m0, s44, 0x10000\n"
"v_mfma_f32_16x16x32_bf16 v[24:27], v[68:71], v[168:171], v[24:27]\n"
"ds_read_b64_tr_b16 v[56:57], v248 offset:0\n"
"global_load_lds_dwordx4 v6, s[48:49]\n"
"v_mfma_f32_16x16x32_bf16 v[28:31], v[68:71], v[172:175], v[28:31]\n"
"ds_read_b64_tr_b16 v[58:59], v248 offset:512\n"
"global_load_lds_dwordx4 v7, s[48:49] offset:1024\n"
"v_mfma_f32_16x16x32_bf16 v[32:35], v[72:75], v[168:171], v[32:35]\n"
"ds_read_b64_tr_b16 v[60:61], v249 offset:0\n"
"v_mfma_f32_16x16x32_bf16 v[36:39], v[72:75], v[172:175], v[36:39]\n"
"ds_read_b64_tr_b16 v[62:63], v249 offset:512\n"
"v_mfma_f32_16x16x32_bf16 v[16:19], v[76:79], v[176:179], v[16:19]\n"
"ds_read_b64_tr_b16 v[64:65], v247 offset:12288\n"
"v_mfma_f32_16x16x32_bf16 v[20:23], v[76:79], v[180:183], v[20:23]\n"
"ds_read_b64_tr_b16 v[66:67], v247 offset:12800\n"
"global_load_lds_dwordx4 v8, s[48:49] offset:2048\n"
"v_mfma_f32_16x16x32_bf16 v[24:27], v[80:83], v[176:179], v[24:27]\n"
"ds_read_b64_tr_b16 v[68:69], v248 offset:12288\n"
"s_add_u32 m0, s44, 0x10c00\n"
"v_mfma_f32_16x16x32_bf16 v[28:31], v[80:83], v[180:183], v[28:31]\n"
"ds_read_b64_tr_b16 v[70:71], v248 offset:12800\n"
"global_load_lds_dwordx4 v9, s[48:49]\n"
"v_mfma_f32_16x16x32_bf16 v[32:35], v[84:87], v[176:179], v[32:35]\n"
"ds_read_b64_tr_b16 v[72:73], v249 offset:12288\n"
"v_mfma_f32_16x16x32_bf16 v[36:39], v[84:87], v[180:183], v[36:39]\n"
"ds_read_b64_tr_b16 v[74:75], v249 offset:12800\n"
"s_waitcnt lgkmcnt(10)\n"
"v_mfma_f32_16x16x32_bf16 v[16:19], v[52:55], v[184:187], v[16:19]\n"
"ds_read_b64_tr_b16 v[76:77], v247 offset:24576\n"
"v_mfma_f32_16x16x32_bf16 v[20:23], v[52:55], v[188:191], v[20:23]\n"
"ds_read_b64_tr_b16 v[78:79], v247 offset:25088\n"
"global_load_lds_dwordx4 v10, s[48:49] offset:1024\n"
"s_waitcnt lgkmcnt(10)\n"
"v_mfma_f32_16x16x32_bf16 v[24:27], v[56:59], v[184:187], v[24:27]\n"
"ds_read_b64_tr_b16 v[80:81], v248 offset:24576\n"
"v_mfma_f32_16x16x32_bf16 v[28:31], v[56:59], v[188:191], v[28:31]\n"
"ds_read_b64_tr_b16 v[82:83], v248 offset:25088\n"
"s_waitcnt lgkmcnt(10)\n"
"v_mfma_f32_16x16x32_bf16 v[32:35], v[60:63], v[184:187], v[32:35]\n"
"ds_read_b64_tr_b16 v[84:85], v249 offset:24576\n"
"v_mfma_f32_16x16x32_bf16 v[36:39], v[60:63], v[188:191], v[36:39]\n"
"ds_read_b64_tr_b16 v[86:87], v249 offset:25088\n"
"s_waitcnt lgkmcnt(10)\n"
"v_mfma_f32_16x16x32_bf16 v[16:19], v[64:67], v[192:195], v[16:19]\n"
"ds_read_b64_tr_b16 v[52:53], v247 offset:36864\n"
"v_mfma_f32_16x16x32_bf16 v[20:23], v[64:67], v[196:199], v[20:23]\n"
"ds_read_b64_tr_b16 v[54:55], v247 offset:37376\n"
"global_load_lds_dwordx4 v11, s[48:49] offset:2048\n"
"s_waitcnt lgkmcnt(10)\n"
"v_mfma_f32_16x16x32_bf16 v[24:27], v[68:71], v[192:195], v[24:27]\n"
"ds_read_b64_tr_b16 v[56:57], v248 offset:36864\n"
"v_mfma_f32_16x16x32_bf16 v[28:31], v[68:71], v[196:199], v[28:31]\n"
"ds_read_b64_tr_b16 v[58:59], v248 offset:37376\n"
"s_waitcnt lgkmcnt(10)\n"
"v_mfma_f32_16x16x32_bf16 v[32:35], v[72:75], v[192:195], v[32:35]\n"
"ds_read_b64_tr_b16 v[60:61], v249 offset:36864\n"
"v_mfma_f32_16x16x32_bf16 v[36:39], v[72:75], v[196:199], v[36:39]\n"
"ds_read_b64_tr_b16 v[62:63], v249 offset:37376\n"
"s_waitcnt lgkmcnt(10)\n"
"v_mfma_f32_16x16x32_bf16 v[16:19], v[76:79], v[200:203], v[16:19]\n"
"s_waitcnt vmcnt(6) lgkmcnt(0)\n"
"s_barrier\n"
"ds_read_b64_tr_b16 v[64:65], v244 offset:0\n"
"v_mfma_f32_16x16x32_bf16 v[20:23], v[76:79], v[204:207], v[20:23]\n"
"ds_read_b64_tr_b16 v[66:67], v244 offset:512\n"
"s_add_u32 s48, s50, 0x48180\n"
"s_addc_u32 s49, s51, 0\n"
"s_mov_b32 m0, s44\n"
"v_mfma_f32_16x16x32_bf16 v[24:27], v[80:83], v[200:203], v[24:27]\n"
"ds_read_b64_tr_b16 v[68:69], v245 offset:0\n"
"global_load_lds_dwordx4 v6, s[48:49]\n"
"v_mfma_f32_16x16x32_bf16 v[28:31], v[80:83], v[204:207], v[28:31]\n"
"ds_read_b64_tr_b16 v[70:71], v245 offset:512\n"
"global_load_lds_dwordx4 v7, s[48:49] offset:1024\n"
"v_mfma_f32_16x16x32_bf16 v[32:35], v[84:87], v[200:203], v[32:35]\n"
"ds_read_b64_tr_b16 v[72:73], v246 offset:0\n"
"v_mfma_f32_16x16x32_bf16 v[36:39], v[84:87], v[204:207], v[36:39]\n"
"ds_read_b64_tr_b16 v[74:75], v246 offset:512\n"
"v_mfma_f32_16x16x32_bf16 v[16:19], v[52:55], v[208:211], v[16:19]\n"
"ds_read_b64_tr_b16 v[76:77], v244 offset:12288\n"
"v_mfma_f32_16x16x32_bf16 v[20:23], v[52:55], v[212:215], v[20:23]\n"
"ds_read_b64_tr_b16 v[78:79], v244 offset:12800\n"
"global_load_lds_dwordx4 v8, s[48:49] offset:2048\n"
"v_mfma_f32_16x16x32_bf16 v[24:27], v[56:59], v[208:211], v[24:27]\n"
"ds_read_b64_tr_b16 v[80:81], v245 offset:12288\n"
"s_add_u32 m0, s44, 0xc00\n"
"v_mfma_f32_16x16x32_bf16 v[28:31], v[56:59], v[212:215], v[28:31]\n"
"ds_read_b64_tr_b16 v[82:83], v245 offset:12800\n"
"global_load_lds_dwordx4 v9, s[48:49]\n"
"v_mfma_f32_16x16x32_bf16 v[32:35], v[60:63], v[208:211], v[32:35]\n"
"ds_read_b64_tr_b16 v[84:85], v246 offset:12288\n"
"v_mfma_f32_16x16x32_bf16 v[36:39], v[60:63], v[212:215], v[36:39]\n"
"ds_read_b64_tr_b16 v[86:87], v246 offset:12800\n"
"v_cvt_pk_bf16_f32 v40, v16, v17\n"
"v_cvt_pk_bf16_f32 v41, v18, v19\n"
"s_waitcnt lgkmcnt(10)\n"
"v_mfma_f32_16x16x32_bf16 v[16:19], v[64:67], v[88:91], 0\n"
"v_cvt_pk_bf16_f32 v44, v20, v21\n"
"v_cvt_pk_bf16_f32 v45, v22, v23\n"
"ds_read_b64_tr_b16 v[52:53], v244 offset:24576\n"
"v_mfma_f32_16x16x32_bf16 v[20:23], v[64:67], v[92:95], 0\n"
"v_cvt_pk_bf16_f32 v42, v24, v25\n"
"v_cvt_pk_bf16_f32 v43, v26, v27\n"
"ds_read_b64_tr_b16 v[54:55], v244 offset:25088\n"
"global_load_lds_dwordx4 v10, s[48:49] offset:1024\n"
"s_waitcnt lgkmcnt(10)\n"
"v_mfma_f32_16x16x32_bf16 v[24:27], v[68:71], v[88:91], 0\n"
"v_cvt_pk_bf16_f32 v46, v28, v29\n"
"v_cvt_pk_bf16_f32 v47, v30, v31\n"
"ds_read_b64_tr_b16 v[56:57], v245 offset:24576\n"
"v_mfma_f32_16x16x32_bf16 v[28:31], v[68:71], v[92:95], 0\n"
"v_cvt_pk_bf16_f32 v48, v32, v33\n"
"v_cvt_pk_bf16_f32 v49, v34, v35\n"
"ds_read_b64_tr_b16 v[58:59], v245 offset:25088\n"
"s_waitcnt lgkmcnt(10)\n"
"v_mfma_f32_16x16x32_bf16 v[32:35], v[72:75], v[88:91], 0\n"
"v_cvt_pk_bf16_f32 v50, v36, v37\n"
"v_cvt_pk_bf16_f32 v51, v38, v39\n"
"ds_read_b64_tr_b16 v[60:61], v246 offset:24576\n"
"v_mfma_f32_16x16x32_bf16 v[36:39], v[72:75], v[92:95], 0\n"
"ds_read_b64_tr_b16 v[62:63], v246 offset:25088\n"
"s_waitcnt lgkmcnt(10)\n"
"v_mfma_f32_16x16x32_bf16 v[16:19], v[76:79], v[96:99], v[16:19]\n"
"ds_read_b64_tr_b16 v[64:65], v244 offset:36864\n"
"v_permlane32_swap_b32 v40, v42\n"
"v_mfma_f32_16x16x32_bf16 v[20:23], v[76:79], v[100:103], v[20:23]\n"
"ds_read_b64_tr_b16 v[66:67], v244 offset:37376\n"
"global_load_lds_dwordx4 v11, s[48:49] offset:2048\n"
"v_permlane32_swap_b32 v41, v43\n"
"s_waitcnt lgkmcnt(10)\n"
"v_mfma_f32_16x16x32_bf16 v[24:27], v[80:83], v[96:99], v[24:27]\n"
"ds_read_b64_tr_b16 v[68:69], v245 offset:36864\n"
"v_permlane16_swap_b32 v40, v42\n"
"v_mfma_f32_16x16x32_bf16 v[28:31], v[80:83], v[100:103], v[28:31]\n"
"ds_read_b64_tr_b16 v[70:71], v245 offset:37376\n"
"v_permlane16_swap_b32 v41, v43\n"
"s_waitcnt lgkmcnt(10)\n"
"v_mfma_f32_16x16x32_bf16 v[32:35], v[84:87], v[96:99], v[32:35]\n"
"ds_read_b64_tr_b16 v[72:73], v246 offset:36864\n"
"v_mfma_f32_16x16x32_bf16 v[36:39], v[84:87], v[100:103], v[36:39]\n"
"ds_read_b64_tr_b16 v[74:75], v246 offset:37376\n"
"global_store_dwordx4 v12, v[40:43], s[52:53] offset:-3072 sc1\n"
"s_waitcnt lgkmcnt(10)\n"
"v_mfma_f32_16x16x32_bf16 v[16:19], v[52:55], v[104:107], v[16:19]\n"
"s_waitcnt vmcnt(7) lgkmcnt(0)\n"
"s_barrier\n"
"ds_read_b64_tr_b16 v[76:77], v2 offset:0\n"
"v_mfma_f32_16x16x32_bf16 v[20:23], v[52:55], v[108:111], v[20:23]\n"
"ds_read_b64_tr_b16 v[78:79], v2 offset:512\n"
"s_add_u32 s48, s50, 0x6c180\n"
"s_addc_u32 s49, s51, 0\n"
"s_add_u32 m0, s44, 0x1c000\n"
"v_mfma_f32_16x16x32_bf16 v[24:27], v[56:59], v[104:107], v[24:27]\n"
"ds_read_b64_tr_b16 v[80:81], v3 offset:0\n"
"global_load_lds_dwordx4 v6, s[48:49]\n"
"v_mfma_f32_16x16x32_bf16 v[28:31], v[56:59], v[108:111], v[28:31]\n"
"ds_read_b64_tr_b16 v[82:83], v3 offset:512\n"
"global_load_lds_dwordx4 v7, s[48:49] offset:1024\n"
"v_mfma_f32_16x16x32_bf16 v[32:35], v[60:63], v[104:107], v[32:35]\n"
"ds_read_b64_tr_b16 v[84:85], v4 offset:0\n"
"v_mfma_f32_16x16x32_bf16 v[36:39], v[60:63], v[108:111], v[36:39]\n"
"ds_read_b64_tr_b16 v[86:87], v4 offset:512\n"
"v_mfma_f32_16x16x32_bf16 v[16:19], v[64:67], v[112:115], v[16:19]\n"
"ds_read_b64_tr_b16 v[52:53], v2 offset:12288\n"
"v_mfma_f32_16x16x32_bf16 v[20:23], v[64:67], v[116:119], v[20:23]\n"
"ds_read_b64_tr_b16 v[54:55], v2 offset:12800\n"
"global_load_lds_dwordx4 v8, s[48:49] offset:2048\n"
"v_mfma_f32_16x16x32_bf16 v[24:27], v[68:71], v[112:115], v[24:27]\n"
"ds_read_b64_tr_b16 v[56:57], v3 offset:12288\n"
"s_add_u32 m0, s44, 0x1cc00\n"
"v_mfma_f32_16x16x32_bf16 v[28:31], v[68:71], v[116:119], v[28:31]\n"
"ds_read_b64_tr_b16 v[58:59], v3 offset:12800\n"
"global_load_lds_dwordx4 v9, s[48:49]\n"
"v_mfma_f32_16x16x32_bf16 v[32:35], v[72:75], v[112:115], v[32:35]\n"
"ds_read_b64_tr_b16 v[60:61], v4 offset:12288\n"
"v_mfma_f32_16x16x32_bf16 v[36:39], v[72:75], v[116:119], v[36:39]\n"
"ds_read_b64_tr_b16 v[62:63], v4 offset:12800\n"
"s_waitcnt lgkmcnt(10)\n"
"v_mfma_f32_16x16x32_bf16 v[16:19], v[76:79], v[120:123], v[16:19]\n"
"ds_read_b64_tr_b16 v[64:65], v2 offset:24576\n"
"v_mfma_f32_16x16x32_bf16 v[20:23], v[76:79], v[124:127], v[20:23]\n"
"ds_read_b64_tr_b16 v[66:67], v2 offset:25088\n"
"global_load_lds_dwordx4 v10, s[48:49] offset:1024\n"
"s_waitcnt lgkmcnt(10)\n"
"v_mfma_f32_16x16x32_bf16 v[24:27], v[80:83], v[120:123], v[24:27]\n"
"ds_read_b64_tr_b16 v[68:69], v3 offset:24576\n"
"v_mfma_f32_16x16x32_bf16 v[28:31], v[80:83], v[124:127], v[28:31]\n"
"ds_read_b64_tr_b16 v[70:71], v3 offset:25088\n"
"s_waitcnt lgkmcnt(10)\n"
"v_mfma_f32_16x16x32_bf16 v[32:35], v[84:87], v[120:123], v[32:35]\n"
"ds_read_b64_tr_b16 v[72:73], v4 offset:24576\n"
"v_mfma_f32_16x16x32_bf16 v[36:39], v[84:87], v[124:127], v[36:39]\n"
"ds_read_b64_tr_b16 v[74:75], v4 offset:25088\n"
"s_waitcnt lgkmcnt(10)\n"
"v_mfma_f32_16x16x32_bf16 v[16:19], v[52:55], v[128:131], v[16:19]\n"
"ds_read_b64_tr_b16 v[76:77], v2 offset:36864\n"
"v_permlane32_swap_b32 v44, v46\n"
"v_mfma_f32_16x16x32_bf16 v[20:23], v[52:55], v[132:135], v[20:23]\n"
"ds_read_b64_tr_b16 v[78:79], v2 offset:37376\n"
"global_load_lds_dwordx4 v11, s[48:49] offset:2048\n"
"v_permlane32_swap_b32 v45, v47\n"
"s_waitcnt lgkmcnt(10)\n"
"v_mfma_f32_16x16x32_bf16 v[24:27], v[56:59], v[128:131], v[24:27]\n"
"ds_read_b64_tr_b16 v[80:81], v3 offset:36864\n"
"v_permlane16_swap_b32 v44, v46\n"
"v_mfma_f32_16x16x32_bf16 v[28:31], v[56:59], v[132:135], v[28:31]\n"
"ds_read_b64_tr_b16 v[82:83], v3 offset:37376\n"
"v_permlane16_swap_b32 v45, v47\n"
"s_waitcnt lgkmcnt(10)\n"
"v_mfma_f32_16x16x32_bf16 v[32:35], v[60:63], v[128:131], v[32:35]\n"
"ds_read_b64_tr_b16 v[84:85], v4 offset:36864\n"
"v_mfma_f32_16x16x32_bf16 v[36:39], v[60:63], v[132:135], v[36:39]\n"
"ds_read_b64_tr_b16 v[86:87], v4 offset:37376\n"
"global_store_dwordx4 v12, v[44:47], s[54:55] offset:-3072 sc1\n"
"s_waitcnt lgkmcnt(10)\n"
"v_mfma_f32_16x16x32_bf16 v[16:19], v[64:67], v[136:139], v[16:19]\n"
"s_waitcnt vmcnt(8) lgkmcnt(0)\n"
"s_barrier\n"
"ds_read_b64_tr_b16 v[52:53], v247 offset:0\n"
"v_mfma_f32_16x16x32_bf16 v[20:23], v[64:67], v[140:143], v[20:23]\n"
"ds_read_b64_tr_b16 v[54:55], v247 offset:512\n"
"s_add_u32 s48, s50, 0x300\n"
"s_addc_u32 s49, s51, 0\n"
"s_add_u32 m0, s44, 0x10000\n"
"v_mfma_f32_16x16x32_bf16 v[24:27], v[68:71], v[136:139], v[24:27]\n"
"ds_read_b64_tr_b16 v[56:57], v248 offset:0\n"
"global_load_lds_dwordx4 v6, s[48:49]\n"
"v_mfma_f32_16x16x32_bf16 v[28:31], v[68:71], v[140:143], v[28:31]\n"
"ds_read_b64_tr_b16 v[58:59], v248 offset:512\n"
"global_load_lds_dwordx4 v7, s[48:49] offset:1024\n"
"v_mfma_f32_16x16x32_bf16 v[32:35], v[72:75], v[136:139], v[32:35]\n"
"ds_read_b64_tr_b16 v[60:61], v249 offset:0\n"
"v_mfma_f32_16x16x32_bf16 v[36:39], v[72:75], v[140:143], v[36:39]\n"
"ds_read_b64_tr_b16 v[62:63], v249 offset:512\n"
"v_mfma_f32_16x16x32_bf16 v[16:19], v[76:79], v[144:147], v[16:19]\n"
"ds_read_b64_tr_b16 v[64:65], v247 offset:12288\n"
"v_mfma_f32_16x16x32_bf16 v[20:23], v[76:79], v[148:151], v[20:23]\n"
"ds_read_b64_tr_b16 v[66:67], v247 offset:12800\n"
"global_load_lds_dwordx4 v8, s[48:49] offset:2048\n"
"v_mfma_f32_16x16x32_bf16 v[24:27], v[80:83], v[144:147], v[24:27]\n"
"ds_read_b64_tr_b16 v[68:69], v248 offset:12288\n"
"s_add_u32 m0, s44, 0x10c00\n"
"v_mfma_f32_16x16x32_bf16 v[28:31], v[80:83], v[148:151], v[28:31]\n"
"ds_read_b64_tr_b16 v[70:71], v248 offset:12800\n"
"global_load_lds_dwordx4 v9, s[48:49]\n"
"v_mfma_f32_16x16x32_bf16 v[32:35], v[84:87], v[144:147], v[32:35]\n"
"ds_read_b64_tr_b16 v[72:73], v249 offset:12288\n"
"v_mfma_f32_16x16x32_bf16 v[36:39], v[84:87], v[148:151], v[36:39]\n"
"ds_read_b64_tr_b16 v[74:75], v249 offset:12800\n"
"s_waitcnt lgkmcnt(10)\n"
"v_mfma_f32_16x16x32_bf16 v[16:19], v[52:55], v[152:155], v[16:19]\n"
"ds_read_b64_tr_b16 v[76:77], v247 offset:24576\n"
"v_mfma_f32_16x16x32_bf16 v[20:23], v[52:55], v[156:159], v[20:23]\n"
"ds_read_b64_tr_b16 v[78:79], v247 offset:25088\n"
"global_load_lds_dwordx4 v10, s[48:49] offset:1024\n"
"s_waitcnt lgkmcnt(10)\n"
"v_mfma_f32_16x16x32_bf16 v[24:27], v[56:59], v[152:155], v[24:27]\n"
"ds_read_b64_tr_b16 v[80:81], v248 offset:24576\n"
"v_mfma_f32_16x16x32_bf16 v[28:31], v[56:59], v[156:159], v[28:31]\n"
"ds_read_b64_tr_b16 v[82:83], v248 offset:25088\n"
"s_waitcnt lgkmcnt(10)\n"
"v_mfma_f32_16x16x32_bf16 v[32:35], v[60:63], v[152:155], v[32:35]\n"
"ds_read_b64_tr_b16 v[84:85], v249 offset:24576\n"
"v_mfma_f32_16x16x32_bf16 v[36:39], v[60:63], v[156:159], v[36:39]\n"
"ds_read_b64_tr_b16 v[86:87], v249 offset:25088\n"
"s_waitcnt lgkmcnt(10)\n"
"v_mfma_f32_16x16x32_bf16 v[16:19], v[64:67], v[160:163], v[16:19]\n"
"ds_read_b64_tr_b16 v[52:53], v247 offset:36864\n"
"v_permlane16_swap_b32 v48, v50\n"
"v_mfma_f32_16x16x32_bf16 v[20:23], v[64:67], v[164:167], v[20:23]\n"
"ds_read_b64_tr_b16 v[54:55], v247 offset:37376\n"
"global_load_lds_dwordx4 v11, s[48:49] offset:2048\n"
"v_permlane16_swap_b32 v49, v51\n"
"s_waitcnt lgkmcnt(10)\n"
"v_mfma_f32_16x16x32_bf16 v[24:27], v[68:71], v[160:163], v[24:27]\n"
"ds_read_b64_tr_b16 v[56:57], v248 offset:36864\n"
"v_mfma_f32_16x16x32_bf16 v[28:31], v[68:71], v[164:167], v[28:31]\n"
"ds_read_b64_tr_b16 v[58:59], v248 offset:37376\n"
"s_waitcnt lgkmcnt(10)\n"
"v_mfma_f32_16x16x32_bf16 v[32:35], v[72:75], v[160:163], v[32:35]\n"
"ds_read_b64_tr_b16 v[60:61], v249 offset:36864\n"
"v_mfma_f32_16x16x32_bf16 v[36:39], v[72:75], v[164:167], v[36:39]\n"
"ds_read_b64_tr_b16 v[62:63], v249 offset:37376\n"
"global_store_dwordx4 v13, v[48:51], s[52:53] offset:-3072 sc1\n"
"s_waitcnt lgkmcnt(10)\n"
"v_mfma_f32_16x16x32_bf16 v[16:19], v[76:79], v[168:171], v[16:19]\n"
"s_waitcnt vmcnt(8) lgkmcnt(0)\n"
"s_barrier\n"
"ds_read_b64_tr_b16 v[64:65], v244 offset:0\n"
"v_mfma_f32_16x16x32_bf16 v[20:23], v[76:79], v[172:175], v[20:23]\n"
"ds_read_b64_tr_b16 v[66:67], v244 offset:512\n"
"s_add_u32 s48, s50, 0x24300\n"
"s_addc_u32 s49, s51, 0\n"
"s_mov_b32 m0, s44\n"
"v_mfma_f32_16x16x32_bf16 v[24:27], v[80:83], v[168:171], v[24:27]\n"
"ds_read_b64_tr_b16 v[68:69], v245 offset:0\n"
"global_load_lds_dwordx4 v6, s[48:49]\n"
"v_mfma_f32_16x16x32_bf16 v[28:31], v[80:83], v[172:175], v[28:31]\n"
"ds_read_b64_tr_b16 v[70:71], v245 offset:512\n"
"global_load_lds_dwordx4 v7, s[48:49] offset:1024\n"
"v_mfma_f32_16x16x32_bf16 v[32:35], v[84:87], v[168:171], v[32:35]\n"
"ds_read_b64_tr_b16 v[72:73], v246 offset:0\n"
"v_mfma_f32_16x16x32_bf16 v[36:39], v[84:87], v[172:175], v[36:39]\n"
"ds_read_b64_tr_b16 v[74:75], v246 offset:512\n"
"v_mfma_f32_16x16x32_bf16 v[16:19], v[52:55], v[176:179], v[16:19]\n"
"ds_read_b64_tr_b16 v[76:77], v244 offset:12288\n"
"v_mfma_f32_16x16x32_bf16 v[20:23], v[52:55], v[180:183], v[20:23]\n"
"ds_read_b64_tr_b16 v[78:79], v244 offset:12800\n"
"global_load_lds_dwordx4 v8, s[48:49] offset:2048\n"
"v_mfma_f32_16x16x32_bf16 v[24:27], v[56:59], v[176:179], v[24:27]\n"
"ds_read_b64_tr_b16 v[80:81], v245 offset:12288\n"
"s_add_u32 m0, s44, 0xc00\n"
"v_mfma_f32_16x16x32_bf16 v[28:31], v[56:59], v[180:183], v[28:31]\n"
"ds_read_b64_tr_b16 v[82:83], v245 offset:12800\n"
"global_load_lds_dwordx4 v9, s[48:49]\n"
"v_mfma_f32_16x16x32_bf16 v[32:35], v[60:63], v[176:179], v[32:35]\n"
"ds_read_b64_tr_b16 v[84:85], v246 offset:12288\n"
"v_mfma_f32_16x16x32_bf16 v[36:39], v[60:63], v[180:183], v[36:39]\n"
"ds_read_b64_tr_b16 v[86:87], v246 offset:12800\n"
"s_waitcnt lgkmcnt(10)\n"
"v_mfma_f32_16x16x32_bf16 v[16:19], v[64:67], v[184:187], v[16:19]\n"
"ds_read_b64_tr_b16 v[52:53], v244 offset:24576\n"
"v_mfma_f32_16x16x32_bf16 v[20:23], v[64:67], v[188:191], v[20:23]\n"
"ds_read_b64_tr_b16 v[54:55], v244 offset:25088\n"
"global_load_lds_dwordx4 v10, s[48:49] offset:1024\n"
"s_waitcnt lgkmcnt(10)\n"
"v_mfma_f32_16x16x32_bf16 v[24:27], v[68:71], v[184:187], v[24:27]\n"
"ds_read_b64_tr_b16 v[56:57], v245 offset:24576\n"
"v_mfma_f32_16x16x32_bf16 v[28:31], v[68:71], v[188:191], v[28:31]\n"
"ds_read_b64_tr_b16 v[58:59], v245 offset:25088\n"
"s_waitcnt lgkmcnt(10)\n"
"v_mfma_f32_16x16x32_bf16 v[32:35], v[72:75], v[184:187], v[32:35]\n"
"ds_read_b64_tr_b16 v[60:61], v246 offset:24576\n"
"v_mfma_f32_16x16x32_bf16 v[36:39], v[72:75], v[188:191], v[36:39]\n"
"ds_read_b64_tr_b16 v[62:63], v246 offset:25088\n"
"s_waitcnt lgkmcnt(10)\n"
"v_mfma_f32_16x16x32_bf16 v[16:19], v[76:79], v[192:195], v[16:19]\n"
"ds_read_b64_tr_b16 v[64:65], v244 offset:36864\n"
"v_mfma_f32_16x16x32_bf16 v[20:23], v[76:79], v[196:199], v[20:23]\n"
"ds_read_b64_tr_b16 v[66:67], v244 offset:37376\n"
"global_load_lds_dwordx4 v11, s[48:49] offset:2048\n"
"s_waitcnt lgkmcnt(10)\n"
"v_mfma_f32_16x16x32_bf16 v[24:27], v[80:83], v[192:195], v[24:27]\n"
"ds_read_b64_tr_b16 v[68:69], v245 offset:36864\n"
"v_mfma_f32_16x16x32_bf16 v[28:31], v[80:83], v[196:199], v[28:31]\n"
"ds_read_b64_tr_b16 v[70:71], v245 offset:37376\n"
"s_waitcnt lgkmcnt(10)\n"
"v_mfma_f32_16x16x32_bf16 v[32:35], v[84:87], v[192:195], v[32:35]\n"
"ds_read_b64_tr_b16 v[72:73], v246 offset:36864\n"
"v_mfma_f32_16x16x32_bf16 v[36:39], v[84:87], v[196:199], v[36:39]\n"
"ds_read_b64_tr_b16 v[74:75], v246 offset:37376\n"
"s_waitcnt lgkmcnt(10)\n"
"v_mfma_f32_16x16x32_bf16 v[16:19], v[52:55], v[200:203], v[16:19]\n"
"s_waitcnt vmcnt(7) lgkmcnt(0)\n"
"s_barrier\n"
"ds_read_b64_tr_b16 v[76:77], v2 offset:0\n"
"v_mfma_f32_16x16x32_bf16 v[20:23], v[52:55], v[204:207], v[20:23]\n"
"ds_read_b64_tr_b16 v[78:79], v2 offset:512\n"
"s_add_u32 s48, s50, 0x48300\n"
"s_addc_u32 s49, s51, 0\n"
"s_add_u32 m0, s44, 0x1c000\n"
"v_mfma_f32_16x16x32_bf16 v[24:27], v[56:59], v[200:203], v[24:27]\n"
"ds_read_b64_tr_b16 v[80:81], v3 offset:0\n"
"global_load_lds_dwordx4 v6, s[48:49]\n"
"v_mfma_f32_16x16x32_bf16 v[28:31], v[56:59], v[204:207], v[28:31]\n"
"ds_read_b64_tr_b16 v[82:83], v3 offset:512\n"
"global_load_lds_dwordx4 v7, s[48:49] offset:1024\n"
"v_mfma_f32_16x16x32_bf16 v[32:35], v[60:63], v[200:203], v[32:35]\n"
"ds_read_b64_tr_b16 v[84:85], v4 offset:0\n"
"v_mfma_f32_16x16x32_bf16 v[36:39], v[60:63], v[204:207], v[36:39]\n"
"ds_read_b64_tr_b16 v[86:87], v4 offset:512\n"
"v_mfma_f32_16x16x32_bf16 v[16:19], v[64:67], v[208:211], v[16:19]\n"
"ds_read_b64_tr_b16 v[52:53], v2 offset:12288\n"
"v_mfma_f32_16x16x32_bf16 v[20:23], v[64:67], v[212:215], v[20:23]\n"
"ds_read_b64_tr_b16 v[54:55], v2 offset:12800\n"
"global_load_lds_dwordx4 v8, s[48:49] offset:2048\n"
"v_mfma_f32_16x16x32_bf16 v[24:27], v[68:71], v[208:211], v[24:27]\n"
"ds_read_b64_tr_b16 v[56:57], v3 offset:12288\n"
"s_add_u32 m0, s44, 0x1cc00\n"
"v_mfma_f32_16x16x32_bf16 v[28:31], v[68:71], v[212:215], v[28:31]\n"
"ds_read_b64_tr_b16 v[58:59], v3 offset:12800\n"
"global_load_lds_dwordx4 v9, s[48:49]\n"
"v_mfma_f32_16x16x32_bf16 v[32:35], v[72:75], v[208:211], v[32:35]\n"
"ds_read_b64_tr_b16 v[60:61], v4 offset:12288\n"
"v_mfma_f32_16x16x32_bf16 v[36:39], v[72:75], v[212:215], v[36:39]\n"
"ds_read_b64_tr_b16 v[62:63], v4 offset:12800\n"
"v_cvt_pk_bf16_f32 v40, v16, v17\n"
"v_cvt_pk_bf16_f32 v41, v18, v19\n"
"s_waitcnt lgkmcnt(10)\n"
"v_mfma_f32_16x16x32_bf16 v[16:19], v[76:79], v[88:91], 0\n"
"v_cvt_pk_bf16_f32 v44, v20, v21\n"
"v_cvt_pk_bf16_f32 v45, v22, v23\n"
"ds_read_b64_tr_b16 v[64:65], v2 offset:24576\n"
"v_mfma_f32_16x16x32_bf16 v[20:23], v[76:79], v[92:95], 0\n"
"v_cvt_pk_bf16_f32 v42, v24, v25\n"
"v_cvt_pk_bf16_f32 v43, v26, v27\n"
"ds_read_b64_tr_b16 v[66:67], v2 offset:25088\n"
"global_load_lds_dwordx4 v10, s[48:49] offset:1024\n"
"s_waitcnt lgkmcnt(10)\n"
"v_mfma_f32_16x16x32_bf16 v[24:27], v[80:83], v[88:91], 0\n"
"v_cvt_pk_bf16_f32 v46, v28, v29\n"
"v_cvt_pk_bf16_f32 v47, v30, v31\n"
"ds_read_b64_tr_b16 v[68:69], v3 offset:24576\n"
"v_mfma_f32_16x16x32_bf16 v[28:31], v[80:83], v[92:95], 0\n"
"v_cvt_pk_bf16_f32 v48, v32, v33\n"
"v_cvt_pk_bf16_f32 v49, v34, v35\n"
"ds_read_b64_tr_b16 v[70:71], v3 offset:25088\n"
"s_waitcnt lgkmcnt(10)\n"
"v_mfma_f32_16x16x32_bf16 v[32:35], v[84:87], v[88:91], 0\n"
"v_cvt_pk_bf16_f32 v50, v36, v37\n"
"v_cvt_pk_bf16_f32 v51, v38, v39\n"
"ds_read_b64_tr_b16 v[72:73], v4 offset:24576\n"
"v_mfma_f32_16x16x32_bf16 v[36:39], v[84:87], v[92:95], 0\n"
"ds_read_b64_tr_b16 v[74:75], v4 offset:25088\n"
"s_waitcnt lgkmcnt(10)\n"
"v_mfma_f32_16x16x32_bf16 v[16:19], v[52:55], v[96:99], v[16:19]\n"
"ds_read_b64_tr_b16 v[76:77], v2 offset:36864\n"
"v_permlane32_swap_b32 v40, v42\n"
"v_mfma_f32_16x16x32_bf16 v[20:23], v[52:55], v[100:103], v[20:23]\n"
"ds_read_b64_tr_b16 v[78:79], v2 offset:37376\n"
"global_load_lds_dwordx4 v11, s[48:49] offset:2048\n"
"v_permlane32_swap_b32 v41, v43\n"
"s_waitcnt lgkmcnt(10)\n"
"v_mfma_f32_16x16x32_bf16 v[24:27], v[56:59], v[96:99], v[24:27]\n"
"ds_read_b64_tr_b16 v[80:81], v3 offset:36864\n"
"v_permlane16_swap_b32 v40, v42\n"
"v_mfma_f32_16x16x32_bf16 v[28:31], v[56:59], v[100:103], v[28:31]\n"
"ds_read_b64_tr_b16 v[82:83], v3 offset:37376\n"
"v_permlane16_swap_b32 v41, v43\n"
"s_waitcnt lgkmcnt(10)\n"
"v_mfma_f32_16x16x32_bf16 v[32:35], v[60:63], v[96:99], v[32:35]\n"
"ds_read_b64_tr_b16 v[84:85], v4 offset:36864\n"
"v_mfma_f32_16x16x32_bf16 v[36:39], v[60:63], v[100:103], v[36:39]\n"
"ds_read_b64_tr_b16 v[86:87], v4 offset:37376\n"
"global_store_dwordx4 v12, v[40:43], s[52:53] offset:0 sc1\n"
"s_waitcnt lgkmcnt(10)\n"
"v_mfma_f32_16x16x32_bf16 v[16:19], v[64:67], v[104:107], v[16:19]\n"
"s_waitcnt vmcnt(7) lgkmcnt(0)\n"
"s_barrier\n"
"ds_read_b64_tr_b16 v[52:53], v247 offset:0\n"
"v_mfma_f32_16x16x32_bf16 v[20:23], v[64:67], v[108:111], v[20:23]\n"
"ds_read_b64_tr_b16 v[54:55], v247 offset:512\n"
"s_add_u32 s48, s50, 0x6c300\n"
"s_addc_u32 s49, s51, 0\n"
"s_add_u32 m0, s44, 0x10000\n"
"v_mfma_f32_16x16x32_bf16 v[24:27], v[68:71], v[104:107], v[24:27]\n"
"ds_read_b64_tr_b16 v[56:57], v248 offset:0\n"
"global_load_lds_dwordx4 v6, s[48:49]\n"
"v_mfma_f32_16x16x32_bf16 v[28:31], v[68:71], v[108:111], v[28:31]\n"
"ds_read_b64_tr_b16 v[58:59], v248 offset:512\n"
"global_load_lds_dwordx4 v7, s[48:49] offset:1024\n"
"v_mfma_f32_16x16x32_bf16 v[32:35], v[72:75], v[104:107], v[32:35]\n"
"ds_read_b64_tr_b16 v[60:61], v249 offset:0\n"
"v_mfma_f32_16x16x32_bf16 v[36:39], v[72:75], v[108:111], v[36:39]\n"
"ds_read_b64_tr_b16 v[62:63], v249 offset:512\n"
"v_mfma_f32_16x16x32_bf16 v[16:19], v[76:79], v[112:115], v[16:19]\n"
"ds_read_b64_tr_b16 v[64:65], v247 offset:12288\n"
"v_mfma_f32_16x16x32_bf16 v[20:23], v[76:79], v[116:119], v[20:23]\n"
"ds_read_b64_tr_b16 v[66:67], v247 offset:12800\n"
"global_load_lds_dwordx4 v8, s[48:49] offset:2048\n"
"v_mfma_f32_16x16x32_bf16 v[24:27], v[80:83], v[112:115], v[24:27]\n"
"ds_read_b64_tr_b16 v[68:69], v248 offset:12288\n"
"s_add_u32 m0, s44, 0x10c00\n"
"v_mfma_f32_16x16x32_bf16 v[28:31], v[80:83], v[116:119], v[28:31]\n"
"ds_read_b64_tr_b16 v[70:71], v248 offset:12800\n"
"global_load_lds_dwordx4 v9, s[48:49]\n"
"v_mfma_f32_16x16x32_bf16 v[32:35], v[84:87], v[112:115], v[32:35]\n"
"ds_read_b64_tr_b16 v[72:73], v249 offset:12288\n"
"v_mfma_f32_16x16x32_bf16 v[36:39], v[84:87], v[116:119], v[36:39]\n"
"ds_read_b64_tr_b16 v[74:75], v249 offset:12800\n"
"s_waitcnt lgkmcnt(10)\n"
"v_mfma_f32_16x16x32_bf16 v[16:19], v[52:55], v[120:123], v[16:19]\n"
"ds_read_b64_tr_b16 v[76:77], v247 offset:24576\n"
"v_mfma_f32_16x16x32_bf16 v[20:23], v[52:55], v[124:127], v[20:23]\n"
"ds_read_b64_tr_b16 v[78:79], v247 offset:25088\n"
"global_load_lds_dwordx4 v10, s[48:49] offset:1024\n"
"v_lshlrev_b32 v92, 7, v1\n"
"s_add_u32 s36, s4, 0xc000\n"
"s_addc_u32 s37, s5, 0\n"
"s_cmp_eq_u32 s16, 0\n"
"s_cselect_b64 exec, -1, 0\n"
"global_load_dwordx4 v[88:91], v92, s[36:37] offset:0 sc1\n"
"s_mov_b64 exec, -1\n"
"s_waitcnt lgkmcnt(10)\n"
"v_mfma_f32_16x16x32_bf16 v[24:27], v[56:59], v[120:123], v[24:27]\n"
"ds_read_b64_tr_b16 v[80:81], v248 offset:24576\n"
"v_mfma_f32_16x16x32_bf16 v[28:31], v[56:59], v[124:127], v[28:31]\n"
"ds_read_b64_tr_b16 v[82:83], v248 offset:25088\n"
"s_waitcnt lgkmcnt(10)\n"
"v_mfma_f32_16x16x32_bf16 v[32:35], v[60:63], v[120:123], v[32:35]\n"
"ds_read_b64_tr_b16 v[84:85], v249 offset:24576\n"
"v_mfma_f32_16x16x32_bf16 v[36:39], v[60:63], v[124:127], v[36:39]\n"
"ds_read_b64_tr_b16 v[86:87], v249 offset:25088\n"
"s_waitcnt lgkmcnt(10)\n"
"v_mfma_f32_16x16x32_bf16 v[16:19], v[64:67], v[128:131], v[16:19]\n"
"ds_read_b64_tr_b16 v[52:53], v247 offset:36864\n"
"v_permlane32_swap_b32 v44, v46\n"
"v_mfma_f32_16x16x32_bf16 v[20:23], v[64:67], v[132:135], v[20:23]\n"
"ds_read_b64_tr_b16 v[54:55], v247 offset:37376\n"
"global_load_lds_dwordx4 v11, s[48:49] offset:2048\n"
"v_permlane32_swap_b32 v45, v47\n"
"s_waitcnt lgkmcnt(10)\n"
"v_mfma_f32_16x16x32_bf16 v[24:27], v[68:71], v[128:131], v[24:27]\n"
"ds_read_b64_tr_b16 v[56:57], v248 offset:36864\n"
"v_permlane16_swap_b32 v44, v46\n"
"v_mfma_f32_16x16x32_bf16 v[28:31], v[68:71], v[132:135], v[28:31]\n"
"ds_read_b64_tr_b16 v[58:59], v248 offset:37376\n"
"v_permlane16_swap_b32 v45, v47\n"
"s_waitcnt lgkmcnt(10)\n"
"v_mfma_f32_16x16x32_bf16 v[32:35], v[72:75], v[128:131], v[32:35]\n"
"ds_read_b64_tr_b16 v[60:61], v249 offset:36864\n"
"v_mfma_f32_16x16x32_bf16 v[36:39], v[72:75], v[132:135], v[36:39]\n"
"ds_read_b64_tr_b16 v[62:63], v249 offset:37376\n"
"global_store_dwordx4 v12, v[44:47], s[54:55] offset:0 sc1\n"
"s_waitcnt lgkmcnt(10)\n"
"v_mfma_f32_16x16x32_bf16 v[16:19], v[76:79], v[136:139], v[16:19]\n"
"s_waitcnt vmcnt(8) lgkmcnt(0)\n"
"s_barrier\n"
"ds_read_b64_tr_b16 v[64:65], v244 offset:0\n"
"v_mfma_f32_16x16x32_bf16 v[20:23], v[76:79], v[140:143], v[20:23]\n"
"ds_read_b64_tr_b16 v[66:67], v244 offset:512\n"
"v_mfma_f32_16x16x32_bf16 v[24:27], v[80:83], v[136:139], v[24:27]\n"
"ds_read_b64_tr_b16 v[68:69], v245 offset:0\n"
"v_mfma_f32_16x16x32_bf16 v[28:31], v[80:83], v[140:143], v[28:31]\n"
"ds_read_b64_tr_b16 v[70:71], v245 offset:512\n"
"v_mfma_f32_16x16x32_bf16 v[32:35], v[84:87], v[136:139], v[32:35]\n"
"ds_read_b64_tr_b16 v[72:73], v246 offset:0\n"
"v_mfma_f32_16x16x32_bf16 v[36:39], v[84:87], v[140:143], v[36:39]\n"
"ds_read_b64_tr_b16 v[74:75], v246 offset:512\n"
"v_mfma_f32_16x16x32_bf16 v[16:19], v[52:55], v[144:147], v[16:19]\n"
"ds_read_b64_tr_b16 v[76:77], v244 offset:12288\n"
"v_mfma_f32_16x16x32_bf16 v[20:23], v[52:55], v[148:151], v[20:23]\n"
"ds_read_b64_tr_b16 v[78:79], v244 offset:12800\n"
"v_mfma_f32_16x16x32_bf16 v[24:27], v[56:59], v[144:147], v[24:27]\n"
"ds_read_b64_tr_b16 v[80:81], v245 offset:12288\n"
"v_mfma_f32_16x16x32_bf16 v[28:31], v[56:59], v[148:151], v[28:31]\n"
"ds_read_b64_tr_b16 v[82:83], v245 offset:12800\n"
"v_mfma_f32_16x16x32_bf16 v[32:35], v[60:63], v[144:147], v[32:35]\n"
"ds_read_b64_tr_b16 v[84:85], v246 offset:12288\n"
"v_mfma_f32_16x16x32_bf16 v[36:39], v[60:63], v[148:151], v[36:39]\n"
"ds_read_b64_tr_b16 v[86:87], v246 offset:12800\n"
"s_waitcnt lgkmcnt(10)\n"
"v_mfma_f32_16x16x32_bf16 v[16:19], v[64:67], v[152:155], v[16:19]\n"
"ds_read_b64_tr_b16 v[52:53], v244 offset:24576\n"
"v_mfma_f32_16x16x32_bf16 v[20:23], v[64:67], v[156:159], v[20:23]\n"
"ds_read_b64_tr_b16 v[54:55], v244 offset:25088\n"
"s_waitcnt lgkmcnt(10)\n"
"v_mfma_f32_16x16x32_bf16 v[24:27], v[68:71], v[152:155], v[24:27]\n"
"ds_read_b64_tr_b16 v[56:57], v245 offset:24576\n"
"v_mfma_f32_16x16x32_bf16 v[28:31], v[68:71], v[156:159], v[28:31]\n"
"ds_read_b64_tr_b16 v[58:59], v245 offset:25088\n"
"s_waitcnt lgkmcnt(10)\n"
"v_mfma_f32_16x16x32_bf16 v[32:35], v[72:75], v[152:155], v[32:35]\n"
"ds_read_b64_tr_b16 v[60:61], v246 offset:24576\n"
"v_mfma_f32_16x16x32_bf16 v[36:39], v[72:75], v[156:159], v[36:39]\n"
"ds_read_b64_tr_b16 v[62:63], v246 offset:25088\n"
"s_waitcnt lgkmcnt(10)\n"
"v_mfma_f32_16x16x32_bf16 v[16:19], v[76:79], v[160:163], v[16:19]\n"
"ds_read_b64_tr_b16 v[64:65], v244 offset:36864\n"
"v_permlane16_swap_b32 v48, v50\n"
"v_mfma_f32_16x16x32_bf16 v[20:23], v[76:79], v[164:167], v[20:23]\n"
"ds_read_b64_tr_b16 v[66:67], v244 offset:37376\n"
"v_permlane16_swap_b32 v49, v51\n"
"s_waitcnt lgkmcnt(10)\n"
"v_mfma_f32_16x16x32_bf16 v[24:27], v[80:83], v[160:163], v[24:27]\n"
"ds_read_b64_tr_b16 v[68:69], v245 offset:36864\n"
"v_mfma_f32_16x16x32_bf16 v[28:31], v[80:83], v[164:167], v[28:31]\n"
"ds_read_b64_tr_b16 v[70:71], v245 offset:37376\n"
"s_waitcnt lgkmcnt(10)\n"
"v_mfma_f32_16x16x32_bf16 v[32:35], v[84:87], v[160:163], v[32:35]\n"
"ds_read_b64_tr_b16 v[72:73], v246 offset:36864\n"
"v_mfma_f32_16x16x32_bf16 v[36:39], v[84:87], v[164:167], v[36:39]\n"
"ds_read_b64_tr_b16 v[74:75], v246 offset:37376\n"
"global_store_dwordx4 v13, v[48:51], s[52:53] offset:0 sc1\n"
"s_waitcnt vmcnt(3)\n"
"s_cmp_lg_u32 s16, 0\ns_cbranch_scc1 TW_OK2\nv_xor_b32 v88, s15, v88\nv_xor_b32 v89, s15, v89\nv_xor_b32 v90, s15, v90\nv_xor_b32 v91, s15, v91\nv_or3_b32 v88, v88, v89, v90\nv_or_b32 v88, v88, v91\nv_cmp_ne_u32 vcc, 0, v88\ns_nop 1\ns_cmp_eq_u64 vcc, 0\ns_cbranch_scc1 TW_OK2\ns_mov_b32 s42, 0x40000\nTW2:\nglobal_load_dwordx4 v[88:91], v92, s[36:37] sc1\ns_waitcnt vmcnt(0)\nv_xor_b32 v88, s15, v88\nv_xor_b32 v89, s15, v89\nv_xor_b32 v90, s15, v90\nv_xor_b32 v91, s15, v91\nv_or3_b32 v88, v88, v89, v90\nv_or_b32 v88, v88, v91\nv_cmp_ne_u32 vcc, 0, v88\ns_sub_u32 s42, s42, 1\ns_nop 1\ns_cselect_b64 vcc, 0, vcc\ns_cmp_eq_u64 vcc, 0\ns_cbranch_scc1 TW_OK2\ns_sleep 1\ns_branch TW2\nTW_OK2:\n"
"v_mov_b32 v88, 0x4000\n"
"v_mov_b32 v89, s15\n"
"s_or_b32 s87, s14, s16\ns_cmp_eq_u32 s87, 0\ns_cselect_b64 exec, 1, 0\n"
"global_store_dword v88, v89, s[4:5] offset:0 sc1\n"
"s_mov_b64 exec, -1\n"
"s_waitcnt lgkmcnt(10)\n"
"v_mfma_f32_16x16x32_bf16 v[16:19], v[52:55], v[168:171], v[16:19]\n"
"s_waitcnt vmcnt(2) lgkmcnt(0)\n"
"s_barrier\n"
"ds_read_b64_tr_b16 v[76:77], v2 offset:0\n"
"v_lshrrev_b32 v130, 4, v1\n"
"v_and_b32 v131, 15, v1\n"
"v_bfe_u32 v132, v1, 3, 1\n"
"v_mfma_f32_16x16x32_bf16 v[20:23], v[52:55], v[172:175], v[20:23]\n"
"ds_read_b64_tr_b16 v[78:79], v2 offset:512\n"
"s_mul_i32 s40, s16, 0x180\n"
"s_add_u32 s40, s40, 0xc00\n"
"s_mov_b32 s41, 0x12000\n"
"v_mfma_f32_16x16x32_bf16 v[24:27], v[56:59], v[168:171], v[24:27]\n"
"ds_read_b64_tr_b16 v[80:81], v3 offset:0\n"
"v_mul_u32_u24 v133, s41, v130\n"
"v_add_u32 v133, s40, v133\n"
"v_lshl_add_u32 v144, v131, 4, v133\n"
"v_mfma_f32_16x16x32_bf16 v[28:31], v[56:59], v[172:175], v[28:31]\n"
"ds_read_b64_tr_b16 v[82:83], v3 offset:512\n"
"s_mov_b32 s41, 0x47f80\n"
"v_mul_u32_u24 v133, s41, v132\n"
"v_add_u32 v133, 0x100, v133\n"
"v_mfma_f32_16x16x32_bf16 v[32:35], v[60:63], v[168:171], v[32:35]\n"
"ds_read_b64_tr_b16 v[84:85], v4 offset:0\n"
"v_add_u32 v145, v133, v144\n"
"s_mul_i32 s40, s18, 0x90000\n"
"s_mul_i32 s41, s19, 0x2400\n"
"v_mfma_f32_16x16x32_bf16 v[36:39], v[60:63], v[172:175], v[36:39]\n"
"ds_read_b64_tr_b16 v[86:87], v4 offset:512\n"
"s_add_u32 s40, s40, s41\n"
"v_mfma_f32_16x16x32_bf16 v[16:19], v[64:67], v[176:179], v[16:19]\n"
"ds_read_b64_tr_b16 v[52:53], v2 offset:12288\n"
"s_add_u32 s40, s40, 0xa8e000\n"
"s_add_u32 s20, s4, s40\n"
"s_addc_u32 s21, s5, 0\n"
"v_mfma_f32_16x16x32_bf16 v[20:23], v[64:67], v[180:183], v[20:23]\n"
"ds_read_b64_tr_b16 v[54:55], v2 offset:12800\n"
"s_add_u32 s22, s20, 0x48000\n"
"s_addc_u32 s23, s21, 0\n"
"v_and_b32 v133, 1, v131\n"
"v_mfma_f32_16x16x32_bf16 v[24:27], v[68:71], v[176:179], v[24:27]\n"
"ds_read_b64_tr_b16 v[56:57], v3 offset:12288\n"
"v_lshlrev_b32 v133, 4, v133\n"
"v_mov_b32 v155, 0x3f80\n"
"v_lshlrev_b32 v133, v133, v155\n"
"v_mfma_f32_16x16x32_bf16 v[28:31], v[68:71], v[180:183], v[28:31]\n"
"ds_read_b64_tr_b16 v[58:59], v3 offset:12800\n"
"v_lshrrev_b32 v155, 1, v131\n"
"v_cmp_eq_u32 vcc, 0, v155\ns_nop 1\nv_cndmask_b32 v136, 0, v133, vcc\n"
"v_cmp_eq_u32 vcc, 1, v155\ns_nop 1\nv_cndmask_b32 v137, 0, v133, vcc\n"
"v_mfma_f32_16x16x32_bf16 v[32:35], v[72:75], v[176:179], v[32:35]\n"
"ds_read_b64_tr_b16 v[60:61], v4 offset:12288\n"
"v_cmp_eq_u32 vcc, 2, v155\ns_nop 1\nv_cndmask_b32 v138, 0, v133, vcc\n"
"v_cmp_eq_u32 vcc, 3, v155\ns_nop 1\nv_cndmask_b32 v139, 0, v133, vcc\n"
"v_cmp_eq_u32 vcc, 4, v155\ns_nop 1\nv_cndmask_b32 v140, 0, v133, vcc\n"
"v_mfma_f32_16x16x32_bf16 v[36:39], v[72:75], v[180:183], v[36:39]\n"
"ds_read_b64_tr_b16 v[62:63], v4 offset:12800\n"
"v_cmp_eq_u32 vcc, 5, v155\ns_nop 1\nv_cndmask_b32 v141, 0, v133, vcc\n"
"s_waitcnt lgkmcnt(10)\n"
"v_mfma_f32_16x16x32_bf16 v[16:19], v[76:79], v[184:187], v[16:19]\n"
"ds_read_b64_tr_b16 v[64:65], v2 offset:24576\n"
"v_cmp_eq_u32 vcc, 6, v155\ns_nop 1\nv_cndmask_b32 v142, 0, v133, vcc\n"
"v_cmp_eq_u32 vcc, 7, v155\ns_nop 1\nv_cndmask_b32 v143, 0, v133, vcc\n"
"v_lshrrev_b32 v133, 1, v130\n"
"v_mfma_f32_16x16x32_bf16 v[20:23], v[76:79], v[188:191], v[20:23]\n"
"ds_read_b64_tr_b16 v[66:67], v2 offset:25088\n"
"v_and_b32 v148, v133, v132\n"
"v_lshlrev_b32 v133, 1, v133\n"
"v_max_u32 v133, v133, v132\n"
"s_waitcnt lgkmcnt(10)\n"
"v_mfma_f32_16x16x32_bf16 v[24:27], v[80:83], v[184:187], v[24:27]\n"
"ds_read_b64_tr_b16 v[68:69], v3 offset:24576\n"
"s_mul_i32 s40, s16, 3\n"
"v_add_u32 v146, s40, v133\n"
"v_and_b32 v147, 1, v130\n"
"v_mfma_f32_16x16x32_bf16 v[28:31], v[80:83], v[188:191], v[28:31]\n"
"ds_read_b64_tr_b16 v[70:71], v3 offset:25088\n"
"v_xor_b32 v148, 1, v148\n"
"v_cvt_f32_u32 v148, v148\n"
"v_and_b32 v132, 7, v1\n"
"s_waitcnt lgkmcnt(10)\n"
"v_mfma_f32_16x16x32_bf16 v[32:35], v[84:87], v[184:187], v[32:35]\n"
"ds_read_b64_tr_b16 v[72:73], v4 offset:24576\n"
"s_lshl_b32 s40, s18, 9\n"
"s_add_u32 s28, s4, s40\n"
"s_addc_u32 s29, s5, 0\n"
"v_mfma_f32_16x16x32_bf16 v[36:39], v[84:87], v[188:191], v[36:39]\n"
"ds_read_b64_tr_b16 v[74:75], v4 offset:25088\n"
"v_mov_b32 v149, 0\n"
"s_waitcnt lgkmcnt(10)\n"
"v_mfma_f32_16x16x32_bf16 v[16:19], v[52:55], v[192:195], v[16:19]\n"
"ds_read_b64_tr_b16 v[76:77], v2 offset:36864\n"
"v_mov_b32 v150, 1\n"
"s_mul_i32 s40, s14, 0x6000\n"
"s_add_u32 s40, s40, 0x48e000\n"
"v_mfma_f32_16x16x32_bf16 v[20:23], v[52:55], v[196:199], v[20:23]\n"
"ds_read_b64_tr_b16 v[78:79], v2 offset:37376\n"
"s_add_u32 s34, s4, s40\n"
"s_addc_u32 s35, s5, 0\n"
"s_mul_i32 s40, s16, 0xc00\n"
"s_waitcnt lgkmcnt(10)\n"
"v_mfma_f32_16x16x32_bf16 v[24:27], v[56:59], v[192:195], v[24:27]\n"
"ds_read_b64_tr_b16 v[80:81], v3 offset:36864\n"
"v_lshl_add_u32 v151, v1, 4, s40\n"
"s_lshl_b32 s40, s14, 3\n"
"v_add_u32 v130, s40, v132\n"
"v_mfma_f32_16x16x32_bf16 v[28:31], v[56:59], v[196:199], v[28:31]\n"
"ds_read_b64_tr_b16 v[82:83], v3 offset:37376\n"
"v_mul_u32_u24 v130, 0x480, v130\n"
"v_lshl_add_u32 v130, v146, 4, v130\n"
"v_lshlrev_b32 v131, 5, v146\n"
"s_waitcnt lgkmcnt(10)\n"
"v_mfma_f32_16x16x32_bf16 v[32:35], v[60:63], v[192:195], v[32:35]\n"
"ds_read_b64_tr_b16 v[84:85], v4 offset:36864\n"
"v_lshl_add_u32 v131, v147, 4, v131\n"
"v_lshl_add_u32 v130, v147, 3, v130\n"
"global_load_dwordx2 v[124:125], v130, s[6:7] offset:0\n"
"v_mfma_f32_16x16x32_bf16 v[36:39], v[60:63], v[196:199], v[36:39]\n"
"ds_read_b64_tr_b16 v[86:87], v4 offset:37376\n"
"global_load_dwordx2 v[126:127], v130, s[6:7] offset:384\n"
"s_waitcnt lgkmcnt(10)\n"
"v_mfma_f32_16x16x32_bf16 v[16:19], v[64:67], v[200:203], v[16:19]\n"
"global_load_dwordx2 v[128:129], v130, s[6:7] offset:768\n"
"s_add_u32 s36, s4, 0x8680\n"
"s_addc_u32 s37, s5, 0\n"
"v_mfma_f32_16x16x32_bf16 v[20:23], v[64:67], v[204:207], v[20:23]\n"
"global_load_dwordx4 v[88:91], v131, s[36:37] offset:-3072 sc1\n"
"global_load_dwordx4 v[92:95], v131, s[36:37] offset:-2304 sc1\n"
"global_load_dwordx4 v[96:99], v131, s[36:37] offset:-1536 sc1\n"
"s_waitcnt lgkmcnt(8)\n"
"v_mfma_f32_16x16x32_bf16 v[24:27], v[68:71], v[200:203], v[24:27]\n"
"global_load_dwordx4 v[100:103], v131, s[36:37] offset:-768 sc1\n"
"global_load_dwordx4 v[104:107], v131, s[36:37] offset:0 sc1\n"
"global_load_dwordx4 v[108:111], v131, s[36:37] offset:768 sc1\n"
"v_mfma_f32_16x16x32_bf16 v[28:31], v[68:71], v[204:207], v[28:31]\n"
"global_load_dwordx4 v[112:115], v131, s[36:37] offset:1536 sc1\n"
"global_load_dwordx4 v[116:119], v131, s[36:37] offset:2304 sc1\n"
"global_load_dwordx4 v[120:123], v131, s[36:37] offset:3072 sc1\n"
"s_waitcnt lgkmcnt(6)\n"
"v_mfma_f32_16x16x32_bf16 v[32:35], v[72:75], v[200:203], v[32:35]\n"
"v_lshlrev_b32 v133, 6, v132\n"
"v_add_u32 v154, 0x27000, v133\n"
"s_lshl_b32 s40, s19, 3\n"
"v_mfma_f32_16x16x32_bf16 v[36:39], v[72:75], v[204:207], v[36:39]\n"
"v_add_u32 v130, s40, v132\n"
"s_waitcnt lgkmcnt(4)\n"
"v_mfma_f32_16x16x32_bf16 v[16:19], v[76:79], v[208:211], v[16:19]\n"
"v_lshrrev_b32 v131, 2, v130\n"
"v_sub_u32 v131, 0, v131\n"
"v_xor_b32 v131, v131, v146\n"
"v_mfma_f32_16x16x32_bf16 v[20:23], v[76:79], v[212:215], v[20:23]\n"
"v_and_b32 v131, 3, v131\n"
"v_lshlrev_b32 v131, 4, v131\n"
"v_lshl_or_b32 v130, v130, 6, v131\n"
"s_waitcnt lgkmcnt(2)\n"
"v_mfma_f32_16x16x32_bf16 v[24:27], v[80:83], v[208:211], v[24:27]\n"
"v_lshrrev_b32 v131, 2, v146\n"
"v_lshl_add_u32 v130, v131, 12, v130\n"
"v_add_u32 v153, 0xc000, v130\n"
"v_mfma_f32_16x16x32_bf16 v[28:31], v[80:83], v[212:215], v[28:31]\n"
"v_mul_u32_u24 v131, 0x6000, v147\n"
"v_add_u32 v152, v130, v131\n"
"s_mul_i32 s40, s18, 0x12000\n"
"s_waitcnt lgkmcnt(0)\n"
"v_mfma_f32_16x16x32_bf16 v[32:35], v[84:87], v[208:211], v[32:35]\n"
"s_add_u32 s40, s40, 0xe000\n"
"s_add_u32 s26, s4, s40\n"
"s_addc_u32 s27, s5, 0\n"
"v_mfma_f32_16x16x32_bf16 v[36:39], v[84:87], v[212:215], v[36:39]\n"
"v_cvt_pk_bf16_f32 v40, v16, v17\n"
"v_cvt_pk_bf16_f32 v41, v18, v19\n"
"v_cvt_pk_bf16_f32 v42, v24, v25\n"
"v_cvt_pk_bf16_f32 v43, v26, v27\n"
"v_cvt_pk_bf16_f32 v44, v20, v21\n"
"v_cvt_pk_bf16_f32 v45, v22, v23\n"
"v_cvt_pk_bf16_f32 v46, v28, v29\n"
"v_cvt_pk_bf16_f32 v47, v30, v31\n"
"v_cvt_pk_bf16_f32 v48, v32, v33\n"
"v_cvt_pk_bf16_f32 v49, v34, v35\n"
"v_cvt_pk_bf16_f32 v50, v36, v37\n"
"v_cvt_pk_bf16_f32 v51, v38, v39\n"
"v_permlane32_swap_b32 v40, v42\n"
"v_permlane32_swap_b32 v41, v43\n"
"v_permlane32_swap_b32 v44, v46\n"
"v_permlane32_swap_b32 v45, v47\n"
"v_permlane16_swap_b32 v48, v50\n"
"v_permlane16_swap_b32 v49, v51\n"
"global_store_dwordx4 v13, v[48:51], s[52:53] offset:3072 sc1\n"
"v_permlane16_swap_b32 v40, v42\n"
"v_permlane16_swap_b32 v41, v43\n"
"global_store_dwordx4 v12, v[40:43], s[52:53] offset:3072 sc1\n"
"v_permlane16_swap_b32 v44, v46\n"
"v_permlane16_swap_b32 v45, v47\n"
"global_store_dwordx4 v12, v[44:47], s[54:55] offset:3072 sc1\n"
"s_waitcnt vmcnt(0) lgkmcnt(0)\n"
"s_barrier\n"
"s_cmp_eq_u32 s16, 0\n"
"s_cselect_b64 exec, 1, 0\n"
"global_atomic_add v155, v149, v150, s[28:29] offset:0 sc0\n"
"s_mov_b64 exec, -1\n"
"s_mov_b32 s42, s17\n"
"s_lshl_b32 s40, s42, 2\n"
"s_mul_i32 s41, s42, 5\n"
"s_add_u32 s41, s41, 16\n"
"s_cmp_lt_u32 s16, 4\n"
"s_cselect_b32 s83, s40, s41\n"
"s_lshl_b32 s82, s83, 10\n"
"v_lshrrev_b32 v246, 3, v1\n"
"v_and_b32 v247, 2, v246\n"
"v_and_b32 v248, 7, v1\n"
"v_xor_b32 v247, v247, v248\n"
"v_lshlrev_b32 v247, 4, v247\n"
"v_mul_u32_u24 v246, 0x2400, v246\n"
"v_add_u32 v246, v246, v247\n"
"s_add_u32 s40, s83, 0\n"
"s_min_u32 s40, s40, 35\n"
"s_mul_i32 s41, s40, 57\n"
"s_lshr_b32 s41, s41, 9\n"
"s_mul_i32 s42, s41, 9\n"
"s_sub_u32 s40, s40, s42\n"
"s_lshl_b32 s40, s40, 7\n"
"s_mul_i32 s42, s41, 0x12000\n"
"s_add_u32 s40, s40, s42\n"
"s_add_u32 s40, s40, 0x1000\n"
"s_and_b32 s41, s41, 1\n"
"s_lshl_b32 s41, s41, 6\n"
"v_xor_b32 v247, s41, v246\n"
"v_add_u32 v235, s40, v247\n"
"s_add_u32 s40, s83, 1\n"
"s_min_u32 s40, s40, 35\n"
"s_mul_i32 s41, s40, 57\n"
"s_lshr_b32 s41, s41, 9\n"
"s_mul_i32 s42, s41, 9\n"
"s_sub_u32 s40, s40, s42\n"
"s_lshl_b32 s40, s40, 7\n"
"s_mul_i32 s42, s41, 0x12000\n"
"s_add_u32 s40, s40, s42\n"
"s_add_u32 s40, s40, 0xc00\n"
"s_and_b32 s41, s41, 1\n"
"s_lshl_b32 s41, s41, 6\n"
"v_xor_b32 v247, s41, v246\n"
"v_add_u32 v236, s40, v247\n"
"s_add_u32 s40, s83, 2\n"
"s_min_u32 s40, s40, 35\n"
"s_mul_i32 s41, s40, 57\n"
"s_lshr_b32 s41, s41, 9\n"
"s_mul_i32 s42, s41, 9\n"
"s_sub_u32 s40, s40, s42\n"
"s_lshl_b32 s40, s40, 7\n"
"s_mul_i32 s42, s41, 0x12000\n"
"s_add_u32 s40, s40, s42\n"
"s_add_u32 s40, s40, 0x800\n"
"s_and_b32 s41, s41, 1\n"
"s_lshl_b32 s41, s41, 6\n"
"v_xor_b32 v247, s41, v246\n"
"v_add_u32 v237, s40, v247\n"
"s_add_u32 s40, s83, 3\n"
"s_min_u32 s40, s40, 35\n"
"s_mul_i32 s41, s40, 57\n"
"s_lshr_b32 s41, s41, 9\n"
"s_mul_i32 s42, s41, 9\n"
"s_sub_u32 s40, s40, s42\n"
"s_lshl_b32 s40, s40, 7\n"
"s_mul_i32 s42, s41, 0x12000\n"
"s_add_u32 s40, s40, s42\n"
"s_add_u32 s40, s40, 0x400\n"
"s_and_b32 s41, s41, 1\n"
"s_lshl_b32 s41, s41, 6\n"
"v_xor_b32 v247, s41, v246\n"
"v_add_u32 v238, s40, v247\n"
"s_add_u32 s40, s83, 4\n"
"s_min_u32 s40, s40, 35\n"
"s_mul_i32 s41, s40, 57\n"
"s_lshr_b32 s41, s41, 9\n"
"s_mul_i32 s42, s41, 9\n"
"s_sub_u32 s40, s40, s42\n"
"s_lshl_b32 s40, s40, 7\n"
"s_mul_i32 s42, s41, 0x12000\n"
"s_add_u32 s40, s40, s42\n"
"s_add_u32 s40, s40, 0x1000\n"
"s_and_b32 s41, s41, 1\n"
"s_lshl_b32 s41, s41, 6\n"
"v_xor_b32 v247, s41, v246\n"
"v_add_u32 v239, s40, v247\n"
"s_mul_i32 s40, s19, 0x480\n"
"s_add_u32 s88, s64, s40\n"
"s_addc_u32 s89, s65, 0\n"
"s_sub_u32 s88, s88, 0x1000\n"
"s_subb_u32 s89, s89, 0\n"
"s_add_u32 s90, s66, s40\n"
"s_addc_u32 s91, s67, 0\n"
"s_sub_u32 s90, s90, 0x1000\n"
"s_subb_u32 s91, s91, 0\n"
"s_lshl_b32 s84, s17, 10\n"
"v_lshl_add_u32 v240, v1, 4, s84\n"
"s_mul_i32 s40, s18, 0x12000\n"
"s_add_u32 s40, s40, 0xe000\n"
"s_add_u32 s80, s4, s40\n"
"s_addc_u32 s81, s5, 0\n"
"s_cmp_eq_u32 s16, 0\ns_cbranch_scc1 WD3\n"
"s_mov_b32 m0, s82\n"
"s_nop 0\n"
"global_load_lds_dwordx4 v235, s[88:89]\n"
"global_load_lds_dwordx4 v236, s[88:89] offset:1024\n"
"global_load_lds_dwordx4 v237, s[88:89] offset:2048\n"
"global_load_lds_dwordx4 v238, s[88:89] offset:3072\n"
"s_cmp_ge_u32 s16, 4\n"
"s_cselect_b64 exec, -1, 0\n"
"s_add_u32 m0, s82, 0x1000\n"
"s_nop 0\n"
"global_load_lds_dwordx4 v239, s[88:89]\n"
"s_mov_b64 exec, -1\n"
"s_add_u32 s88, s88, 0x48000\n"
"s_addc_u32 s89, s89, 0\n"
"s_add_u32 m0, s82, 0x9000\n"
"s_nop 0\n"
"global_load_lds_dwordx4 v235, s[90:91]\n"
"global_load_lds_dwordx4 v236, s[90:91] offset:1024\n"
"global_load_lds_dwordx4 v237, s[90:91] offset:2048\n"
"global_load_lds_dwordx4 v238, s[90:91] offset:3072\n"
"s_cmp_ge_u32 s16, 4\n"
"s_cselect_b64 exec, -1, 0\n"
"s_add_u32 m0, s82, 0xa000\n"
"s_nop 0\n"
"global_load_lds_dwordx4 v239, s[90:91]\n"
"s_mov_b64 exec, -1\n"
"s_add_u32 s90, s90, 0x48000\n"
"s_addc_u32 s91, s91, 0\n"
"WD3:\n"
"v_lshlrev_b32 v160, 16, v124\n"
"v_and_b32 v161, 0xffff0000, v124\n"
"v_lshlrev_b32 v162, 16, v125\n"
"v_and_b32 v163, 0xffff0000, v125\n"
"v_lshlrev_b32 v164, 16, v126\n"
"v_and_b32 v165, 0xffff0000, v126\n"
"v_lshlrev_b32 v166, 16, v127\n"
"v_and_b32 v167, 0xffff0000, v127\n"
"v_lshlrev_b32 v168, 16, v128\n"
"v_and_b32 v169, 0xffff0000, v128\n"
"v_lshlrev_b32 v170, 16, v129\n"
"v_and_b32 v171, 0xffff0000, v129\n"
"s_lshl_b32 s40, s16, 3\n"
"v_add_u32 v36, s40, v154\n"
"s_waitcnt vmcnt(0)\n"
"v_readfirstlane_b32 s40, v155\n"
"s_nop 1\n"
"s_cmp_lg_u32 s16, 0\ns_cbranch_scc1 CW_OK4\ns_add_u32 s40, s40, 1\ns_cmp_eq_u32 s40, s94\ns_cbranch_scc1 CW_OK4\ns_mov_b32 s42, 0x40000\ns_mov_b64 exec, 1\nCW4:\ns_sleep 1\nglobal_atomic_add v155, v149, v149, s[28:29] offset:0 sc0\ns_waitcnt vmcnt(0)\nv_readfirstlane_b32 s40, v155\ns_sub_u32 s42, s42, 1\ns_cselect_b32 s40, s94, s40\ns_cmp_lg_u32 s40, s94\ns_cbranch_scc1 CW4\ns_mov_b64 exec, -1\nCW_OK4:\n"
"s_barrier\n"
"global_load_dwordx4 v[52:55], v144, s[20:21] offset:-3072 sc1 nt\n"
"global_load_dwordx4 v[56:59], v144, s[22:23] offset:-3072 sc1 nt\n"
"global_load_dwordx4 v[60:63], v145, s[20:21] offset:-3072 sc1 nt\n"
"global_load_dwordx4 v[64:67], v144, s[20:21] offset:0 sc1 nt\n"
"global_load_dwordx4 v[68:71], v144, s[22:23] offset:0 sc1 nt\n"
"global_load_dwordx4 v[72:75], v145, s[20:21] offset:0 sc1 nt\n"
"global_load_dwordx4 v[76:79], v144, s[20:21] offset:3072 sc1 nt\n"
"global_load_dwordx4 v[80:83], v144, s[22:23] offset:3072 sc1 nt\n"
"global_load_dwordx4 v[84:87], v145, s[20:21] offset:3072 sc1 nt\n"
"s_waitcnt vmcnt(8)\n"
"v_mfma_f32_16x16x32_bf16 v[16:19], v[136:139], v[52:55], 0\n"
"s_waitcnt vmcnt(7)\n"
"v_mfma_f32_16x16x32_bf16 v[16:19], v[136:139], v[56:59], v[16:19]\n"
"s_waitcnt vmcnt(6)\n"
"v_mfma_f32_16x16x32_bf16 v[16:19], v[140:143], v[60:63], v[16:19]\n"
"s_waitcnt vmcnt(5)\n"
"v_mfma_f32_16x16x32_bf16 v[20:23], v[136:139], v[64:67], 0\n"
"s_waitcnt vmcnt(4)\n"
"v_mfma_f32_16x16x32_bf16 v[20:23], v[136:139], v[68:71], v[20:23]\n"
"s_waitcnt vmcnt(3)\n"
"v_mfma_f32_16x16x32_bf16 v[20:23], v[140:143], v[72:75], v[20:23]\n"
"s_waitcnt vmcnt(2)\n"
"v_mfma_f32_16x16x32_bf16 v[24:27], v[136:139], v[76:79], 0\n"
"s_waitcnt vmcnt(1)\n"
"v_mfma_f32_16x16x32_bf16 v[24:27], v[136:139], v[80:83], v[24:27]\n"
"s_waitcnt vmcnt(0)\n"
"v_mfma_f32_16x16x32_bf16 v[24:27], v[140:143], v[84:87], v[24:27]\n"
"v_add_f32_dpp v16, v16, v16 row_ror:8 row_mask:0xc bank_mask:0xf\n"
"v_add_f32_dpp v17, v17, v17 row_ror:8 row_mask:0xc bank_mask:0xf\n"
"v_add_f32_dpp v18, v18, v18 row_ror:8 row_mask:0xc bank_mask:0xf\n"
"v_add_f32_dpp v19, v19, v19 row_ror:8 row_mask:0xc bank_mask:0xf\n"
"v_add_f32_dpp v20, v20, v20 row_ror:8 row_mask:0xc bank_mask:0xf\n"
"v_add_f32_dpp v21, v21, v21 row_ror:8 row_mask:0xc bank_mask:0xf\n"
"v_add_f32_dpp v22, v22, v22 row_ror:8 row_mask:0xc bank_mask:0xf\n"
"v_add_f32_dpp v23, v23, v23 row_ror:8 row_mask:0xc bank_mask:0xf\n"
"v_add_f32_dpp v24, v24, v24 row_ror:8 row_mask:0xc bank_mask:0xf\n"
"v_add_f32_dpp v25, v25, v25 row_ror:8 row_mask:0xc bank_mask:0xf\n"
"v_add_f32_dpp v26, v26, v26 row_ror:8 row_mask:0xc bank_mask:0xf\n"
"v_add_f32_dpp v27, v27, v27 row_ror:8 row_mask:0xc bank_mask:0xf\n"
"v_fma_f32 v16, v88, v16, v160\n"
"v_fma_f32 v17, v89, v17, v161\n"
"v_fma_f32 v18, v90, v18, v162\n"
"v_fma_f32 v19, v91, v19, v163\n"
"v_fma_f32 v20, v92, v20, v164\n"
"v_fma_f32 v21, v93, v21, v165\n"
"v_fma_f32 v22, v94, v22, v166\n"
"v_fma_f32 v23, v95, v23, v167\n"
"v_fma_f32 v24, v96, v24, v168\n"
"v_fma_f32 v25, v97, v25, v169\n"
"v_fma_f32 v26, v98, v26, v170\n"
"v_fma_f32 v27, v99, v27, v171\n"
"v_add_f32 v28, v16, v17\n"
"v_mul_f32 v29, v16, v16\n"
"v_add_f32 v30, v18, v19\n"
"v_mul_f32 v31, v17, v17\n"
"v_fmac_f32 v31, v18, v18\n"
"v_fmac_f32 v29, v19, v19\n"
"v_add_f32 v30, v30, v20\n"
"v_fmac_f32 v31, v20, v20\n"
"v_add_f32 v28, v28, v21\n"
"v_fmac_f32 v29, v21, v21\n"
"v_add_f32 v30, v30, v22\n"
"v_fmac_f32 v31, v22, v22\n"
"v_add_f32 v28, v28, v23\n"
"v_fmac_f32 v29, v23, v23\n"
"v_add_f32 v30, v30, v24\n"
"v_fmac_f32 v31, v24, v24\n"
"v_add_f32 v28, v28, v25\n"
"v_fmac_f32 v29, v25, v25\n"
"v_add_f32 v30, v30, v26\n"
"v_fmac_f32 v31, v26, v26\n"
"v_add_f32 v28, v28, v27\n"
"v_fmac_f32 v29, v27, v27\n"
"v_add_f32 v28, v28, v30\n"
"v_add_f32 v29, v29, v31\n"
"v_mul_f32 v28, v28, v148\n"
"v_mul_f32 v29, v29, v148\n"
"s_nop 0\n"
"v_add_f32_dpp v28, v28, v28 row_ror:8 row_mask:0xf bank_mask:0xf\n"
"v_add_f32_dpp v29, v29, v29 row_ror:8 row_mask:0xf bank_mask:0xf\n"
"v_mov_b32 v30, v28\n"
"v_mov_b32 v31, v29\n"
"s_nop 0\n"
"v_permlane32_swap_b32 v28, v30\n"
"v_permlane32_swap_b32 v29, v31\n"
"v_add_f32 v28, v28, v30\n"
"v_add_f32 v29, v29, v31\n"
"v_mov_b32 v30, v28\n"
"v_mov_b32 v31, v29\n"
"s_nop 0\n"
"v_permlane16_swap_b32 v28, v30\n"
"v_permlane16_swap_b32 v29, v31\n"
"v_add_f32 v28, v28, v30\n"
"v_add_f32 v29, v29, v31\n"
"ds_write_b64 v36, v[28:29] offset:0\n"
"s_waitcnt lgkmcnt(0)\n"
"s_barrier\n"
"ds_read_b128 v[52:55], v154 offset:0\n"
"ds_read_b128 v[56:59], v154 offset:16\n"
"ds_read_b128 v[60:63], v154 offset:32\n"
"ds_read_b128 v[64:67], v154 offset:48\n"
"s_waitcnt lgkmcnt(1)\n"
"v_add_f32 v52, v52, v60\n"
"v_add_f32 v53, v53, v61\n"
"v_add_f32 v54, v54, v62\n"
"v_add_f32 v55, v55, v63\n"
"s_waitcnt lgkmcnt(0)\n"
"v_add_f32 v56, v56, v64\n"
"v_add_f32 v57, v57, v65\n"
"v_add_f32 v58, v58, v66\n"
"v_add_f32 v59, v59, v67\n"
"v_add_f32 v52, v52, v56\n"
"v_add_f32 v53, v53, v57\n"
"v_add_f32 v54, v54, v58\n"
"v_add_f32 v55, v55, v59\n"
"v_add_f32 v52, v52, v54\n"
"v_add_f32 v53, v53, v55\n"
"v_mul_f32 v52, 0x3ae38e39, v52\n"
"v_mul_f32 v53, 0x3ae38e39, v53\n"
"v_fma_f32 v53, -v52, v52, v53\n"
"v_max_f32 v53, 0, v53\n"
"v_add_f32 v53, 0x3727c5ac, v53\n"
"v_rsq_f32 v53, v53\n"
"v_sub_f32 v56, v16, v52\n"
"v_sub_f32 v57, v17, v52\n"
"v_sub_f32 v58, v18, v52\n"
"v_sub_f32 v59, v19, v52\n"
"v_sub_f32 v60, v20, v52\n"
"v_sub_f32 v61, v21, v52\n"
"v_sub_f32 v62, v22, v52\n"
"v_sub_f32 v63, v23, v52\n"
"v_sub_f32 v64, v24, v52\n"
"v_sub_f32 v65, v25, v52\n"
"v_sub_f32 v66, v26, v52\n"
"v_sub_f32 v67, v27, v52\n"
"v_mul_f32 v56, v56, v53\n"
"v_mul_f32 v57, v57, v53\n"
"v_mul_f32 v58, v58, v53\n"
"v_mul_f32 v59, v59, v53\n"
"v_mul_f32 v60, v60, v53\n"
"v_mul_f32 v61, v61, v53\n"
"v_mul_f32 v62, v62, v53\n"
"v_mul_f32 v63, v63, v53\n"
"v_mul_f32 v64, v64, v53\n"
"v_mul_f32 v65, v65, v53\n"
"v_mul_f32 v66, v66, v53\n"
"v_mul_f32 v67, v67, v53\n"
"v_fma_f32 v56, v56, v112, v100\n"
"v_fma_f32 v57, v57, v113, v101\n"
"v_fma_f32 v58, v58, v114, v102\n"
"v_fma_f32 v59, v59, v115, v103\n"
"v_fma_f32 v60, v60, v116, v104\n"
"v_fma_f32 v61, v61, v117, v105\n"
"v_fma_f32 v62, v62, v118, v106\n"
"v_fma_f32 v63, v63, v119, v107\n"
"v_fma_f32 v64, v64, v120, v108\n"
"v_fma_f32 v65, v65, v121, v109\n"
"v_fma_f32 v66, v66, v122, v110\n"
"v_fma_f32 v67, v67, v123, v111\n"
"v_cvt_pk_bf16_f32 v28, v56, v57\n"
"v_cvt_pk_bf16_f32 v29, v58, v59\n"
"v_cvt_pk_bf16_f32 v30, v60, v61\n"
"v_cvt_pk_bf16_f32 v31, v62, v63\n"
"v_cvt_pk_bf16_f32 v32, v64, v65\n"
"v_cvt_pk_bf16_f32 v33, v66, v67\n"
"v_mov_b32 v34, v32\n"
"v_mov_b32 v35, v33\n"
"v_permlane16_swap_b32 v28, v30\n"
"v_permlane16_swap_b32 v29, v31\n"
"global_store_dwordx4 v152, v[28:31], s[26:27] offset:0 sc1\n"
"v_permlane16_swap_b32 v32, v34\n"
"v_permlane16_swap_b32 v33, v35\n"
"global_store_dwordx4 v153, v[32:35], s[26:27] offset:0 sc1\n"
"s_waitcnt vmcnt(0) lgkmcnt(0)\n"
"s_barrier\n"
"s_cmp_eq_u32 s16, 0\n"
"s_cselect_b64 exec, 1, 0\n"
"global_atomic_add v155, v149, v150, s[28:29] offset:64 sc0\n"
"s_mov_b64 exec, -1\n"
"s_cmp_eq_u32 s16, 0\ns_cbranch_scc1 WD5\n"
"s_add_u32 m0, s82, 0x12000\n"
"s_nop 0\n"
"global_load_lds_dwordx4 v235, s[88:89]\n"
"global_load_lds_dwordx4 v236, s[88:89] offset:1024\n"
"global_load_lds_dwordx4 v237, s[88:89] offset:2048\n"
"global_load_lds_dwordx4 v238, s[88:89] offset:3072\n"
"s_cmp_ge_u32 s16, 4\n"
"s_cselect_b64 exec, -1, 0\n"
"s_add_u32 m0, s82, 0x13000\n"
"s_nop 0\n"
"global_load_lds_dwordx4 v239, s[88:89]\n"
"s_mov_b64 exec, -1\n"
"s_add_u32 s88, s88, 0x48000\n"
"s_addc_u32 s89, s89, 0\n"
"s_add_u32 m0, s82, 0x1b000\n"
"s_nop 0\n"
"global_load_lds_dwordx4 v235, s[90:91]\n"
"global_load_lds_dwordx4 v236, s[90:91] offset:1024\n"
"global_load_lds_dwordx4 v237, s[90:91] offset:2048\n"
"global_load_lds_dwordx4 v238, s[90:91] offset:3072\n"
"s_cmp_ge_u32 s16, 4\n"
"s_cselect_b64 exec, -1, 0\n"
"s_add_u32 m0, s82, 0x1c000\n"
"s_nop 0\n"
"global_load_lds_dwordx4 v239, s[90:91]\n"
"s_mov_b64 exec, -1\n"
"s_add_u32 s90, s90, 0x48000\n"
"s_addc_u32 s91, s91, 0\n"
"WD5:\n"
"v_lshrrev_b32 v246, 4, v1\n"
"v_bfe_u32 v247, v1, 2, 2\n"
"v_mul_u32_u24 v248, 0x2400, v246\n"
"v_lshl_or_b32 v248, v247, 7, v248\n"
"v_and_b32 v249, 3, v1\n"
"v_lshl_or_b32 v248, v249, 3, v248\n"
"v_lshrrev_b32 v247, 1, v247\n"
"v_and_b32 v246, 1, v246\n"
"v_lshl_or_b32 v247, v246, 1, v247\n"
"v_lshl_or_b32 v248, v247, 5, v248\n"
"s_mul_i32 s40, s17, 9\n"
"s_mul_i32 s41, s43, 5\n"
"s_add_u32 s85, s40, s41\n"
"s_add_u32 s40, s85, 0\n"
"s_min_u32 s40, s40, 35\n"
"s_and_b32 s42, s40, 3\n"
"s_lshl_b32 s42, s42, 5\n"
"s_lshr_b32 s41, s40, 2\n"
"s_lshl_b32 s41, s41, 10\n"
"v_xor_b32 v249, s42, v248\n"
"v_add_u32 v224, s41, v249\n"
"v_add_u32 v229, 0x12000, v224\n"
"s_add_u32 s40, s85, 1\n"
"s_min_u32 s40, s40, 35\n"
"s_and_b32 s42, s40, 3\n"
"s_lshl_b32 s42, s42, 5\n"
"s_lshr_b32 s41, s40, 2\n"
"s_lshl_b32 s41, s41, 10\n"
"v_xor_b32 v249, s42, v248\n"
"v_add_u32 v225, s41, v249\n"
"v_add_u32 v230, 0x12000, v225\n"
"s_add_u32 s40, s85, 2\n"
"s_min_u32 s40, s40, 35\n"
"s_and_b32 s42, s40, 3\n"
"s_lshl_b32 s42, s42, 5\n"
"s_lshr_b32 s41, s40, 2\n"
"s_lshl_b32 s41, s41, 10\n"
"v_xor_b32 v249, s42, v248\n"
"v_add_u32 v226, s41, v249\n"
"v_add_u32 v231, 0x12000, v226\n"
"s_add_u32 s40, s85, 3\n"
"s_min_u32 s40, s40, 35\n"
"s_and_b32 s42, s40, 3\n"
"s_lshl_b32 s42, s42, 5\n"
"s_lshr_b32 s41, s40, 2\n"
"s_lshl_b32 s41, s41, 10\n"
"v_xor_b32 v249, s42, v248\n"
"v_add_u32 v227, s41, v249\n"
"v_add_u32 v232, 0x12000, v227\n"
"s_add_u32 s40, s85, 4\n"
"s_min_u32 s40, s40, 35\n"
"s_and_b32 s42, s40, 3\n"
"s_lshl_b32 s42, s42, 5\n"
"s_lshr_b32 s41, s40, 2\n"
"s_lshl_b32 s41, s41, 10\n"
"v_xor_b32 v249, s42, v248\n"
"v_add_u32 v228, s41, v249\n"
"v_add_u32 v233, 0x12000, v228\n"
"v_bfe_u32 v247, v1, 2, 2\n"
"v_sub_u32 v247, 0, v247\n"
"v_lshrrev_b32 v246, 4, v1\n"
"v_xor_b32 v247, v247, v246\n"
"v_and_b32 v247, 3, v247\n"
"v_lshlrev_b32 v247, 4, v247\n"
"v_and_b32 v246, 15, v1\n"
"v_lshl_or_b32 v246, v246, 6, v247\n"
"v_add_u32 v234, 0x24000, v246\n"
"v_lshrrev_b32 v246, 4, v1\n"
"v_bfe_u32 v247, v1, 2, 2\n"
"v_mul_u32_u24 v248, 0xc00, v246\n"
"v_lshl_or_b32 v248, v247, 7, v248\n"
"v_and_b32 v249, 3, v1\n"
"v_lshl_or_b32 v248, v249, 3, v248\n"
"v_lshrrev_b32 v247, 1, v247\n"
"v_and_b32 v246, 1, v246\n"
"v_lshl_or_b32 v247, v246, 1, v247\n"
"v_lshl_or_b32 v248, v247, 5, v248\n"
"s_mul_i32 s41, s17, 3\n"
"s_add_u32 s42, s41, 0\n"
"s_and_b32 s40, s42, 3\n"
"s_lshl_b32 s40, s40, 5\n"
"s_lshr_b32 s42, s42, 2\n"
"s_lshl_b32 s42, s42, 10\n"
"s_add_u32 s42, s42, 0\n"
"v_xor_b32 v249, s40, v248\n"
"v_add_u32 v2, s42, v249\n"
"s_add_u32 s42, s41, 1\n"
"s_and_b32 s40, s42, 3\n"
"s_lshl_b32 s40, s40, 5\n"
"s_lshr_b32 s42, s42, 2\n"
"s_lshl_b32 s42, s42, 10\n"
"s_add_u32 s42, s42, 0\n"
"v_xor_b32 v249, s40, v248\n"
"v_add_u32 v3, s42, v249\n"
"s_add_u32 s42, s41, 2\n"
"s_and_b32 s40, s42, 3\n"
"s_lshl_b32 s40, s40, 5\n"
"s_lshr_b32 s42, s42, 2\n"
"s_lshl_b32 s42, s42, 10\n"
"s_add_u32 s42, s42, 0\n"
"v_xor_b32 v249, s40, v248\n"
"v_add_u32 v4, s42, v249\n"
"v_bfe_u32 v247, v1, 2, 2\n"
"v_sub_u32 v247, 0, v247\n"
"v_lshrrev_b32 v246, 4, v1\n"
"v_xor_b32 v247, v247, v246\n"
"v_and_b32 v247, 3, v247\n"
"v_lshlrev_b32 v247, 4, v247\n"
"v_and_b32 v246, 15, v1\n"
"v_lshl_or_b32 v246, v246, 6, v247\n"
"s_lshl_b32 s40, s43, 11\n"
"s_add_u32 s40, s40, 0x16000\n"
"v_add_u32 v5, s40, v246\n"
"v_lshrrev_b32 v246, 3, v1\n"
"v_bfe_u32 v247, v1, 4, 1\n"
"v_lshlrev_b32 v247, 1, v247\n"
"v_and_b32 v248, 7, v1\n"
"v_xor_b32 v247, v247, v248\n"
"v_lshlrev_b32 v247, 4, v247\n"
"s_and_b32 s40, s16, 1\n"
"s_lshl_b32 s40, s40, 4\n"
"v_add_u32 v246, s40, v246\n"
"v_mul_u32_u24 v246, 0x480, v246\n"
"v_add_u32 v246, 0x1000, v246\n"
"v_add_u32 v250, v246, v247\n"
"v_xor_b32 v247, 64, v247\n"
"v_add_u32 v246, 0x2400, v246\n"
"v_add_u32 v253, v246, v247\n"
"v_add_u32 v251, 0xfffffc80, v250\n"
"v_add_u32 v252, 0xfffff900, v250\n"
"v_add_u32 v254, 0xfffffc80, v253\n"
"v_add_u32 v255, 0xfffff900, v253\n"
"s_mul_i32 s44, s16, 0x1800\n"
"s_cmp_ge_u32 s16, 6\n"
"s_cselect_b32 s45, 0x3000, 0\n"
"s_sub_u32 s45, s44, s45\n"
"s_mul_i32 s40, s19, 0xa2000\n"
"s_lshr_b32 s42, s16, 1\n"
"s_mul_i32 s42, s42, 0x9000\n"
"s_add_u32 s40, s40, s42\n"
"s_add_u32 s50, s68, s40\n"
"s_addc_u32 s51, s69, 0\n"
"s_sub_u32 s50, s50, 0x1000\n"
"s_subb_u32 s51, s51, 0\n"
"v_bfe_u32 v246, v1, 3, 1\n"
"s_lshl_b32 s40, s43, 2\n"
"v_add_u32 v246, s40, v246\n"
"s_mov_b32 s40, 0x12000\n"
"v_mul_u32_u24 v246, s40, v246\n"
"v_and_b32 v247, 7, v1\n"
"v_lshl_add_u32 v246, v247, 4, v246\n"
"s_mul_i32 s42, s17, 0x300\n"
"s_add_u32 s42, s42, 0xc00\n"
"v_add_u32 v246, s42, v246\n"
"v_lshrrev_b32 v247, 4, v1\n"
"v_lshl_add_u32 v6, v247, 7, v246\n"
"v_lshrrev_b32 v248, 1, v247\n"
"v_lshl_add_u32 v246, v248, 7, v246\n"
"v_add_u32 v246, 0x200, v246\n"
"v_and_b32 v247, 1, v247\n"
"s_mov_b32 s40, 0x24000\n"
"v_mul_u32_u24 v247, s40, v247\n"
"v_add_u32 v7, v246, v247\n"
"s_mul_i32 s40, s18, 0x90000\n"
"s_mul_i32 s42, s19, 0x2400\n"
"s_add_u32 s40, s40, s42\n"
"s_add_u32 s40, s40, 0xa8e000\n"
"s_add_u32 s52, s4, s40\n"
"s_addc_u32 s53, s5, 0\n"
"s_add_u32 s54, s52, 0x24000\n"
"s_addc_u32 s55, s53, 0\n"
"v_bfe_u32 v247, v1, 2, 2\n"
"v_sub_u32 v247, 0, v247\n"
"v_lshrrev_b32 v246, 5, v1\n"
"v_xor_b32 v247, v247, v246\n"
"v_and_b32 v247, 3, v247\n"
"v_lshlrev_b32 v247, 4, v247\n"
"v_and_b32 v246, 15, v1\n"
"v_lshl_or_b32 v246, v246, 6, v247\n"
"v_bfe_u32 v247, v1, 4, 1\n"
"v_lshl_or_b32 v246, v247, 3, v246\n"
"s_lshr_b32 s41, s85, 1\n"
"s_lshl_b32 s41, s41, 12\n"
"s_add_u32 s40, s41, 0x16000\n"
"v_add_u32 v246, s40, v246\n"
"s_and_b32 s40, s85, 1\n"
"s_lshl_b32 s41, s40, 5\n"
"v_xor_b32 v13, s41, v246\n"
"s_lshl_b32 s40, s40, 12\n"
"v_xor_b32 v246, 32, v13\n"
"v_add_u32 v14, s40, v246\n"
"s_waitcnt vmcnt(0)\n"
"v_readfirstlane_b32 s40, v155\n"
"s_nop 1\n"
"s_cmp_lg_u32 s16, 0\ns_cbranch_scc1 CW_OK6\ns_add_u32 s40, s40, 1\ns_cmp_eq_u32 s40, s95\ns_cbranch_scc1 CW_OK6\ns_mov_b32 s42, 0x40000\ns_mov_b64 exec, 1\nCW6:\ns_sleep 1\nglobal_atomic_add v155, v149, v149, s[28:29] offset:64 sc0\ns_waitcnt vmcnt(0)\nv_readfirstlane_b32 s40, v155\ns_sub_u32 s42, s42, 1\ns_cselect_b32 s40, s95, s40\ns_cmp_lg_u32 s40, s95\ns_cbranch_scc1 CW6\ns_mov_b64 exec, -1\nCW_OK6:\n"
"s_barrier\n"
"v_lshlrev_b32 v246, 4, v0\n"
"s_lshl_b32 s40, s16, 10\n"
"s_add_u32 m0, s40, 0x24000\n"
"s_nop 0\n"
"global_load_lds_dwordx4 v246, s[80:81] sc1\n"
"s_add_u32 s80, s80, 0x2000\n"
"s_addc_u32 s81, s81, 0\n"
"s_cmp_lg_u32 s16, 0\ns_cbranch_scc1 WD7\n"
"s_mov_b32 m0, s82\n"
"s_nop 0\n"
"global_load_lds_dwordx4 v235, s[88:89]\n"
"global_load_lds_dwordx4 v236, s[88:89] offset:1024\n"
"global_load_lds_dwordx4 v237, s[88:89] offset:2048\n"
"global_load_lds_dwordx4 v238, s[88:89] offset:3072\n"
"s_add_u32 s88, s88, 0x48000\n"
"s_addc_u32 s89, s89, 0\n"
"s_add_u32 m0, s82, 0x9000\n"
"s_nop 0\n"
"global_load_lds_dwordx4 v235, s[90:91]\n"
"global_load_lds_dwordx4 v236, s[90:91] offset:1024\n"
"global_load_lds_dwordx4 v237, s[90:91] offset:2048\n"
"global_load_lds_dwordx4 v238, s[90:91] offset:3072\n"
"s_add_u32 s90, s90, 0x48000\n"
"s_addc_u32 s91, s91, 0\n"
"s_add_u32 m0, s82, 0x12000\n"
"s_nop 0\n"
"global_load_lds_dwordx4 v235, s[88:89]\n"
"global_load_lds_dwordx4 v236, s[88:89] offset:1024\n"
"global_load_lds_dwordx4 v237, s[88:89] offset:2048\n"
"global_load_lds_dwordx4 v238, s[88:89] offset:3072\n"
"s_add_u32 s88, s88, 0x48000\n"
"s_addc_u32 s89, s89, 0\n"
"s_add_u32 m0, s82, 0x1b000\n"
"s_nop 0\n"
"global_load_lds_dwordx4 v235, s[90:91]\n"
"global_load_lds_dwordx4 v236, s[90:91] offset:1024\n"
"global_load_lds_dwordx4 v237, s[90:91] offset:2048\n"
"global_load_lds_dwordx4 v238, s[90:91] offset:3072\n"
"s_add_u32 s90, s90, 0x48000\n"
"s_addc_u32 s91, s91, 0\n"
"s_waitcnt vmcnt(12)\ns_branch WDJ7\nWD7:\ns_waitcnt vmcnt(0)\nWDJ7:\n"
"s_barrier\n"
"global_store_dwordx4 v151, v[16:19], s[34:35] offset:0 sc1\n"
"global_store_dwordx4 v151, v[20:23], s[34:35] offset:1024 sc1\n"
"global_store_dwordx4 v151, v[24:27], s[34:35] offset:2048 sc1\n"
"s_cmp_lt_u32 s16, 4\n"
"s_cbranch_scc0 F2_CLSB\n"
"ds_read_b128 v[192:195], v234 offset:0\n"
"ds_read_b64_tr_b16 v[176:177], v224 offset:0\n"
"ds_read_b64_tr_b16 v[178:179], v224 offset:512\n"
"ds_read_b128 v[196:199], v234 offset:1024\n"
"ds_read_b128 v[200:203], v234 offset:2048\n"
"ds_read_b128 v[204:207], v234 offset:3072\n"
"ds_read_b64_tr_b16 v[180:181], v225 offset:0\n"
"ds_read_b64_tr_b16 v[182:183], v225 offset:512\n"
"ds_read_b64_tr_b16 v[184:185], v226 offset:0\n"
"ds_read_b64_tr_b16 v[186:187], v226 offset:512\n"
"s_waitcnt lgkmcnt(7)\n"
"v_mfma_f32_16x16x32_bf16 v[16:19], v[176:179], v[192:195], 0\n"
"ds_read_b64_tr_b16 v[188:189], v227 offset:0\n"
"s_waitcnt lgkmcnt(7)\n"
"v_mfma_f32_16x16x32_bf16 v[20:23], v[176:179], v[196:199], 0\n"
"ds_read_b64_tr_b16 v[190:191], v227 offset:512\n"
"s_waitcnt lgkmcnt(7)\n"
"v_mfma_f32_16x16x32_bf16 v[24:27], v[176:179], v[200:203], 0\n"
"s_waitcnt lgkmcnt(6)\n"
"v_mfma_f32_16x16x32_bf16 v[28:31], v[176:179], v[204:207], 0\n"
"s_waitcnt lgkmcnt(4)\n"
"v_mfma_f32_16x16x32_bf16 v[32:35], v[180:183], v[192:195], 0\n"
"ds_read_b64_tr_b16 v[176:177], v228 offset:0\n"
"v_mfma_f32_16x16x32_bf16 v[36:39], v[180:183], v[196:199], 0\n"
"ds_read_b64_tr_b16 v[178:179], v228 offset:512\n"
"v_mfma_f32_16x16x32_bf16 v[40:43], v[180:183], v[200:203], 0\n"
"v_mfma_f32_16x16x32_bf16 v[44:47], v[180:183], v[204:207], 0\n"
"s_waitcnt lgkmcnt(4)\n"
"v_mfma_f32_16x16x32_bf16 v[48:51], v[184:187], v[192:195], 0\n"
"v_mfma_f32_16x16x32_bf16 v[52:55], v[184:187], v[196:199], 0\n"
"v_mfma_f32_16x16x32_bf16 v[56:59], v[184:187], v[200:203], 0\n"
"v_mfma_f32_16x16x32_bf16 v[60:63], v[184:187], v[204:207], 0\n"
"s_waitcnt vmcnt(11) lgkmcnt(0)\n"
"s_barrier\n"
"ds_read_b64_tr_b16 v[180:181], v224 offset:36864\n"
"v_mfma_f32_16x16x32_bf16 v[64:67], v[188:191], v[192:195], 0\n"
"ds_read_b64_tr_b16 v[182:183], v224 offset:37376\n"
"s_mov_b32 m0, s82\n"
"v_mfma_f32_16x16x32_bf16 v[68:71], v[188:191], v[196:199], 0\n"
"global_load_lds_dwordx4 v235, s[88:89]\n"
"v_mfma_f32_16x16x32_bf16 v[72:75], v[188:191], v[200:203], 0\n"
"ds_read_b64_tr_b16 v[184:185], v225 offset:36864\n"
"v_mfma_f32_16x16x32_bf16 v[76:79], v[188:191], v[204:207], 0\n"
"ds_read_b64_tr_b16 v[186:187], v225 offset:37376\n"
"v_mfma_f32_16x16x32_bf16 v[80:83], v[176:179], v[192:195], 0\n"
"global_load_lds_dwordx4 v236, s[88:89] offset:1024\n"
"v_mfma_f32_16x16x32_bf16 v[84:87], v[176:179], v[196:199], 0\n"
"v_mfma_f32_16x16x32_bf16 v[88:91], v[176:179], v[200:203], 0\n"
"ds_read_b64_tr_b16 v[188:189], v226 offset:36864\n"
"ds_read_b64_tr_b16 v[190:191], v226 offset:37376\n"
"v_mfma_f32_16x16x32_bf16 v[92:95], v[176:179], v[204:207], 0\n"
"s_waitcnt lgkmcnt(4)\n"
"v_mfma_f32_16x16x32_bf16 v[96:99], v[180:183], v[192:195], 0\n"
"ds_read_b64_tr_b16 v[176:177], v227 offset:36864\n"
"v_mfma_f32_16x16x32_bf16 v[100:103], v[180:183], v[196:199], 0\n"
"ds_read_b64_tr_b16 v[178:179], v227 offset:37376\n"
"global_load_lds_dwordx4 v237, s[88:89] offset:2048\n"
"v_mfma_f32_16x16x32_bf16 v[104:107], v[180:183], v[200:203], 0\n"
"v_mfma_f32_16x16x32_bf16 v[108:111], v[180:183], v[204:207], 0\n"
"s_waitcnt lgkmcnt(4)\n"
"v_mfma_f32_16x16x32_bf16 v[112:115], v[184:187], v[192:195], 0\n"
"ds_read_b64_tr_b16 v[180:181], v228 offset:36864\n"
"global_load_lds_dwordx4 v238, s[88:89] offset:3072\n"
"s_add_u32 s88, s88, 0x48000\n"
"s_addc_u32 s89, s89, 0\n"
"v_mfma_f32_16x16x32_bf16 v[116:119], v[184:187], v[196:199], 0\n"
"ds_read_b64_tr_b16 v[182:183], v228 offset:37376\n"
"v_mfma_f32_16x16x32_bf16 v[120:123], v[184:187], v[200:203], 0\n"
"s_add_u32 m0, s84, 0x26000\n"
"v_mfma_f32_16x16x32_bf16 v[124:127], v[184:187], v[204:207], 0\n"
"global_load_lds_dwordx4 v240, s[80:81] sc1\n"
"s_add_u32 s80, s80, 0x1000\n"
"s_addc_u32 s81, s81, 0\n"
"s_waitcnt lgkmcnt(4)\n"
"v_mfma_f32_16x16x32_bf16 v[128:131], v[188:191], v[192:195], 0\n"
"v_mfma_f32_16x16x32_bf16 v[132:135], v[188:191], v[196:199], 0\n"
"v_mfma_f32_16x16x32_bf16 v[136:139], v[188:191], v[200:203], 0\n"
"v_mfma_f32_16x16x32_bf16 v[140:143], v[188:191], v[204:207], 0\n"
"s_waitcnt vmcnt(12) lgkmcnt(0)\n"
"s_barrier\n"
"ds_read_b128 v[208:211], v234 offset:4096\n"
"ds_read_b64_tr_b16 v[184:185], v229 offset:0\n"
"v_mfma_f32_16x16x32_bf16 v[144:147], v[176:179], v[192:195], 0\n"
"ds_read_b64_tr_b16 v[186:187], v229 offset:512\n"
"s_add_u32 m0, s82, 0x9000\n"
"v_mfma_f32_16x16x32_bf16 v[148:151], v[176:179], v[196:199], 0\n"
"ds_read_b128 v[212:215], v234 offset:5120\n"
"global_load_lds_dwordx4 v235, s[90:91]\n"
"v_mfma_f32_16x16x32_bf16 v[152:155], v[176:179], v[200:203], 0\n"
"ds_read_b128 v[216:219], v234 offset:6144\n"
"ds_read_b128 v[220:223], v234 offset:7168\n"
"v_mfma_f32_16x16x32_bf16 v[156:159], v[176:179], v[204:207], 0\n"
"ds_read_b64_tr_b16 v[188:189], v230 offset:0\n"
"v_mfma_f32_16x16x32_bf16 v[160:163], v[180:183], v[192:195], 0\n"
"ds_read_b64_tr_b16 v[190:191], v230 offset:512\n"
"global_load_lds_dwordx4 v236, s[90:91] offset:1024\n"
"v_mfma_f32_16x16x32_bf16 v[164:167], v[180:183], v[196:199], 0\n"
"v_mfma_f32_16x16x32_bf16 v[168:171], v[180:183], v[200:203], 0\n"
"ds_read_b64_tr_b16 v[176:177], v231 offset:0\n"
"ds_read_b64_tr_b16 v[178:179], v231 offset:512\n"
"v_mfma_f32_16x16x32_bf16 v[172:175], v[180:183], v[204:207], 0\n"
"s_waitcnt lgkmcnt(7)\n"
"v_mfma_f32_16x16x32_bf16 v[16:19], v[184:187], v[208:211], v[16:19]\n"
"ds_read_b64_tr_b16 v[180:181], v232 offset:0\n"
"s_waitcnt lgkmcnt(7)\n"
"v_mfma_f32_16x16x32_bf16 v[20:23], v[184:187], v[212:215], v[20:23]\n"
"ds_read_b64_tr_b16 v[182:183], v232 offset:512\n"
"global_load_lds_dwordx4 v237, s[90:91] offset:2048\n"
"s_waitcnt lgkmcnt(7)\n"
"v_mfma_f32_16x16x32_bf16 v[24:27], v[184:187], v[216:219], v[24:27]\n"
"s_waitcnt lgkmcnt(6)\n"
"v_mfma_f32_16x16x32_bf16 v[28:31], v[184:187], v[220:223], v[28:31]\n"
"s_waitcnt lgkmcnt(4)\n"
"v_mfma_f32_16x16x32_bf16 v[32:35], v[188:191], v[208:211], v[32:35]\n"
"ds_read_b64_tr_b16 v[184:185], v233 offset:0\n"
"global_load_lds_dwordx4 v238, s[90:91] offset:3072\n"
"s_add_u32 s90, s90, 0x48000\n"
"s_addc_u32 s91, s91, 0\n"
"v_mfma_f32_16x16x32_bf16 v[36:39], v[188:191], v[212:215], v[36:39]\n"
"ds_read_b64_tr_b16 v[186:187], v233 offset:512\n"
"v_mfma_f32_16x16x32_bf16 v[40:43], v[188:191], v[216:219], v[40:43]\n"
"v_mfma_f32_16x16x32_bf16 v[44:47], v[188:191], v[220:223], v[44:47]\n"
"s_waitcnt lgkmcnt(4)\n"
"v_mfma_f32_16x16x32_bf16 v[48:51], v[176:179], v[208:211], v[48:51]\n"
"v_mfma_f32_16x16x32_bf16 v[52:55], v[176:179], v[212:215], v[52:55]\n"
"v_mfma_f32_16x16x32_bf16 v[56:59], v[176:179], v[216:219], v[56:59]\n"
"v_mfma_f32_16x16x32_bf16 v[60:63], v[176:179], v[220:223], v[60:63]\n"
"s_waitcnt vmcnt(12) lgkmcnt(0)\n"
"s_barrier\n"
"ds_read_b64_tr_b16 v[188:189], v229 offset:36864\n"
"v_mfma_f32_16x16x32_bf16 v[64:67], v[180:183], v[208:211], v[64:67]\n"
"ds_read_b64_tr_b16 v[190:191], v229 offset:37376\n"
"s_add_u32 m0, s82, 0x12000\n"
"v_mfma_f32_16x16x32_bf16 v[68:71], v[180:183], v[212:215], v[68:71]\n"
"global_load_lds_dwordx4 v235, s[88:89]\n"
"v_mfma_f32_16x16x32_bf16 v[72:75], v[180:183], v[216:219], v[72:75]\n"
"ds_read_b64_tr_b16 v[176:177], v230 offset:36864\n"
"v_mfma_f32_16x16x32_bf16 v[76:79], v[180:183], v[220:223], v[76:79]\n"
"ds_read_b64_tr_b16 v[178:179], v230 offset:37376\n"
"v_mfma_f32_16x16x32_bf16 v[80:83], v[184:187], v[208:211], v[80:83]\n"
"global_load_lds_dwordx4 v236, s[88:89] offset:1024\n"
"v_mfma_f32_16x16x32_bf16 v[84:87], v[184:187], v[212:215], v[84:87]\n"
"v_mfma_f32_16x16x32_bf16 v[88:91], v[184:187], v[216:219], v[88:91]\n"
"ds_read_b64_tr_b16 v[180:181], v231 offset:36864\n"
"ds_read_b64_tr_b16 v[182:183], v231 offset:37376\n"
"v_mfma_f32_16x16x32_bf16 v[92:95], v[184:187], v[220:223], v[92:95]\n"
"s_waitcnt lgkmcnt(4)\n"
"v_mfma_f32_16x16x32_bf16 v[96:99], v[188:191], v[208:211], v[96:99]\n"
"ds_read_b64_tr_b16 v[184:185], v232 offset:36864\n"
"v_mfma_f32_16x16x32_bf16 v[100:103], v[188:191], v[212:215], v[100:103]\n"
"ds_read_b64_tr_b16 v[186:187], v232 offset:37376\n"
"global_load_lds_dwordx4 v237, s[88:89] offset:2048\n"
"v_mfma_f32_16x16x32_bf16 v[104:107], v[188:191], v[216:219], v[104:107]\n"
"v_mfma_f32_16x16x32_bf16 v[108:111], v[188:191], v[220:223], v[108:111]\n"
"s_waitcnt lgkmcnt(4)\n"
"v_mfma_f32_16x16x32_bf16 v[112:115], v[176:179], v[208:211], v[112:115]\n"
"ds_read_b64_tr_b16 v[188:189], v233 offset:36864\n"
"global_load_lds_dwordx4 v238, s[88:89] offset:3072\n"
"s_add_u32 s88, s88, 0x48000\n"
"s_addc_u32 s89, s89, 0\n"
"v_mfma_f32_16x16x32_bf16 v[116:119], v[176:179], v[212:215], v[116:119]\n"
"ds_read_b64_tr_b16 v[190:191], v233 offset:37376\n"
"v_mfma_f32_16x16x32_bf16 v[120:123], v[176:179], v[216:219], v[120:123]\n"
"s_add_u32 m0, s84, 0x24000\n"
"v_mfma_f32_16x16x32_bf16 v[124:127], v[176:179], v[220:223], v[124:127]\n"
"global_load_lds_dwordx4 v240, s[80:81] sc1\n"
"s_add_u32 s80, s80, 0x1000\n"
"s_addc_u32 s81, s81, 0\n"
"s_waitcnt lgkmcnt(4)\n"
"v_mfma_f32_16x16x32_bf16 v[128:131], v[180:183], v[208:211], v[128:131]\n"
"v_mfma_f32_16x16x32_bf16 v[132:135], v[180:183], v[212:215], v[132:135]\n"
"v_mfma_f32_16x16x32_bf16 v[136:139], v[180:183], v[216:219], v[136:139]\n"
"v_mfma_f32_16x16x32_bf16 v[140:143], v[180:183], v[220:223], v[140:143]\n"
"s_waitcnt vmcnt(9) lgkmcnt(0)\n"
"s_barrier\n"
"ds_read_b128 v[192:195], v234 offset:8192\n"
"ds_read_b64_tr_b16 v[176:177], v224 offset:0\n"
"v_mfma_f32_16x16x32_bf16 v[144:147], v[184:187], v[208:211], v[144:147]\n"
"ds_read_b64_tr_b16 v[178:179], v224 offset:512\n"
"s_add_u32 m0, s82, 0x1b000\n"
"v_mfma_f32_16x16x32_bf16 v[148:151], v[184:187], v[212:215], v[148:151]\n"
"ds_read_b128 v[196:199], v234 offset:9216\n"
"global_load_lds_dwordx4 v235, s[90:91]\n"
"v_mfma_f32_16x16x32_bf16 v[152:155], v[184:187], v[216:219], v[152:155]\n"
"ds_read_b128 v[200:203], v234 offset:10240\n"
"ds_read_b128 v[204:207], v234 offset:11264\n"
"v_mfma_f32_16x16x32_bf16 v[156:159], v[184:187], v[220:223], v[156:159]\n"
"ds_read_b64_tr_b16 v[180:181], v225 offset:0\n"
"v_mfma_f32_16x16x32_bf16 v[160:163], v[188:191], v[208:211], v[160:163]\n"
"ds_read_b64_tr_b16 v[182:183], v225 offset:512\n"
"global_load_lds_dwordx4 v236, s[90:91] offset:1024\n"
"v_mfma_f32_16x16x32_bf16 v[164:167], v[188:191], v[212:215], v[164:167]\n"
"v_mfma_f32_16x16x32_bf16 v[168:171], v[188:191], v[216:219], v[168:171]\n"
"ds_read_b64_tr_b16 v[184:185], v226 offset:0\n"
"ds_read_b64_tr_b16 v[186:187], v226 offset:512\n"
"v_mfma_f32_16x16x32_bf16 v[172:175], v[188:191], v[220:223], v[172:175]\n"
"s_waitcnt lgkmcnt(7)\n"
"v_mfma_f32_16x16x32_bf16 v[16:19], v[176:179], v[192:195], v[16:19]\n"
"ds_read_b64_tr_b16 v[188:189], v227 offset:0\n"
"s_waitcnt lgkmcnt(7)\n"
"v_mfma_f32_16x16x32_bf16 v[20:23], v[176:179], v[196:199], v[20:23]\n"
"ds_read_b64_tr_b16 v[190:191], v227 offset:512\n"
"global_load_lds_dwordx4 v237, s[90:91] offset:2048\n"
"s_waitcnt lgkmcnt(7)\n"
"v_mfma_f32_16x16x32_bf16 v[24:27], v[176:179], v[200:203], v[24:27]\n"
"s_waitcnt lgkmcnt(6)\n"
"v_mfma_f32_16x16x32_bf16 v[28:31], v[176:179], v[204:207], v[28:31]\n"
"s_waitcnt lgkmcnt(4)\n"
"v_mfma_f32_16x16x32_bf16 v[32:35], v[180:183], v[192:195], v[32:35]\n"
"ds_read_b64_tr_b16 v[176:177], v228 offset:0\n"
"global_load_lds_dwordx4 v238, s[90:91] offset:3072\n"
"s_add_u32 s90, s90, 0x48000\n"
"s_addc_u32 s91, s91, 0\n"
"v_mfma_f32_16x16x32_bf16 v[36:39], v[180:183], v[196:199], v[36:39]\n"
"ds_read_b64_tr_b16 v[178:179], v228 offset:512\n"
"v_mfma_f32_16x16x32_bf16 v[40:43], v[180:183], v[200:203], v[40:43]\n"
"v_mfma_f32_16x16x32_bf16 v[44:47], v[180:183], v[204:207], v[44:47]\n"
"s_waitcnt lgkmcnt(4)\n"
"v_mfma_f32_16x16x32_bf16 v[48:51], v[184:187], v[192:195], v[48:51]\n"
"v_mfma_f32_16x16x32_bf16 v[52:55], v[184:187], v[196:199], v[52:55]\n"
"v_mfma_f32_16x16x32_bf16 v[56:59], v[184:187], v[200:203], v[56:59]\n"
"v_mfma_f32_16x16x32_bf16 v[60:63], v[184:187], v[204:207], v[60:63]\n"
"s_waitcnt vmcnt(9) lgkmcnt(0)\n"
"s_barrier\n"
"ds_read_b64_tr_b16 v[180:181], v224 offset:36864\n"
"v_mfma_f32_16x16x32_bf16 v[64:67], v[188:191], v[192:195], v[64:67]\n"
"ds_read_b64_tr_b16 v[182:183], v224 offset:37376\n"
"s_mov_b32 m0, s82\n"
"v_mfma_f32_16x16x32_bf16 v[68:71], v[188:191], v[196:199], v[68:71]\n"
"global_load_lds_dwordx4 v235, s[88:89]\n"
"v_mfma_f32_16x16x32_bf16 v[72:75], v[188:191], v[200:203], v[72:75]\n"
"ds_read_b64_tr_b16 v[184:185], v225 offset:36864\n"
"v_mfma_f32_16x16x32_bf16 v[76:79], v[188:191], v[204:207], v[76:79]\n"
"ds_read_b64_tr_b16 v[186:187], v225 offset:37376\n"
"v_mfma_f32_16x16x32_bf16 v[80:83], v[176:179], v[192:195], v[80:83]\n"
"global_load_lds_dwordx4 v236, s[88:89] offset:1024\n"
"v_mfma_f32_16x16x32_bf16 v[84:87], v[176:179], v[196:199], v[84:87]\n"
"v_mfma_f32_16x16x32_bf16 v[88:91], v[176:179], v[200:203], v[88:91]\n"
"ds_read_b64_tr_b16 v[188:189], v226 offset:36864\n"
"ds_read_b64_tr_b16 v[190:191], v226 offset:37376\n"
"v_mfma_f32_16x16x32_bf16 v[92:95], v[176:179], v[204:207], v[92:95]\n"
"s_waitcnt lgkmcnt(4)\n"
"v_mfma_f32_16x16x32_bf16 v[96:99], v[180:183], v[192:195], v[96:99]\n"
"ds_read_b64_tr_b16 v[176:177], v227 offset:36864\n"
"v_mfma_f32_16x16x32_bf16 v[100:103], v[180:183], v[196:199], v[100:103]\n"
"ds_read_b64_tr_b16 v[178:179], v227 offset:37376\n"
"global_load_lds_dwordx4 v237, s[88:89] offset:2048\n"
"v_mfma_f32_16x16x32_bf16 v[104:107], v[180:183], v[200:203], v[104:107]\n"
"v_mfma_f32_16x16x32_bf16 v[108:111], v[180:183], v[204:207], v[108:111]\n"
"s_waitcnt lgkmcnt(4)\n"
"v_mfma_f32_16x16x32_bf16 v[112:115], v[184:187], v[192:195], v[112:115]\n"
"ds_read_b64_tr_b16 v[180:181], v228 offset:36864\n"
"global_load_lds_dwordx4 v238, s[88:89] offset:3072\n"
"s_add_u32 s88, s88, 0x48000\n"
"s_addc_u32 s89, s89, 0\n"
"v_mfma_f32_16x16x32_bf16 v[116:119], v[184:187], v[196:199], v[116:119]\n"
"ds_read_b64_tr_b16 v[182:183], v228 offset:37376\n"
"v_mfma_f32_16x16x32_bf16 v[120:123], v[184:187], v[200:203], v[120:123]\n"
"s_add_u32 m0, s84, 0x25000\n"
"v_mfma_f32_16x16x32_bf16 v[124:127], v[184:187], v[204:207], v[124:127]\n"
"global_load_lds_dwordx4 v240, s[80:81] sc1\n"
"s_add_u32 s80, s80, 0x1000\n"
"s_addc_u32 s81, s81, 0\n"
"s_waitcnt lgkmcnt(4)\n"
"v_mfma_f32_16x16x32_bf16 v[128:131], v[188:191], v[192:195], v[128:131]\n"
"v_mfma_f32_16x16x32_bf16 v[132:135], v[188:191], v[196:199], v[132:135]\n"
"v_mfma_f32_16x16x32_bf16 v[136:139], v[188:191], v[200:203], v[136:139]\n"
"v_mfma_f32_16x16x32_bf16 v[140:143], v[188:191], v[204:207], v[140:143]\n"
"s_waitcnt vmcnt(9) lgkmcnt(0)\n"
"s_barrier\n"
"ds_read_b128 v[208:211], v234 offset:0\n"
"ds_read_b64_tr_b16 v[184:185], v229 offset:0\n"
"v_mfma_f32_16x16x32_bf16 v[144:147], v[176:179], v[192:195], v[144:147]\n"
"ds_read_b64_tr_b16 v[186:187], v229 offset:512\n"
"s_add_u32 m0, s82, 0x9000\n"
"v_mfma_f32_16x16x32_bf16 v[148:151], v[176:179], v[196:199], v[148:151]\n"
"ds_read_b128 v[212:215], v234 offset:1024\n"
"global_load_lds_dwordx4 v235, s[90:91]\n"
"v_mfma_f32_16x16x32_bf16 v[152:155], v[176:179], v[200:203], v[152:155]\n"
"ds_read_b128 v[216:219], v234 offset:2048\n"
"ds_read_b128 v[220:223], v234 offset:3072\n"
"v_mfma_f32_16x16x32_bf16 v[156:159], v[176:179], v[204:207], v[156:159]\n"
"ds_read_b64_tr_b16 v[188:189], v230 offset:0\n"
"v_mfma_f32_16x16x32_bf16 v[160:163], v[180:183], v[192:195], v[160:163]\n"
"ds_read_b64_tr_b16 v[190:191], v230 offset:512\n"
"global_load_lds_dwordx4 v236, s[90:91] offset:1024\n"
"v_mfma_f32_16x16x32_bf16 v[164:167], v[180:183], v[196:199], v[164:167]\n"
"v_mfma_f32_16x16x32_bf16 v[168:171], v[180:183], v[200:203], v[168:171]\n"
"ds_read_b64_tr_b16 v[176:177], v231 offset:0\n"
"ds_read_b64_tr_b16 v[178:179], v231 offset:512\n"
"v_mfma_f32_16x16x32_bf16 v[172:175], v[180:183], v[204:207], v[172:175]\n"
"s_waitcnt lgkmcnt(7)\n"
"v_mfma_f32_16x16x32_bf16 v[16:19], v[184:187], v[208:211], v[16:19]\n"
"ds_read_b64_tr_b16 v[180:181], v232 offset:0\n"
"s_waitcnt lgkmcnt(7)\n"
"v_mfma_f32_16x16x32_bf16 v[20:23], v[184:187], v[212:215], v[20:23]\n"
"ds_read_b64_tr_b16 v[182:183], v232 offset:512\n"
"global_load_lds_dwordx4 v237, s[90:91] offset:2048\n"
"s_waitcnt lgkmcnt(7)\n"
"v_mfma_f32_16x16x32_bf16 v[24:27], v[184:187], v[216:219], v[24:27]\n"
"s_waitcnt lgkmcnt(6)\n"
"v_mfma_f32_16x16x32_bf16 v[28:31], v[184:187], v[220:223], v[28:31]\n"
"s_waitcnt lgkmcnt(4)\n"
"v_mfma_f32_16x16x32_bf16 v[32:35], v[188:191], v[208:211], v[32:35]\n"
"ds_read_b64_tr_b16 v[184:185], v233 offset:0\n"
"global_load_lds_dwordx4 v238, s[90:91] offset:3072\n"
"s_add_u32 s90, s90, 0x48000\n"
"s_addc_u32 s91, s91, 0\n"
"v_mfma_f32_16x16x32_bf16 v[36:39], v[188:191], v[212:215], v[36:39]\n"
"ds_read_b64_tr_b16 v[186:187], v233 offset:512\n"
"v_mfma_f32_16x16x32_bf16 v[40:43], v[188:191], v[216:219], v[40:43]\n"
"v_mfma_f32_16x16x32_bf16 v[44:47], v[188:191], v[220:223], v[44:47]\n"
"s_waitcnt lgkmcnt(4)\n"
"v_mfma_f32_16x16x32_bf16 v[48:51], v[176:179], v[208:211], v[48:51]\n"
"v_mfma_f32_16x16x32_bf16 v[52:55], v[176:179], v[212:215], v[52:55]\n"
"v_mfma_f32_16x16x32_bf16 v[56:59], v[176:179], v[216:219], v[56:59]\n"
"v_mfma_f32_16x16x32_bf16 v[60:63], v[176:179], v[220:223], v[60:63]\n"
"s_waitcnt vmcnt(9) lgkmcnt(0)\n"
"s_barrier\n"
"ds_read_b64_tr_b16 v[188:189], v229 offset:36864\n"
"v_mfma_f32_16x16x32_bf16 v[64:67], v[180:183], v[208:211], v[64:67]\n"
"ds_read_b64_tr_b16 v[190:191], v229 offset:37376\n"
"s_add_u32 m0, s82, 0x12000\n"
"v_mfma_f32_16x16x32_bf16 v[68:71], v[180:183], v[212:215], v[68:71]\n"
"global_load_lds_dwordx4 v235, s[88:89]\n"
"v_mfma_f32_16x16x32_bf16 v[72:75], v[180:183], v[216:219], v[72:75]\n"
"ds_read_b64_tr_b16 v[176:177], v230 offset:36864\n"
"v_mfma_f32_16x16x32_bf16 v[76:79], v[180:183], v[220:223], v[76:79]\n"
"ds_read_b64_tr_b16 v[178:179], v230 offset:37376\n"
"v_mfma_f32_16x16x32_bf16 v[80:83], v[184:187], v[208:211], v[80:83]\n"
"global_load_lds_dwordx4 v236, s[88:89] offset:1024\n"
"v_mfma_f32_16x16x32_bf16 v[84:87], v[184:187], v[212:215], v[84:87]\n"
"v_mfma_f32_16x16x32_bf16 v[88:91], v[184:187], v[216:219], v[88:91]\n"
"ds_read_b64_tr_b16 v[180:181], v231 offset:36864\n"
"ds_read_b64_tr_b16 v[182:183], v231 offset:37376\n"
"v_mfma_f32_16x16x32_bf16 v[92:95], v[184:187], v[220:223], v[92:95]\n"
"s_waitcnt lgkmcnt(4)\n"
"v_mfma_f32_16x16x32_bf16 v[96:99], v[188:191], v[208:211], v[96:99]\n"
"ds_read_b64_tr_b16 v[184:185], v232 offset:36864\n"
"v_mfma_f32_16x16x32_bf16 v[100:103], v[188:191], v[212:215], v[100:103]\n"
"ds_read_b64_tr_b16 v[186:187], v232 offset:37376\n"
"global_load_lds_dwordx4 v237, s[88:89] offset:2048\n"
"v_mfma_f32_16x16x32_bf16 v[104:107], v[188:191], v[216:219], v[104:107]\n"
"v_mfma_f32_16x16x32_bf16 v[108:111], v[188:191], v[220:223], v[108:111]\n"
"s_waitcnt lgkmcnt(4)\n"
"v_mfma_f32_16x16x32_bf16 v[112:115], v[176:179], v[208:211], v[112:115]\n"
"ds_read_b64_tr_b16 v[188:189], v233 offset:36864\n"
"global_load_lds_dwordx4 v238, s[88:89] offset:3072\n"
"s_add_u32 s88, s88, 0x48000\n"
"s_addc_u32 s89, s89, 0\n"
"v_mfma_f32_16x16x32_bf16 v[116:119], v[176:179], v[212:215], v[116:119]\n"
"ds_read_b64_tr_b16 v[190:191], v233 offset:37376\n"
"v_mfma_f32_16x16x32_bf16 v[120:123], v[176:179], v[216:219], v[120:123]\n"
"s_add_u32 m0, s84, 0x26000\n"
"v_mfma_f32_16x16x32_bf16 v[124:127], v[176:179], v[220:223], v[124:127]\n"
"global_load_lds_dwordx4 v240, s[80:81] sc1\n"
"s_add_u32 s80, s80, 0x1000\n"
"s_addc_u32 s81, s81, 0\n"
"s_waitcnt lgkmcnt(4)\n"
"v_mfma_f32_16x16x32_bf16 v[128:131], v[180:183], v[208:211], v[128:131]\n"
"v_mfma_f32_16x16x32_bf16 v[132:135], v[180:183], v[212:215], v[132:135]\n"
"v_mfma_f32_16x16x32_bf16 v[136:139], v[180:183], v[216:219], v[136:139]\n"
"v_mfma_f32_16x16x32_bf16 v[140:143], v[180:183], v[220:223], v[140:143]\n"
"s_waitcnt vmcnt(9) lgkmcnt(0)\n"
"s_barrier\n"
"ds_read_b128 v[192:195], v234 offset:4096\n"
"ds_read_b64_tr_b16 v[176:177], v224 offset:0\n"
"v_mfma_f32_16x16x32_bf16 v[144:147], v[184:187], v[208:211], v[144:147]\n"
"ds_read_b64_tr_b16 v[178:179], v224 offset:512\n"
"s_add_u32 m0, s82, 0x1b000\n"
"v_mfma_f32_16x16x32_bf16 v[148:151], v[184:187], v[212:215], v[148:151]\n"
"ds_read_b128 v[196:199], v234 offset:5120\n"
"global_load_lds_dwordx4 v235, s[90:91]\n"
"v_mfma_f32_16x16x32_bf16 v[152:155], v[184:187], v[216:219], v[152:155]\n"
"ds_read_b128 v[200:203], v234 offset:6144\n"
"ds_read_b128 v[204:207], v234 offset:7168\n"
"v_mfma_f32_16x16x32_bf16 v[156:159], v[184:187], v[220:223], v[156:159]\n"
"ds_read_b64_tr_b16 v[180:181], v225 offset:0\n"
"v_mfma_f32_16x16x32_bf16 v[160:163], v[188:191], v[208:211], v[160:163]\n"
"ds_read_b64_tr_b16 v[182:183], v225 offset:512\n"
"global_load_lds_dwordx4 v236, s[90:91] offset:1024\n"
"v_mfma_f32_16x16x32_bf16 v[164:167], v[188:191], v[212:215], v[164:167]\n"
"v_mfma_f32_16x16x32_bf16 v[168:171], v[188:191], v[216:219], v[168:171]\n"
"ds_read_b64_tr_b16 v[184:185], v226 offset:0\n"
"ds_read_b64_tr_b16 v[186:187], v226 offset:512\n"
"v_mfma_f32_16x16x32_bf16 v[172:175], v[188:191], v[220:223], v[172:175]\n"
"s_waitcnt lgkmcnt(7)\n"
"v_mfma_f32_16x16x32_bf16 v[16:19], v[176:179], v[192:195], v[16:19]\n"
"ds_read_b64_tr_b16 v[188:189], v227 offset:0\n"
"s_waitcnt lgkmcnt(7)\n"
"v_mfma_f32_16x16x32_bf16 v[20:23], v[176:179], v[196:199], v[20:23]\n"
"ds_read_b64_tr_b16 v[190:191], v227 offset:512\n"
"global_load_lds_dwordx4 v237, s[90:91] offset:2048\n"
"s_waitcnt lgkmcnt(7)\n"
"v_mfma_f32_16x16x32_bf16 v[24:27], v[176:179], v[200:203], v[24:27]\n"
"s_waitcnt lgkmcnt(6)\n"
"v_mfma_f32_16x16x32_bf16 v[28:31], v[176:179], v[204:207], v[28:31]\n"
"s_waitcnt lgkmcnt(4)\n"
"v_mfma_f32_16x16x32_bf16 v[32:35], v[180:183], v[192:195], v[32:35]\n"
"ds_read_b64_tr_b16 v[176:177], v228 offset:0\n"
"global_load_lds_dwordx4 v238, s[90:91] offset:3072\n"
"s_add_u32 s90, s90, 0x48000\n"
"s_addc_u32 s91, s91, 0\n"
"v_mfma_f32_16x16x32_bf16 v[36:39], v[180:183], v[196:199], v[36:39]\n"
"ds_read_b64_tr_b16 v[178:179], v228 offset:512\n"
"v_mfma_f32_16x16x32_bf16 v[40:43], v[180:183], v[200:203], v[40:43]\n"
"v_mfma_f32_16x16x32_bf16 v[44:47], v[180:183], v[204:207], v[44:47]\n"
"s_waitcnt lgkmcnt(4)\n"
"v_mfma_f32_16x16x32_bf16 v[48:51], v[184:187], v[192:195], v[48:51]\n"
"v_mfma_f32_16x16x32_bf16 v[52:55], v[184:187], v[196:199], v[52:55]\n"
"v_mfma_f32_16x16x32_bf16 v[56:59], v[184:187], v[200:203], v[56:59]\n"
"v_mfma_f32_16x16x32_bf16 v[60:63], v[184:187], v[204:207], v[60:63]\n"
"s_waitcnt vmcnt(9) lgkmcnt(0)\n"
"s_barrier\n"
"ds_read_b64_tr_b16 v[180:181], v224 offset:36864\n"
"v_mfma_f32_16x16x32_bf16 v[64:67], v[188:191], v[192:195], v[64:67]\n"
"ds_read_b64_tr_b16 v[182:183], v224 offset:37376\n"
"s_mov_b32 m0, s82\n"
"v_mfma_f32_16x16x32_bf16 v[68:71], v[188:191], v[196:199], v[68:71]\n"
"global_load_lds_dwordx4 v235, s[88:89]\n"
"v_mfma_f32_16x16x32_bf16 v[72:75], v[188:191], v[200:203], v[72:75]\n"
"ds_read_b64_tr_b16 v[184:185], v225 offset:36864\n"
"v_mfma_f32_16x16x32_bf16 v[76:79], v[188:191], v[204:207], v[76:79]\n"
"ds_read_b64_tr_b16 v[186:187], v225 offset:37376\n"
"v_mfma_f32_16x16x32_bf16 v[80:83], v[176:179], v[192:195], v[80:83]\n"
"global_load_lds_dwordx4 v236, s[88:89] offset:1024\n"
"v_mfma_f32_16x16x32_bf16 v[84:87], v[176:179], v[196:199], v[84:87]\n"
"v_mfma_f32_16x16x32_bf16 v[88:91], v[176:179], v[200:203], v[88:91]\n"
"ds_read_b64_tr_b16 v[188:189], v226 offset:36864\n"
"ds_read_b64_tr_b16 v[190:191], v226 offset:37376\n"
"v_mfma_f32_16x16x32_bf16 v[92:95], v[176:179], v[204:207], v[92:95]\n"
"s_waitcnt lgkmcnt(4)\n"
"v_mfma_f32_16x16x32_bf16 v[96:99], v[180:183], v[192:195], v[96:99]\n"
"ds_read_b64_tr_b16 v[176:177], v227 offset:36864\n"
"v_mfma_f32_16x16x32_bf16 v[100:103], v[180:183], v[196:199], v[100:103]\n"
"ds_read_b64_tr_b16 v[178:179], v227 offset:37376\n"
"global_load_lds_dwordx4 v237, s[88:89] offset:2048\n"
"v_mfma_f32_16x16x32_bf16 v[104:107], v[180:183], v[200:203], v[104:107]\n"
"v_mfma_f32_16x16x32_bf16 v[108:111], v[180:183], v[204:207], v[108:111]\n"
"s_waitcnt lgkmcnt(4)\n"
"v_mfma_f32_16x16x32_bf16 v[112:115], v[184:187], v[192:195], v[112:115]\n"
"ds_read_b64_tr_b16 v[180:181], v228 offset:36864\n"
"global_load_lds_dwordx4 v238, s[88:89] offset:3072\n"
"s_add_u32 s88, s88, 0x48000\n"
"s_addc_u32 s89, s89, 0\n"
"v_mfma_f32_16x16x32_bf16 v[116:119], v[184:187], v[196:199], v[116:119]\n"
"ds_read_b64_tr_b16 v[182:183], v228 offset:37376\n"
"v_mfma_f32_16x16x32_bf16 v[120:123], v[184:187], v[200:203], v[120:123]\n"
"s_add_u32 m0, s84, 0x24000\n"
"v_mfma_f32_16x16x32_bf16 v[124:127], v[184:187], v[204:207], v[124:127]\n"
"global_load_lds_dwordx4 v240, s[80:81] sc1\n"
"s_add_u32 s80, s80, 0x1000\n"
"s_addc_u32 s81, s81, 0\n"
"s_waitcnt lgkmcnt(4)\n"
"v_mfma_f32_16x16x32_bf16 v[128:131], v[188:191], v[192:195], v[128:131]\n"
"v_mfma_f32_16x16x32_bf16 v[132:135], v[188:191], v[196:199], v[132:135]\n"
"v_mfma_f32_16x16x32_bf16 v[136:139], v[188:191], v[200:203], v[136:139]\n"
"v_mfma_f32_16x16x32_bf16 v[140:143], v[188:191], v[204:207], v[140:143]\n"
"s_waitcnt vmcnt(9) lgkmcnt(0)\n"
"s_barrier\n"
"ds_read_b128 v[208:211], v234 offset:8192\n"
"ds_read_b64_tr_b16 v[184:185], v229 offset:0\n"
"v_mfma_f32_16x16x32_bf16 v[144:147], v[176:179], v[192:195], v[144:147]\n"
"ds_read_b64_tr_b16 v[186:187], v229 offset:512\n"
"s_add_u32 m0, s82, 0x9000\n"
"v_mfma_f32_16x16x32_bf16 v[148:151], v[176:179], v[196:199], v[148:151]\n"
"ds_read_b128 v[212:215], v234 offset:9216\n"
"global_load_lds_dwordx4 v235, s[90:91]\n"
"v_mfma_f32_16x16x32_bf16 v[152:155], v[176:179], v[200:203], v[152:155]\n"
"ds_read_b128 v[216:219], v234 offset:10240\n"
"ds_read_b128 v[220:223], v234 offset:11264\n"
"v_mfma_f32_16x16x32_bf16 v[156:159], v[176:179], v[204:207], v[156:159]\n"
"ds_read_b64_tr_b16 v[188:189], v230 offset:0\n"
"v_mfma_f32_16x16x32_bf16 v[160:163], v[180:183], v[192:195], v[160:163]\n"
"ds_read_b64_tr_b16 v[190:191], v230 offset:512\n"
"global_load_lds_dwordx4 v236, s[90:91] offset:1024\n"
"v_mfma_f32_16x16x32_bf16 v[164:167], v[180:183], v[196:199], v[164:167]\n"
"v_mfma_f32_16x16x32_bf16 v[168:171], v[180:183], v[200:203], v[168:171]\n"
"ds_read_b64_tr_b16 v[176:177], v231 offset:0\n"
"ds_read_b64_tr_b16 v[178:179], v231 offset:512\n"
"v_mfma_f32_16x16x32_bf16 v[172:175], v[180:183], v[204:207], v[172:175]\n"
"s_waitcnt lgkmcnt(7)\n"
"v_mfma_f32_16x16x32_bf16 v[16:19], v[184:187], v[208:211], v[16:19]\n"
"ds_read_b64_tr_b16 v[180:181], v232 offset:0\n"
"s_waitcnt lgkmcnt(7)\n"
"v_mfma_f32_16x16x32_bf16 v[20:23], v[184:187], v[212:215], v[20:23]\n"
"ds_read_b64_tr_b16 v[182:183], v232 offset:512\n"
"global_load_lds_dwordx4 v237, s[90:91] offset:2048\n"
"s_waitcnt lgkmcnt(7)\n"
"v_mfma_f32_16x16x32_bf16 v[24:27], v[184:187], v[216:219], v[24:27]\n"
"s_waitcnt lgkmcnt(6)\n"
"v_mfma_f32_16x16x32_bf16 v[28:31], v[184:187], v[220:223], v[28:31]\n"
"s_waitcnt lgkmcnt(4)\n"
"v_mfma_f32_16x16x32_bf16 v[32:35], v[188:191], v[208:211], v[32:35]\n"
"ds_read_b64_tr_b16 v[184:185], v233 offset:0\n"
"global_load_lds_dwordx4 v238, s[90:91] offset:3072\n"
"s_add_u32 s90, s90, 0x48000\n"
"s_addc_u32 s91, s91, 0\n"
"v_mfma_f32_16x16x32_bf16 v[36:39], v[188:191], v[212:215], v[36:39]\n"
"ds_read_b64_tr_b16 v[186:187], v233 offset:512\n"
"v_mfma_f32_16x16x32_bf16 v[40:43], v[188:191], v[216:219], v[40:43]\n"
"v_mfma_f32_16x16x32_bf16 v[44:47], v[188:191], v[220:223], v[44:47]\n"
"s_waitcnt lgkmcnt(4)\n"
"v_mfma_f32_16x16x32_bf16 v[48:51], v[176:179], v[208:211], v[48:51]\n"
"v_mfma_f32_16x16x32_bf16 v[52:55], v[176:179], v[212:215], v[52:55]\n"
"v_mfma_f32_16x16x32_bf16 v[56:59], v[176:179], v[216:219], v[56:59]\n"
"v_mfma_f32_16x16x32_bf16 v[60:63], v[176:179], v[220:223], v[60:63]\n"
"s_waitcnt vmcnt(9) lgkmcnt(0)\n"
"s_barrier\n"
"ds_read_b64_tr_b16 v[188:189], v229 offset:36864\n"
"v_mfma_f32_16x16x32_bf16 v[64:67], v[180:183], v[208:211], v[64:67]\n"
"ds_read_b64_tr_b16 v[190:191], v229 offset:37376\n"
"s_add_u32 m0, s82, 0x12000\n"
"v_mfma_f32_16x16x32_bf16 v[68:71], v[180:183], v[212:215], v[68:71]\n"
"global_load_lds_dwordx4 v235, s[88:89]\n"
"v_mfma_f32_16x16x32_bf16 v[72:75], v[180:183], v[216:219], v[72:75]\n"
"ds_read_b64_tr_b16 v[176:177], v230 offset:36864\n"
"v_mfma_f32_16x16x32_bf16 v[76:79], v[180:183], v[220:223], v[76:79]\n"
"ds_read_b64_tr_b16 v[178:179], v230 offset:37376\n"
"v_mfma_f32_16x16x32_bf16 v[80:83], v[184:187], v[208:211], v[80:83]\n"
"global_load_lds_dwordx4 v236, s[88:89] offset:1024\n"
"v_mfma_f32_16x16x32_bf16 v[84:87], v[184:187], v[212:215], v[84:87]\n"
"v_mfma_f32_16x16x32_bf16 v[88:91], v[184:187], v[216:219], v[88:91]\n"
"ds_read_b64_tr_b16 v[180:181], v231 offset:36864\n"
"ds_read_b64_tr_b16 v[182:183], v231 offset:37376\n"
"v_mfma_f32_16x16x32_bf16 v[92:95], v[184:187], v[220:223], v[92:95]\n"
"s_waitcnt lgkmcnt(4)\n"
"v_mfma_f32_16x16x32_bf16 v[96:99], v[188:191], v[208:211], v[96:99]\n"
"ds_read_b64_tr_b16 v[184:185], v232 offset:36864\n"
"v_mfma_f32_16x16x32_bf16 v[100:103], v[188:191], v[212:215], v[100:103]\n"
"ds_read_b64_tr_b16 v[186:187], v232 offset:37376\n"
"global_load_lds_dwordx4 v237, s[88:89] offset:2048\n"
"v_mfma_f32_16x16x32_bf16 v[104:107], v[188:191], v[216:219], v[104:107]\n"
"v_mfma_f32_16x16x32_bf16 v[108:111], v[188:191], v[220:223], v[108:111]\n"
"s_waitcnt lgkmcnt(4)\n"
"v_mfma_f32_16x16x32_bf16 v[112:115], v[176:179], v[208:211], v[112:115]\n"
"ds_read_b64_tr_b16 v[188:189], v233 offset:36864\n"
"global_load_lds_dwordx4 v238, s[88:89] offset:3072\n"
"s_add_u32 s88, s88, 0x48000\n"
"s_addc_u32 s89, s89, 0\n"
"v_mfma_f32_16x16x32_bf16 v[116:119], v[176:179], v[212:215], v[116:119]\n"
"ds_read_b64_tr_b16 v[190:191], v233 offset:37376\n"
"v_mfma_f32_16x16x32_bf16 v[120:123], v[176:179], v[216:219], v[120:123]\n"
"s_add_u32 m0, s84, 0x25000\n"
"v_mfma_f32_16x16x32_bf16 v[124:127], v[176:179], v[220:223], v[124:127]\n"
"global_load_lds_dwordx4 v240, s[80:81] sc1\n"
"s_add_u32 s80, s80, 0x1000\n"
"s_addc_u32 s81, s81, 0\n"
"s_waitcnt lgkmcnt(4)\n"
"v_mfma_f32_16x16x32_bf16 v[128:131], v[180:183], v[208:211], v[128:131]\n"
"v_mfma_f32_16x16x32_bf16 v[132:135], v[180:183], v[212:215], v[132:135]\n"
"v_mfma_f32_16x16x32_bf16 v[136:139], v[180:183], v[216:219], v[136:139]\n"
"v_mfma_f32_16x16x32_bf16 v[140:143], v[180:183], v[220:223], v[140:143]\n"
"s_waitcnt vmcnt(9) lgkmcnt(0)\n"
"s_barrier\n"
"ds_read_b128 v[192:195], v234 offset:0\n"
"ds_read_b64_tr_b16 v[176:177], v224 offset:0\n"
"v_mfma_f32_16x16x32_bf16 v[144:147], v[184:187], v[208:211], v[144:147]\n"
"ds_read_b64_tr_b16 v[178:179], v224 offset:512\n"
"s_add_u32 m0, s82, 0x1b000\n"
"v_mfma_f32_16x16x32_bf16 v[148:151], v[184:187], v[212:215], v[148:151]\n"
"ds_read_b128 v[196:199], v234 offset:1024\n"
"global_load_lds_dwordx4 v235, s[90:91]\n"
"v_mfma_f32_16x16x32_bf16 v[152:155], v[184:187], v[216:219], v[152:155]\n"
"ds_read_b128 v[200:203], v234 offset:2048\n"
"ds_read_b128 v[204:207], v234 offset:3072\n"
"v_mfma_f32_16x16x32_bf16 v[156:159], v[184:187], v[220:223], v[156:159]\n"
"ds_read_b64_tr_b16 v[180:181], v225 offset:0\n"
"v_mfma_f32_16x16x32_bf16 v[160:163], v[188:191], v[208:211], v[160:163]\n"
"ds_read_b64_tr_b16 v[182:183], v225 offset:512\n"
"global_load_lds_dwordx4 v236, s[90:91] offset:1024\n"
"v_mfma_f32_16x16x32_bf16 v[164:167], v[188:191], v[212:215], v[164:167]\n"
"v_mfma_f32_16x16x32_bf16 v[168:171], v[188:191], v[216:219], v[168:171]\n"
"ds_read_b64_tr_b16 v[184:185], v226 offset:0\n"
"ds_read_b64_tr_b16 v[186:187], v226 offset:512\n"
"v_mfma_f32_16x16x32_bf16 v[172:175], v[188:191], v[220:223], v[172:175]\n"
"s_waitcnt lgkmcnt(7)\n"
"v_mfma_f32_16x16x32_bf16 v[16:19], v[176:179], v[192:195], v[16:19]\n"
"ds_read_b64_tr_b16 v[188:189], v227 offset:0\n"
"s_waitcnt lgkmcnt(7)\n"
"v_mfma_f32_16x16x32_bf16 v[20:23], v[176:179], v[196:199], v[20:23]\n"
"ds_read_b64_tr_b16 v[190:191], v227 offset:512\n"
"global_load_lds_dwordx4 v237, s[90:91] offset:2048\n"
"s_waitcnt lgkmcnt(7)\n"
"v_mfma_f32_16x16x32_bf16 v[24:27], v[176:179], v[200:203], v[24:27]\n"
"s_waitcnt lgkmcnt(6)\n"
"v_mfma_f32_16x16x32_bf16 v[28:31], v[176:179], v[204:207], v[28:31]\n"
"s_waitcnt lgkmcnt(4)\n"
"v_mfma_f32_16x16x32_bf16 v[32:35], v[180:183], v[192:195], v[32:35]\n"
"ds_read_b64_tr_b16 v[176:177], v228 offset:0\n"
"global_load_lds_dwordx4 v238, s[90:91] offset:3072\n"
"s_add_u32 s90, s90, 0x48000\n"
"s_addc_u32 s91, s91, 0\n"
"v_mfma_f32_16x16x32_bf16 v[36:39], v[180:183], v[196:199], v[36:39]\n"
"ds_read_b64_tr_b16 v[178:179], v228 offset:512\n"
"v_mfma_f32_16x16x32_bf16 v[40:43], v[180:183], v[200:203], v[40:43]\n"
"v_mfma_f32_16x16x32_bf16 v[44:47], v[180:183], v[204:207], v[44:47]\n"
"s_waitcnt lgkmcnt(4)\n"
"v_mfma_f32_16x16x32_bf16 v[48:51], v[184:187], v[192:195], v[48:51]\n"
"v_mfma_f32_16x16x32_bf16 v[52:55], v[184:187], v[196:199], v[52:55]\n"
"v_mfma_f32_16x16x32_bf16 v[56:59], v[184:187], v[200:203], v[56:59]\n"
"v_mfma_f32_16x16x32_bf16 v[60:63], v[184:187], v[204:207], v[60:63]\n"
"s_waitcnt vmcnt(9) lgkmcnt(0)\n"
"s_barrier\n"
"ds_read_b64_tr_b16 v[180:181], v224 offset:36864\n"
"v_mfma_f32_16x16x32_bf16 v[64:67], v[188:191], v[192:195], v[64:67]\n"
"ds_read_b64_tr_b16 v[182:183], v224 offset:37376\n"
"s_mov_b32 m0, s82\n"
"v_mfma_f32_16x16x32_bf16 v[68:71], v[188:191], v[196:199], v[68:71]\n"
"global_load_lds_dwordx4 v235, s[88:89]\n"
"v_mfma_f32_16x16x32_bf16 v[72:75], v[188:191], v[200:203], v[72:75]\n"
"ds_read_b64_tr_b16 v[184:185], v225 offset:36864\n"
"v_mfma_f32_16x16x32_bf16 v[76:79], v[188:191], v[204:207], v[76:79]\n"
"ds_read_b64_tr_b16 v[186:187], v225 offset:37376\n"
"v_mfma_f32_16x16x32_bf16 v[80:83], v[176:179], v[192:195], v[80:83]\n"
"global_load_lds_dwordx4 v236, s[88:89] offset:1024\n"
"v_mfma_f32_16x16x32_bf16 v[84:87], v[176:179], v[196:199], v[84:87]\n"
"v_mfma_f32_16x16x32_bf16 v[88:91], v[176:179], v[200:203], v[88:91]\n"
"ds_read_b64_tr_b16 v[188:189], v226 offset:36864\n"
"ds_read_b64_tr_b16 v[190:191], v226 offset:37376\n"
"v_mfma_f32_16x16x32_bf16 v[92:95], v[176:179], v[204:207], v[92:95]\n"
"s_waitcnt lgkmcnt(4)\n"
"v_mfma_f32_16x16x32_bf16 v[96:99], v[180:183], v[192:195], v[96:99]\n"
"ds_read_b64_tr_b16 v[176:177], v227 offset:36864\n"
"v_mfma_f32_16x16x32_bf16 v[100:103], v[180:183], v[196:199], v[100:103]\n"
"ds_read_b64_tr_b16 v[178:179], v227 offset:37376\n"
"global_load_lds_dwordx4 v237, s[88:89] offset:2048\n"
"v_mfma_f32_16x16x32_bf16 v[104:107], v[180:183], v[200:203], v[104:107]\n"
"v_mfma_f32_16x16x32_bf16 v[108:111], v[180:183], v[204:207], v[108:111]\n"
"s_waitcnt lgkmcnt(4)\n"
"v_mfma_f32_16x16x32_bf16 v[112:115], v[184:187], v[192:195], v[112:115]\n"
"ds_read_b64_tr_b16 v[180:181], v228 offset:36864\n"
"global_load_lds_dwordx4 v238, s[88:89] offset:3072\n"
"s_add_u32 s88, s88, 0x48000\n"
"s_addc_u32 s89, s89, 0\n"
"v_mfma_f32_16x16x32_bf16 v[116:119], v[184:187], v[196:199], v[116:119]\n"
"ds_read_b64_tr_b16 v[182:183], v228 offset:37376\n"
"v_mfma_f32_16x16x32_bf16 v[120:123], v[184:187], v[200:203], v[120:123]\n"
"s_add_u32 m0, s84, 0x26000\n"
"v_mfma_f32_16x16x32_bf16 v[124:127], v[184:187], v[204:207], v[124:127]\n"
"global_load_lds_dwordx4 v240, s[80:81] sc1\n"
"s_add_u32 s80, s80, 0x1000\n"
"s_addc_u32 s81, s81, 0\n"
"s_waitcnt lgkmcnt(4)\n"
"v_mfma_f32_16x16x32_bf16 v[128:131], v[188:191], v[192:195], v[128:131]\n"
"v_mfma_f32_16x16x32_bf16 v[132:135], v[188:191], v[196:199], v[132:135]\n"
"v_mfma_f32_16x16x32_bf16 v[136:139], v[188:191], v[200:203], v[136:139]\n"
"v_mfma_f32_16x16x32_bf16 v[140:143], v[188:191], v[204:207], v[140:143]\n"
"s_waitcnt vmcnt(9) lgkmcnt(0)\n"
"s_barrier\n"
"ds_read_b128 v[208:211], v234 offset:4096\n"
"ds_read_b64_tr_b16 v[184:185], v229 offset:0\n"
"v_mfma_f32_16x16x32_bf16 v[144:147], v[176:179], v[192:195], v[144:147]\n"
"ds_read_b64_tr_b16 v[186:187], v229 offset:512\n"
"s_add_u32 m0, s82, 0x9000\n"
"v_mfma_f32_16x16x32_bf16 v[148:151], v[176:179], v[196:199], v[148:151]\n"
"ds_read_b128 v[212:215], v234 offset:5120\n"
"global_load_lds_dwordx4 v235, s[90:91]\n"
"v_mfma_f32_16x16x32_bf16 v[152:155], v[176:179], v[200:203], v[152:155]\n"
"ds_read_b128 v[216:219], v234 offset:6144\n"
"ds_read_b128 v[220:223], v234 offset:7168\n"
"v_mfma_f32_16x16x32_bf16 v[156:159], v[176:179], v[204:207], v[156:159]\n"
"ds_read_b64_tr_b16 v[188:189], v230 offset:0\n"
"v_mfma_f32_16x16x32_bf16 v[160:163], v[180:183], v[192:195], v[160:163]\n"
"ds_read_b64_tr_b16 v[190:191], v230 offset:512\n"
"global_load_lds_dwordx4 v236, s[90:91] offset:1024\n"
"v_mfma_f32_16x16x32_bf16 v[164:167], v[180:183], v[196:199], v[164:167]\n"
"v_mfma_f32_16x16x32_bf16 v[168:171], v[180:183], v[200:203], v[168:171]\n"
"ds_read_b64_tr_b16 v[176:177], v231 offset:0\n"
"ds_read_b64_tr_b16 v[178:179], v231 offset:512\n"
"v_mfma_f32_16x16x32_bf16 v[172:175], v[180:183], v[204:207], v[172:175]\n"
"s_waitcnt lgkmcnt(7)\n"
"v_mfma_f32_16x16x32_bf16 v[16:19], v[184:187], v[208:211], v[16:19]\n"
"ds_read_b64_tr_b16 v[180:181], v232 offset:0\n"
"s_waitcnt lgkmcnt(7)\n"
"v_mfma_f32_16x16x32_bf16 v[20:23], v[184:187], v[212:215], v[20:23]\n"
"ds_read_b64_tr_b16 v[182:183], v232 offset:512\n"
"global_load_lds_dwordx4 v237, s[90:91] offset:2048\n"
"s_waitcnt lgkmcnt(7)\n"
"v_mfma_f32_16x16x32_bf16 v[24:27], v[184:187], v[216:219], v[24:27]\n"
"s_waitcnt lgkmcnt(6)\n"
"v_mfma_f32_16x16x32_bf16 v[28:31], v[184:187], v[220:223], v[28:31]\n"
"s_waitcnt lgkmcnt(4)\n"
"v_mfma_f32_16x16x32_bf16 v[32:35], v[188:191], v[208:211], v[32:35]\n"
"ds_read_b64_tr_b16 v[184:185], v233 offset:0\n"
"global_load_lds_dwordx4 v238, s[90:91] offset:3072\n"
"s_add_u32 s90, s90, 0x48000\n"
"s_addc_u32 s91, s91, 0\n"
"v_mfma_f32_16x16x32_bf16 v[36:39], v[188:191], v[212:215], v[36:39]\n"
"ds_read_b64_tr_b16 v[186:187], v233 offset:512\n"
"v_mfma_f32_16x16x32_bf16 v[40:43], v[188:191], v[216:219], v[40:43]\n"
"v_mfma_f32_16x16x32_bf16 v[44:47], v[188:191], v[220:223], v[44:47]\n"
"s_waitcnt lgkmcnt(4)\n"
"v_mfma_f32_16x16x32_bf16 v[48:51], v[176:179], v[208:211], v[48:51]\n"
"v_mfma_f32_16x16x32_bf16 v[52:55], v[176:179], v[212:215], v[52:55]\n"
"v_mfma_f32_16x16x32_bf16 v[56:59], v[176:179], v[216:219], v[56:59]\n"
"v_mfma_f32_16x16x32_bf16 v[60:63], v[176:179], v[220:223], v[60:63]\n"
"s_waitcnt vmcnt(9) lgkmcnt(0)\n"
"s_barrier\n"
"ds_read_b64_tr_b16 v[188:189], v229 offset:36864\n"
"v_mfma_f32_16x16x32_bf16 v[64:67], v[180:183], v[208:211], v[64:67]\n"
"ds_read_b64_tr_b16 v[190:191], v229 offset:37376\n"
"s_add_u32 m0, s82, 0x12000\n"
"v_mfma_f32_16x16x32_bf16 v[68:71], v[180:183], v[212:215], v[68:71]\n"
"global_load_lds_dwordx4 v235, s[88:89]\n"
"v_mfma_f32_16x16x32_bf16 v[72:75], v[180:183], v[216:219], v[72:75]\n"
"ds_read_b64_tr_b16 v[176:177], v230 offset:36864\n"
"v_mfma_f32_16x16x32_bf16 v[76:79], v[180:183], v[220:223], v[76:79]\n"
"ds_read_b64_tr_b16 v[178:179], v230 offset:37376\n"
"v_mfma_f32_16x16x32_bf16 v[80:83], v[184:187], v[208:211], v[80:83]\n"
"global_load_lds_dwordx4 v236, s[88:89] offset:1024\n"
"v_mfma_f32_16x16x32_bf16 v[84:87], v[184:187], v[212:215], v[84:87]\n"
"v_mfma_f32_16x16x32_bf16 v[88:91], v[184:187], v[216:219], v[88:91]\n"
"ds_read_b64_tr_b16 v[180:181], v231 offset:36864\n"
"ds_read_b64_tr_b16 v[182:183], v231 offset:37376\n"
"v_mfma_f32_16x16x32_bf16 v[92:95], v[184:187], v[220:223], v[92:95]\n"
"s_waitcnt lgkmcnt(4)\n"
"v_mfma_f32_16x16x32_bf16 v[96:99], v[188:191], v[208:211], v[96:99]\n"
"ds_read_b64_tr_b16 v[184:185], v232 offset:36864\n"
"v_mfma_f32_16x16x32_bf16 v[100:103], v[188:191], v[212:215], v[100:103]\n"
"ds_read_b64_tr_b16 v[186:187], v232 offset:37376\n"
"global_load_lds_dwordx4 v237, s[88:89] offset:2048\n"
"v_mfma_f32_16x16x32_bf16 v[104:107], v[188:191], v[216:219], v[104:107]\n"
"v_mfma_f32_16x16x32_bf16 v[108:111], v[188:191], v[220:223], v[108:111]\n"
"s_waitcnt lgkmcnt(4)\n"
"v_mfma_f32_16x16x32_bf16 v[112:115], v[176:179], v[208:211], v[112:115]\n"
"ds_read_b64_tr_b16 v[188:189], v233 offset:36864\n"
"global_load_lds_dwordx4 v238, s[88:89] offset:3072\n"
"s_add_u32 s88, s88, 0x48000\n"
"s_addc_u32 s89, s89, 0\n"
"v_mfma_f32_16x16x32_bf16 v[116:119], v[176:179], v[212:215], v[116:119]\n"
"ds_read_b64_tr_b16 v[190:191], v233 offset:37376\n"
"v_mfma_f32_16x16x32_bf16 v[120:123], v[176:179], v[216:219], v[120:123]\n"
"s_add_u32 m0, s84, 0x24000\n"
"v_mfma_f32_16x16x32_bf16 v[124:127], v[176:179], v[220:223], v[124:127]\n"
"global_load_lds_dwordx4 v240, s[80:81] sc1\n"
"s_add_u32 s80, s80, 0x1000\n"
"s_addc_u32 s81, s81, 0\n"
"s_waitcnt lgkmcnt(4)\n"
"v_mfma_f32_16x16x32_bf16 v[128:131], v[180:183], v[208:211], v[128:131]\n"
"v_mfma_f32_16x16x32_bf16 v[132:135], v[180:183], v[212:215], v[132:135]\n"
"v_mfma_f32_16x16x32_bf16 v[136:139], v[180:183], v[216:219], v[136:139]\n"
"v_mfma_f32_16x16x32_bf16 v[140:143], v[180:183], v[220:223], v[140:143]\n"
"s_waitcnt vmcnt(9) lgkmcnt(0)\n"
"s_barrier\n"
"ds_read_b128 v[192:195], v234 offset:8192\n"
"ds_read_b64_tr_b16 v[176:177], v224 offset:0\n"
"v_mfma_f32_16x16x32_bf16 v[144:147], v[184:187], v[208:211], v[144:147]\n"
"ds_read_b64_tr_b16 v[178:179], v224 offset:512\n"
"s_add_u32 m0, s82, 0x1b000\n"
"v_mfma_f32_16x16x32_bf16 v[148:151], v[184:187], v[212:215], v[148:151]\n"
"ds_read_b128 v[196:199], v234 offset:9216\n"
"global_load_lds_dwordx4 v235, s[90:91]\n"
"v_mfma_f32_16x16x32_bf16 v[152:155], v[184:187], v[216:219], v[152:155]\n"
"ds_read_b128 v[200:203], v234 offset:10240\n"
"ds_read_b128 v[204:207], v234 offset:11264\n"
"v_mfma_f32_16x16x32_bf16 v[156:159], v[184:187], v[220:223], v[156:159]\n"
"ds_read_b64_tr_b16 v[180:181], v225 offset:0\n"
"v_mfma_f32_16x16x32_bf16 v[160:163], v[188:191], v[208:211], v[160:163]\n"
"ds_read_b64_tr_b16 v[182:183], v225 offset:512\n"
"global_load_lds_dwordx4 v236, s[90:91] offset:1024\n"
"v_mfma_f32_16x16x32_bf16 v[164:167], v[188:191], v[212:215], v[164:167]\n"
"v_mfma_f32_16x16x32_bf16 v[168:171], v[188:191], v[216:219], v[168:171]\n"
"ds_read_b64_tr_b16 v[184:185], v226 offset:0\n"
"ds_read_b64_tr_b16 v[186:187], v226 offset:512\n"
"v_mfma_f32_16x16x32_bf16 v[172:175], v[188:191], v[220:223], v[172:175]\n"
"s_waitcnt lgkmcnt(7)\n"
"v_mfma_f32_16x16x32_bf16 v[16:19], v[176:179], v[192:195], v[16:19]\n"
"ds_read_b64_tr_b16 v[188:189], v227 offset:0\n"
"s_waitcnt lgkmcnt(7)\n"
"v_mfma_f32_16x16x32_bf16 v[20:23], v[176:179], v[196:199], v[20:23]\n"
"ds_read_b64_tr_b16 v[190:191], v227 offset:512\n"
"global_load_lds_dwordx4 v237, s[90:91] offset:2048\n"
"s_waitcnt lgkmcnt(7)\n"
"v_mfma_f32_16x16x32_bf16 v[24:27], v[176:179], v[200:203], v[24:27]\n"
"s_waitcnt lgkmcnt(6)\n"
"v_mfma_f32_16x16x32_bf16 v[28:31], v[176:179], v[204:207], v[28:31]\n"
"s_waitcnt lgkmcnt(4)\n"
"v_mfma_f32_16x16x32_bf16 v[32:35], v[180:183], v[192:195], v[32:35]\n"
"ds_read_b64_tr_b16 v[176:177], v228 offset:0\n"
"global_load_lds_dwordx4 v238, s[90:91] offset:3072\n"
"s_add_u32 s90, s90, 0x48000\n"
"s_addc_u32 s91, s91, 0\n"
"v_mfma_f32_16x16x32_bf16 v[36:39], v[180:183], v[196:199], v[36:39]\n"
"ds_read_b64_tr_b16 v[178:179], v228 offset:512\n"
"v_mfma_f32_16x16x32_bf16 v[40:43], v[180:183], v[200:203], v[40:43]\n"
"v_mfma_f32_16x16x32_bf16 v[44:47], v[180:183], v[204:207], v[44:47]\n"
"s_waitcnt lgkmcnt(4)\n"
"v_mfma_f32_16x16x32_bf16 v[48:51], v[184:187], v[192:195], v[48:51]\n"
"v_mfma_f32_16x16x32_bf16 v[52:55], v[184:187], v[196:199], v[52:55]\n"
"v_mfma_f32_16x16x32_bf16 v[56:59], v[184:187], v[200:203], v[56:59]\n"
"v_mfma_f32_16x16x32_bf16 v[60:63], v[184:187], v[204:207], v[60:63]\n"
"s_waitcnt vmcnt(9) lgkmcnt(0)\n"
"s_barrier\n"
"ds_read_b64_tr_b16 v[180:181], v224 offset:36864\n"
"v_mfma_f32_16x16x32_bf16 v[64:67], v[188:191], v[192:195], v[64:67]\n"
"ds_read_b64_tr_b16 v[182:183], v224 offset:37376\n"
"s_mov_b32 m0, s82\n"
"v_mfma_f32_16x16x32_bf16 v[68:71], v[188:191], v[196:199], v[68:71]\n"
"global_load_lds_dwordx4 v235, s[88:89]\n"
"v_mfma_f32_16x16x32_bf16 v[72:75], v[188:191], v[200:203], v[72:75]\n"
"ds_read_b64_tr_b16 v[184:185], v225 offset:36864\n"
"v_mfma_f32_16x16x32_bf16 v[76:79], v[188:191], v[204:207], v[76:79]\n"
"ds_read_b64_tr_b16 v[186:187], v225 offset:37376\n"
"v_mfma_f32_16x16x32_bf16 v[80:83], v[176:179], v[192:195], v[80:83]\n"
"global_load_lds_dwordx4 v236, s[88:89] offset:1024\n"
"v_mfma_f32_16x16x32_bf16 v[84:87], v[176:179], v[196:199], v[84:87]\n"
"v_mfma_f32_16x16x32_bf16 v[88:91], v[176:179], v[200:203], v[88:91]\n"
"ds_read_b64_tr_b16 v[188:189], v226 offset:36864\n"
"ds_read_b64_tr_b16 v[190:191], v226 offset:37376\n"
"v_mfma_f32_16x16x32_bf16 v[92:95], v[176:179], v[204:207], v[92:95]\n"
"s_waitcnt lgkmcnt(4)\n"
"v_mfma_f32_16x16x32_bf16 v[96:99], v[180:183], v[192:195], v[96:99]\n"
"ds_read_b64_tr_b16 v[176:177], v227 offset:36864\n"
"v_mfma_f32_16x16x32_bf16 v[100:103], v[180:183], v[196:199], v[100:103]\n"
"ds_read_b64_tr_b16 v[178:179], v227 offset:37376\n"
"global_load_lds_dwordx4 v237, s[88:89] offset:2048\n"
"v_mfma_f32_16x16x32_bf16 v[104:107], v[180:183], v[200:203], v[104:107]\n"
"v_mfma_f32_16x16x32_bf16 v[108:111], v[180:183], v[204:207], v[108:111]\n"
"s_waitcnt lgkmcnt(4)\n"
"v_mfma_f32_16x16x32_bf16 v[112:115], v[184:187], v[192:195], v[112:115]\n"
"ds_read_b64_tr_b16 v[180:181], v228 offset:36864\n"
"global_load_lds_dwordx4 v238, s[88:89] offset:3072\n"
"s_add_u32 s88, s88, 0x48000\n"
"s_addc_u32 s89, s89, 0\n"
"v_mfma_f32_16x16x32_bf16 v[116:119], v[184:187], v[196:199], v[116:119]\n"
"ds_read_b64_tr_b16 v[182:183], v228 offset:37376\n"
"v_mfma_f32_16x16x32_bf16 v[120:123], v[184:187], v[200:203], v[120:123]\n"
"s_add_u32 m0, s84, 0x25000\n"
"v_mfma_f32_16x16x32_bf16 v[124:127], v[184:187], v[204:207], v[124:127]\n"
"global_load_lds_dwordx4 v240, s[80:81] sc1\n"
"s_add_u32 s80, s80, 0x1000\n"
"s_addc_u32 s81, s81, 0\n"
"s_waitcnt lgkmcnt(4)\n"
"v_mfma_f32_16x16x32_bf16 v[128:131], v[188:191], v[192:195], v[128:131]\n"
"v_mfma_f32_16x16x32_bf16 v[132:135], v[188:191], v[196:199], v[132:135]\n"
"v_mfma_f32_16x16x32_bf16 v[136:139], v[188:191], v[200:203], v[136:139]\n"
"v_mfma_f32_16x16x32_bf16 v[140:143], v[188:191], v[204:207], v[140:143]\n"
"s_waitcnt vmcnt(9) lgkmcnt(0)\n"
"s_barrier\n"
"ds_read_b128 v[208:211], v234 offset:0\n"
"ds_read_b64_tr_b16 v[184:185], v229 offset:0\n"
"v_mfma_f32_16x16x32_bf16 v[144:147], v[176:179], v[192:195], v[144:147]\n"
"ds_read_b64_tr_b16 v[186:187], v229 offset:512\n"
"s_add_u32 m0, s82, 0x9000\n"
"v_mfma_f32_16x16x32_bf16 v[148:151], v[176:179], v[196:199], v[148:151]\n"
"ds_read_b128 v[212:215], v234 offset:1024\n"
"global_load_lds_dwordx4 v235, s[90:91]\n"
"v_mfma_f32_16x16x32_bf16 v[152:155], v[176:179], v[200:203], v[152:155]\n"
"ds_read_b128 v[216:219], v234 offset:2048\n"
"ds_read_b128 v[220:223], v234 offset:3072\n"
"v_mfma_f32_16x16x32_bf16 v[156:159], v[176:179], v[204:207], v[156:159]\n"
"ds_read_b64_tr_b16 v[188:189], v230 offset:0\n"
"v_mfma_f32_16x16x32_bf16 v[160:163], v[180:183], v[192:195], v[160:163]\n"
"ds_read_b64_tr_b16 v[190:191], v230 offset:512\n"
"global_load_lds_dwordx4 v236, s[90:91] offset:1024\n"
"v_mfma_f32_16x16x32_bf16 v[164:167], v[180:183], v[196:199], v[164:167]\n"
"v_mfma_f32_16x16x32_bf16 v[168:171], v[180:183], v[200:203], v[168:171]\n"
"ds_read_b64_tr_b16 v[176:177], v231 offset:0\n"
"ds_read_b64_tr_b16 v[178:179], v231 offset:512\n"
"v_mfma_f32_16x16x32_bf16 v[172:175], v[180:183], v[204:207], v[172:175]\n"
"s_waitcnt lgkmcnt(7)\n"
"v_mfma_f32_16x16x32_bf16 v[16:19], v[184:187], v[208:211], v[16:19]\n"
"ds_read_b64_tr_b16 v[180:181], v232 offset:0\n"
"s_waitcnt lgkmcnt(7)\n"
"v_mfma_f32_16x16x32_bf16 v[20:23], v[184:187], v[212:215], v[20:23]\n"
"ds_read_b64_tr_b16 v[182:183], v232 offset:512\n"
"global_load_lds_dwordx4 v237, s[90:91] offset:2048\n"
"s_waitcnt lgkmcnt(7)\n"
"v_mfma_f32_16x16x32_bf16 v[24:27], v[184:187], v[216:219], v[24:27]\n"
"s_waitcnt lgkmcnt(6)\n"
"v_mfma_f32_16x16x32_bf16 v[28:31], v[184:187], v[220:223], v[28:31]\n"
"s_waitcnt lgkmcnt(4)\n"
"v_mfma_f32_16x16x32_bf16 v[32:35], v[188:191], v[208:211], v[32:35]\n"
"ds_read_b64_tr_b16 v[184:185], v233 offset:0\n"
"global_load_lds_dwordx4 v238, s[90:91] offset:3072\n"
"s_add_u32 s90, s90, 0x48000\n"
"s_addc_u32 s91, s91, 0\n"
"v_mfma_f32_16x16x32_bf16 v[36:39], v[188:191], v[212:215], v[36:39]\n"
"ds_read_b64_tr_b16 v[186:187], v233 offset:512\n"
"v_mfma_f32_16x16x32_bf16 v[40:43], v[188:191], v[216:219], v[40:43]\n"
"v_mfma_f32_16x16x32_bf16 v[44:47], v[188:191], v[220:223], v[44:47]\n"
"s_waitcnt lgkmcnt(4)\n"
"v_mfma_f32_16x16x32_bf16 v[48:51], v[176:179], v[208:211], v[48:51]\n"
"v_mfma_f32_16x16x32_bf16 v[52:55], v[176:179], v[212:215], v[52:55]\n"
"v_mfma_f32_16x16x32_bf16 v[56:59], v[176:179], v[216:219], v[56:59]\n"
"v_mfma_f32_16x16x32_bf16 v[60:63], v[176:179], v[220:223], v[60:63]\n"
"s_waitcnt vmcnt(9) lgkmcnt(0)\n"
"s_barrier\n"
"ds_read_b64_tr_b16 v[188:189], v229 offset:36864\n"
"v_mfma_f32_16x16x32_bf16 v[64:67], v[180:183], v[208:211], v[64:67]\n"
"ds_read_b64_tr_b16 v[190:191], v229 offset:37376\n"
"s_add_u32 m0, s82, 0x12000\n"
"v_mfma_f32_16x16x32_bf16 v[68:71], v[180:183], v[212:215], v[68:71]\n"
"global_load_lds_dwordx4 v235, s[88:89]\n"
"v_mfma_f32_16x16x32_bf16 v[72:75], v[180:183], v[216:219], v[72:75]\n"
"ds_read_b64_tr_b16 v[176:177], v230 offset:36864\n"
"v_mfma_f32_16x16x32_bf16 v[76:79], v[180:183], v[220:223], v[76:79]\n"
"ds_read_b64_tr_b16 v[178:179], v230 offset:37376\n"
"v_mfma_f32_16x16x32_bf16 v[80:83], v[184:187], v[208:211], v[80:83]\n"
"global_load_lds_dwordx4 v236, s[88:89] offset:1024\n"
"v_mfma_f32_16x16x32_bf16 v[84:87], v[184:187], v[212:215], v[84:87]\n"
"v_mfma_f32_16x16x32_bf16 v[88:91], v[184:187], v[216:219], v[88:91]\n"
"ds_read_b64_tr_b16 v[180:181], v231 offset:36864\n"
"ds_read_b64_tr_b16 v[182:183], v231 offset:37376\n"
"v_mfma_f32_16x16x32_bf16 v[92:95], v[184:187], v[220:223], v[92:95]\n"
"s_waitcnt lgkmcnt(4)\n"
"v_mfma_f32_16x16x32_bf16 v[96:99], v[188:191], v[208:211], v[96:99]\n"
"ds_read_b64_tr_b16 v[184:185], v232 offset:36864\n"
"v_mfma_f32_16x16x32_bf16 v[100:103], v[188:191], v[212:215], v[100:103]\n"
"ds_read_b64_tr_b16 v[186:187], v232 offset:37376\n"
"global_load_lds_dwordx4 v237, s[88:89] offset:2048\n"
"v_mfma_f32_16x16x32_bf16 v[104:107], v[188:191], v[216:219], v[104:107]\n"
"v_mfma_f32_16x16x32_bf16 v[108:111], v[188:191], v[220:223], v[108:111]\n"
"s_waitcnt lgkmcnt(4)\n"
"v_mfma_f32_16x16x32_bf16 v[112:115], v[176:179], v[208:211], v[112:115]\n"
"ds_read_b64_tr_b16 v[188:189], v233 offset:36864\n"
"global_load_lds_dwordx4 v238, s[88:89] offset:3072\n"
"s_add_u32 s88, s88, 0x48000\n"
"s_addc_u32 s89, s89, 0\n"
"v_mfma_f32_16x16x32_bf16 v[116:119], v[176:179], v[212:215], v[116:119]\n"
"ds_read_b64_tr_b16 v[190:191], v233 offset:37376\n"
"v_mfma_f32_16x16x32_bf16 v[120:123], v[176:179], v[216:219], v[120:123]\n"
"s_add_u32 m0, s84, 0x26000\n"
"v_mfma_f32_16x16x32_bf16 v[124:127], v[176:179], v[220:223], v[124:127]\n"
"global_load_lds_dwordx4 v240, s[80:81] sc1\n"
"s_add_u32 s80, s80, 0x1000\n"
"s_addc_u32 s81, s81, 0\n"
"s_waitcnt lgkmcnt(4)\n"
"v_mfma_f32_16x16x32_bf16 v[128:131], v[180:183], v[208:211], v[128:131]\n"
"v_mfma_f32_16x16x32_bf16 v[132:135], v[180:183], v[212:215], v[132:135]\n"
"v_mfma_f32_16x16x32_bf16 v[136:139], v[180:183], v[216:219], v[136:139]\n"
"v_mfma_f32_16x16x32_bf16 v[140:143], v[180:183], v[220:223], v[140:143]\n"
"s_waitcnt vmcnt(9) lgkmcnt(0)\n"
"s_barrier\n"
"ds_read_b128 v[192:195], v234 offset:4096\n"
"ds_read_b64_tr_b16 v[176:177], v224 offset:0\n"
"v_mfma_f32_16x16x32_bf16 v[144:147], v[184:187], v[208:211], v[144:147]\n"
"ds_read_b64_tr_b16 v[178:179], v224 offset:512\n"
"s_add_u32 m0, s82, 0x1b000\n"
"v_mfma_f32_16x16x32_bf16 v[148:151], v[184:187], v[212:215], v[148:151]\n"
"ds_read_b128 v[196:199], v234 offset:5120\n"
"global_load_lds_dwordx4 v235, s[90:91]\n"
"v_mfma_f32_16x16x32_bf16 v[152:155], v[184:187], v[216:219], v[152:155]\n"
"ds_read_b128 v[200:203], v234 offset:6144\n"
"ds_read_b128 v[204:207], v234 offset:7168\n"
"v_mfma_f32_16x16x32_bf16 v[156:159], v[184:187], v[220:223], v[156:159]\n"
"ds_read_b64_tr_b16 v[180:181], v225 offset:0\n"
"v_mfma_f32_16x16x32_bf16 v[160:163], v[188:191], v[208:211], v[160:163]\n"
"ds_read_b64_tr_b16 v[182:183], v225 offset:512\n"
"global_load_lds_dwordx4 v236, s[90:91] offset:1024\n"
"v_mfma_f32_16x16x32_bf16 v[164:167], v[188:191], v[212:215], v[164:167]\n"
"v_mfma_f32_16x16x32_bf16 v[168:171], v[188:191], v[216:219], v[168:171]\n"
"ds_read_b64_tr_b16 v[184:185], v226 offset:0\n"
"ds_read_b64_tr_b16 v[186:187], v226 offset:512\n"
"v_mfma_f32_16x16x32_bf16 v[172:175], v[188:191], v[220:223], v[172:175]\n"
"s_waitcnt lgkmcnt(7)\n"
"v_mfma_f32_16x16x32_bf16 v[16:19], v[176:179], v[192:195], v[16:19]\n"
"ds_read_b64_tr_b16 v[188:189], v227 offset:0\n"
"s_waitcnt lgkmcnt(7)\n"
"v_mfma_f32_16x16x32_bf16 v[20:23], v[176:179], v[196:199], v[20:23]\n"
"ds_read_b64_tr_b16 v[190:191], v227 offset:512\n"
"global_load_lds_dwordx4 v237, s[90:91] offset:2048\n"
"s_waitcnt lgkmcnt(7)\n"
"v_mfma_f32_16x16x32_bf16 v[24:27], v[176:179], v[200:203], v[24:27]\n"
"s_waitcnt lgkmcnt(6)\n"
"v_mfma_f32_16x16x32_bf16 v[28:31], v[176:179], v[204:207], v[28:31]\n"
"s_waitcnt lgkmcnt(4)\n"
"v_mfma_f32_16x16x32_bf16 v[32:35], v[180:183], v[192:195], v[32:35]\n"
"ds_read_b64_tr_b16 v[176:177], v228 offset:0\n"
"global_load_lds_dwordx4 v238, s[90:91] offset:3072\n"
"s_add_u32 s90, s90, 0x48000\n"
"s_addc_u32 s91, s91, 0\n"
"v_mfma_f32_16x16x32_bf16 v[36:39], v[180:183], v[196:199], v[36:39]\n"
"ds_read_b64_tr_b16 v[178:179], v228 offset:512\n"
"v_mfma_f32_16x16x32_bf16 v[40:43], v[180:183], v[200:203], v[40:43]\n"
"v_mfma_f32_16x16x32_bf16 v[44:47], v[180:183], v[204:207], v[44:47]\n"
"s_waitcnt lgkmcnt(4)\n"
"v_mfma_f32_16x16x32_bf16 v[48:51], v[184:187], v[192:195], v[48:51]\n"
"v_mfma_f32_16x16x32_bf16 v[52:55], v[184:187], v[196:199], v[52:55]\n"
"v_mfma_f32_16x16x32_bf16 v[56:59], v[184:187], v[200:203], v[56:59]\n"
"v_mfma_f32_16x16x32_bf16 v[60:63], v[184:187], v[204:207], v[60:63]\n"
"s_waitcnt vmcnt(9) lgkmcnt(0)\n"
"s_barrier\n"
"ds_read_b64_tr_b16 v[180:181], v224 offset:36864\n"
"v_mfma_f32_16x16x32_bf16 v[64:67], v[188:191], v[192:195], v[64:67]\n"
"ds_read_b64_tr_b16 v[182:183], v224 offset:37376\n"
"s_mov_b32 m0, s82\n"
"v_mfma_f32_16x16x32_bf16 v[68:71], v[188:191], v[196:199], v[68:71]\n"
"global_load_lds_dwordx4 v235, s[88:89]\n"
"v_mfma_f32_16x16x32_bf16 v[72:75], v[188:191], v[200:203], v[72:75]\n"
"ds_read_b64_tr_b16 v[184:185], v225 offset:36864\n"
"v_mfma_f32_16x16x32_bf16 v[76:79], v[188:191], v[204:207], v[76:79]\n"
"ds_read_b64_tr_b16 v[186:187], v225 offset:37376\n"
"v_mfma_f32_16x16x32_bf16 v[80:83], v[176:179], v[192:195], v[80:83]\n"
"global_load_lds_dwordx4 v236, s[88:89] offset:1024\n"
"v_mfma_f32_16x16x32_bf16 v[84:87], v[176:179], v[196:199], v[84:87]\n"
"v_mfma_f32_16x16x32_bf16 v[88:91], v[176:179], v[200:203], v[88:91]\n"
"ds_read_b64_tr_b16 v[188:189], v226 offset:36864\n"
"ds_read_b64_tr_b16 v[190:191], v226 offset:37376\n"
"v_mfma_f32_16x16x32_bf16 v[92:95], v[176:179], v[204:207], v[92:95]\n"
"s_waitcnt lgkmcnt(4)\n"
"v_mfma_f32_16x16x32_bf16 v[96:99], v[180:183], v[192:195], v[96:99]\n"
"ds_read_b64_tr_b16 v[176:177], v227 offset:36864\n"
"v_mfma_f32_16x16x32_bf16 v[100:103], v[180:183], v[196:199], v[100:103]\n"
"ds_read_b64_tr_b16 v[178:179], v227 offset:37376\n"
"global_load_lds_dwordx4 v237, s[88:89] offset:2048\n"
"v_mfma_f32_16x16x32_bf16 v[104:107], v[180:183], v[200:203], v[104:107]\n"
"v_mfma_f32_16x16x32_bf16 v[108:111], v[180:183], v[204:207], v[108:111]\n"
"s_waitcnt lgkmcnt(4)\n"
"v_mfma_f32_16x16x32_bf16 v[112:115], v[184:187], v[192:195], v[112:115]\n"
"ds_read_b64_tr_b16 v[180:181], v228 offset:36864\n"
"global_load_lds_dwordx4 v238, s[88:89] offset:3072\n"
"s_add_u32 s88, s88, 0x48000\n"
"s_addc_u32 s89, s89, 0\n"
"v_mfma_f32_16x16x32_bf16 v[116:119], v[184:187], v[196:199], v[116:119]\n"
"ds_read_b64_tr_b16 v[182:183], v228 offset:37376\n"
"v_mfma_f32_16x16x32_bf16 v[120:123], v[184:187], v[200:203], v[120:123]\n"
"s_add_u32 m0, s84, 0x24000\n"
"v_mfma_f32_16x16x32_bf16 v[124:127], v[184:187], v[204:207], v[124:127]\n"
"global_load_lds_dwordx4 v240, s[80:81] sc1\n"
"s_add_u32 s80, s80, 0x1000\n"
"s_addc_u32 s81, s81, 0\n"
"s_waitcnt lgkmcnt(4)\n"
"v_mfma_f32_16x16x32_bf16 v[128:131], v[188:191], v[192:195], v[128:131]\n"
"v_mfma_f32_16x16x32_bf16 v[132:135], v[188:191], v[196:199], v[132:135]\n"
"v_mfma_f32_16x16x32_bf16 v[136:139], v[188:191], v[200:203], v[136:139]\n"
"v_mfma_f32_16x16x32_bf16 v[140:143], v[188:191], v[204:207], v[140:143]\n"
"s_waitcnt vmcnt(9) lgkmcnt(0)\n"
"s_barrier\n"
"ds_read_b128 v[208:211], v234 offset:8192\n"
"ds_read_b64_tr_b16 v[184:185], v229 offset:0\n"
"v_mfma_f32_16x16x32_bf16 v[144:147], v[176:179], v[192:195], v[144:147]\n"
"ds_read_b64_tr_b16 v[186:187], v229 offset:512\n"
"s_add_u32 m0, s82, 0x9000\n"
"v_mfma_f32_16x16x32_bf16 v[148:151], v[176:179], v[196:199], v[148:151]\n"
"ds_read_b128 v[212:215], v234 offset:9216\n"
"global_load_lds_dwordx4 v235, s[90:91]\n"
"v_mfma_f32_16x16x32_bf16 v[152:155], v[176:179], v[200:203], v[152:155]\n"
"ds_read_b128 v[216:219], v234 offset:10240\n"
"ds_read_b128 v[220:223], v234 offset:11264\n"
"v_mfma_f32_16x16x32_bf16 v[156:159], v[176:179], v[204:207], v[156:159]\n"
"ds_read_b64_tr_b16 v[188:189], v230 offset:0\n"
"v_mfma_f32_16x16x32_bf16 v[160:163], v[180:183], v[192:195], v[160:163]\n"
"ds_read_b64_tr_b16 v[190:191], v230 offset:512\n"
"global_load_lds_dwordx4 v236, s[90:91] offset:1024\n"
"v_mfma_f32_16x16x32_bf16 v[164:167], v[180:183], v[196:199], v[164:167]\n"
"v_mfma_f32_16x16x32_bf16 v[168:171], v[180:183], v[200:203], v[168:171]\n"
"ds_read_b64_tr_b16 v[176:177], v231 offset:0\n"
"ds_read_b64_tr_b16 v[178:179], v231 offset:512\n"
"v_mfma_f32_16x16x32_bf16 v[172:175], v[180:183], v[204:207], v[172:175]\n"
"s_waitcnt lgkmcnt(7)\n"
"v_mfma_f32_16x16x32_bf16 v[16:19], v[184:187], v[208:211], v[16:19]\n"
"ds_read_b64_tr_b16 v[180:181], v232 offset:0\n"
"s_waitcnt lgkmcnt(7)\n"
"v_mfma_f32_16x16x32_bf16 v[20:23], v[184:187], v[212:215], v[20:23]\n"
"ds_read_b64_tr_b16 v[182:183], v232 offset:512\n"
"global_load_lds_dwordx4 v237, s[90:91] offset:2048\n"
"s_waitcnt lgkmcnt(7)\n"
"v_mfma_f32_16x16x32_bf16 v[24:27], v[184:187], v[216:219], v[24:27]\n"
"s_waitcnt lgkmcnt(6)\n"
"v_mfma_f32_16x16x32_bf16 v[28:31], v[184:187], v[220:223], v[28:31]\n"
"s_waitcnt lgkmcnt(4)\n"
"v_mfma_f32_16x16x32_bf16 v[32:35], v[188:191], v[208:211], v[32:35]\n"
"ds_read_b64_tr_b16 v[184:185], v233 offset:0\n"
"global_load_lds_dwordx4 v238, s[90:91] offset:3072\n"
"s_add_u32 s90, s90, 0x48000\n"
"s_addc_u32 s91, s91, 0\n"
"v_mfma_f32_16x16x32_bf16 v[36:39], v[188:191], v[212:215], v[36:39]\n"
"ds_read_b64_tr_b16 v[186:187], v233 offset:512\n"
"v_mfma_f32_16x16x32_bf16 v[40:43], v[188:191], v[216:219], v[40:43]\n"
"v_mfma_f32_16x16x32_bf16 v[44:47], v[188:191], v[220:223], v[44:47]\n"
"s_waitcnt lgkmcnt(4)\n"
"v_mfma_f32_16x16x32_bf16 v[48:51], v[176:179], v[208:211], v[48:51]\n"
"v_mfma_f32_16x16x32_bf16 v[52:55], v[176:179], v[212:215], v[52:55]\n"
"v_mfma_f32_16x16x32_bf16 v[56:59], v[176:179], v[216:219], v[56:59]\n"
"v_mfma_f32_16x16x32_bf16 v[60:63], v[176:179], v[220:223], v[60:63]\n"
"s_waitcnt vmcnt(9) lgkmcnt(0)\n"
"s_barrier\n"
"ds_read_b64_tr_b16 v[188:189], v229 offset:36864\n"
"v_mfma_f32_16x16x32_bf16 v[64:67], v[180:183], v[208:211], v[64:67]\n"
"ds_read_b64_tr_b16 v[190:191], v229 offset:37376\n"
"s_add_u32 m0, s82, 0x12000\n"
"v_mfma_f32_16x16x32_bf16 v[68:71], v[180:183], v[212:215], v[68:71]\n"
"global_load_lds_dwordx4 v235, s[88:89]\n"
"v_mfma_f32_16x16x32_bf16 v[72:75], v[180:183], v[216:219], v[72:75]\n"
"ds_read_b64_tr_b16 v[176:177], v230 offset:36864\n"
"v_mfma_f32_16x16x32_bf16 v[76:79], v[180:183], v[220:223], v[76:79]\n"
"ds_read_b64_tr_b16 v[178:179], v230 offset:37376\n"
"v_mfma_f32_16x16x32_bf16 v[80:83], v[184:187], v[208:211], v[80:83]\n"
"global_load_lds_dwordx4 v236, s[88:89] offset:1024\n"
"v_mfma_f32_16x16x32_bf16 v[84:87], v[184:187], v[212:215], v[84:87]\n"
"v_mfma_f32_16x16x32_bf16 v[88:91], v[184:187], v[216:219], v[88:91]\n"
"ds_read_b64_tr_b16 v[180:181], v231 offset:36864\n"
"ds_read_b64_tr_b16 v[182:183], v231 offset:37376\n"
"v_mfma_f32_16x16x32_bf16 v[92:95], v[184:187], v[220:223], v[92:95]\n"
"s_waitcnt lgkmcnt(4)\n"
"v_mfma_f32_16x16x32_bf16 v[96:99], v[188:191], v[208:211], v[96:99]\n"
"ds_read_b64_tr_b16 v[184:185], v232 offset:36864\n"
"v_mfma_f32_16x16x32_bf16 v[100:103], v[188:191], v[212:215], v[100:103]\n"
"ds_read_b64_tr_b16 v[186:187], v232 offset:37376\n"
"global_load_lds_dwordx4 v237, s[88:89] offset:2048\n"
"v_mfma_f32_16x16x32_bf16 v[104:107], v[188:191], v[216:219], v[104:107]\n"
"v_mfma_f32_16x16x32_bf16 v[108:111], v[188:191], v[220:223], v[108:111]\n"
"s_waitcnt lgkmcnt(4)\n"
"v_mfma_f32_16x16x32_bf16 v[112:115], v[176:179], v[208:211], v[112:115]\n"
"ds_read_b64_tr_b16 v[188:189], v233 offset:36864\n"
"global_load_lds_dwordx4 v238, s[88:89] offset:3072\n"
"s_add_u32 s88, s88, 0x48000\n"
"s_addc_u32 s89, s89, 0\n"
"v_mfma_f32_16x16x32_bf16 v[116:119], v[176:179], v[212:215], v[116:119]\n"
"ds_read_b64_tr_b16 v[190:191], v233 offset:37376\n"
"v_mfma_f32_16x16x32_bf16 v[120:123], v[176:179], v[216:219], v[120:123]\n"
"s_add_u32 m0, s84, 0x25000\n"
"v_mfma_f32_16x16x32_bf16 v[124:127], v[176:179], v[220:223], v[124:127]\n"
"global_load_lds_dwordx4 v240, s[80:81] sc1\n"
"s_add_u32 s80, s80, 0x1000\n"
"s_addc_u32 s81, s81, 0\n"
"s_waitcnt lgkmcnt(4)\n"
"v_mfma_f32_16x16x32_bf16 v[128:131], v[180:183], v[208:211], v[128:131]\n"
"v_mfma_f32_16x16x32_bf16 v[132:135], v[180:183], v[212:215], v[132:135]\n"
"v_mfma_f32_16x16x32_bf16 v[136:139], v[180:183], v[216:219], v[136:139]\n"
"v_mfma_f32_16x16x32_bf16 v[140:143], v[180:183], v[220:223], v[140:143]\n"
"s_waitcnt vmcnt(9) lgkmcnt(0)\n"
"s_barrier\n"
"ds_read_b128 v[192:195], v234 offset:0\n"
"ds_read_b64_tr_b16 v[176:177], v224 offset:0\n"
"v_mfma_f32_16x16x32_bf16 v[144:147], v[184:187], v[208:211], v[144:147]\n"
"ds_read_b64_tr_b16 v[178:179], v224 offset:512\n"
"s_add_u32 m0, s82, 0x1b000\n"
"v_mfma_f32_16x16x32_bf16 v[148:151], v[184:187], v[212:215], v[148:151]\n"
"ds_read_b128 v[196:199], v234 offset:1024\n"
"global_load_lds_dwordx4 v235, s[90:91]\n"
"v_mfma_f32_16x16x32_bf16 v[152:155], v[184:187], v[216:219], v[152:155]\n"
"ds_read_b128 v[200:203], v234 offset:2048\n"
"ds_read_b128 v[204:207], v234 offset:3072\n"
"v_mfma_f32_16x16x32_bf16 v[156:159], v[184:187], v[220:223], v[156:159]\n"
"ds_read_b64_tr_b16 v[180:181], v225 offset:0\n"
"v_mfma_f32_16x16x32_bf16 v[160:163], v[188:191], v[208:211], v[160:163]\n"
"ds_read_b64_tr_b16 v[182:183], v225 offset:512\n"
"global_load_lds_dwordx4 v236, s[90:91] offset:1024\n"
"v_mfma_f32_16x16x32_bf16 v[164:167], v[188:191], v[212:215], v[164:167]\n"
"v_mfma_f32_16x16x32_bf16 v[168:171], v[188:191], v[216:219], v[168:171]\n"
"ds_read_b64_tr_b16 v[184:185], v226 offset:0\n"
"ds_read_b64_tr_b16 v[186:187], v226 offset:512\n"
"v_mfma_f32_16x16x32_bf16 v[172:175], v[188:191], v[220:223], v[172:175]\n"
"s_waitcnt lgkmcnt(7)\n"
"v_mfma_f32_16x16x32_bf16 v[16:19], v[176:179], v[192:195], v[16:19]\n"
"ds_read_b64_tr_b16 v[188:189], v227 offset:0\n"
"s_waitcnt lgkmcnt(7)\n"
"v_mfma_f32_16x16x32_bf16 v[20:23], v[176:179], v[196:199], v[20:23]\n"
"ds_read_b64_tr_b16 v[190:191], v227 offset:512\n"
"global_load_lds_dwordx4 v237, s[90:91] offset:2048\n"
"s_waitcnt lgkmcnt(7)\n"
"v_mfma_f32_16x16x32_bf16 v[24:27], v[176:179], v[200:203], v[24:27]\n"
"s_waitcnt lgkmcnt(6)\n"
"v_mfma_f32_16x16x32_bf16 v[28:31], v[176:179], v[204:207], v[28:31]\n"
"s_waitcnt lgkmcnt(4)\n"
"v_mfma_f32_16x16x32_bf16 v[32:35], v[180:183], v[192:195], v[32:35]\n"
"ds_read_b64_tr_b16 v[176:177], v228 offset:0\n"
"global_load_lds_dwordx4 v238, s[90:91] offset:3072\n"
"s_add_u32 s90, s90, 0x48000\n"
"s_addc_u32 s91, s91, 0\n"
"v_mfma_f32_16x16x32_bf16 v[36:39], v[180:183], v[196:199], v[36:39]\n"
"ds_read_b64_tr_b16 v[178:179], v228 offset:512\n"
"v_mfma_f32_16x16x32_bf16 v[40:43], v[180:183], v[200:203], v[40:43]\n"
"v_mfma_f32_16x16x32_bf16 v[44:47], v[180:183], v[204:207], v[44:47]\n"
"s_waitcnt lgkmcnt(4)\n"
"v_mfma_f32_16x16x32_bf16 v[48:51], v[184:187], v[192:195], v[48:51]\n"
"v_mfma_f32_16x16x32_bf16 v[52:55], v[184:187], v[196:199], v[52:55]\n"
"v_mfma_f32_16x16x32_bf16 v[56:59], v[184:187], v[200:203], v[56:59]\n"
"v_mfma_f32_16x16x32_bf16 v[60:63], v[184:187], v[204:207], v[60:63]\n"
"s_waitcnt vmcnt(9) lgkmcnt(0)\n"
"s_barrier\n"
"ds_read_b64_tr_b16 v[180:181], v224 offset:36864\n"
"v_mfma_f32_16x16x32_bf16 v[64:67], v[188:191], v[192:195], v[64:67]\n"
"ds_read_b64_tr_b16 v[182:183], v224 offset:37376\n"
"s_mov_b32 m0, s82\n"
"v_mfma_f32_16x16x32_bf16 v[68:71], v[188:191], v[196:199], v[68:71]\n"
"global_load_lds_dwordx4 v235, s[88:89]\n"
"v_mfma_f32_16x16x32_bf16 v[72:75], v[188:191], v[200:203], v[72:75]\n"
"ds_read_b64_tr_b16 v[184:185], v225 offset:36864\n"
"v_mfma_f32_16x16x32_bf16 v[76:79], v[188:191], v[204:207], v[76:79]\n"
"ds_read_b64_tr_b16 v[186:187], v225 offset:37376\n"
"v_mfma_f32_16x16x32_bf16 v[80:83], v[176:179], v[192:195], v[80:83]\n"
"global_load_lds_dwordx4 v236, s[88:89] offset:1024\n"
"v_mfma_f32_16x16x32_bf16 v[84:87], v[176:179], v[196:199], v[84:87]\n"
"v_mfma_f32_16x16x32_bf16 v[88:91], v[176:179], v[200:203], v[88:91]\n"
"ds_read_b64_tr_b16 v[188:189], v226 offset:36864\n"
"ds_read_b64_tr_b16 v[190:191], v226 offset:37376\n"
"v_mfma_f32_16x16x32_bf16 v[92:95], v[176:179], v[204:207], v[92:95]\n"
"s_waitcnt lgkmcnt(4)\n"
"v_mfma_f32_16x16x32_bf16 v[96:99], v[180:183], v[192:195], v[96:99]\n"
"ds_read_b64_tr_b16 v[176:177], v227 offset:36864\n"
"v_mfma_f32_16x16x32_bf16 v[100:103], v[180:183], v[196:199], v[100:103]\n"
"ds_read_b64_tr_b16 v[178:179], v227 offset:37376\n"
"global_load_lds_dwordx4 v237, s[88:89] offset:2048\n"
"v_mfma_f32_16x16x32_bf16 v[104:107], v[180:183], v[200:203], v[104:107]\n"
"v_mfma_f32_16x16x32_bf16 v[108:111], v[180:183], v[204:207], v[108:111]\n"
"s_waitcnt lgkmcnt(4)\n"
"v_mfma_f32_16x16x32_bf16 v[112:115], v[184:187], v[192:195], v[112:115]\n"
"ds_read_b64_tr_b16 v[180:181], v228 offset:36864\n"
"global_load_lds_dwordx4 v238, s[88:89] offset:3072\n"
"s_add_u32 s88, s88, 0x48000\n"
"s_addc_u32 s89, s89, 0\n"
"v_mfma_f32_16x16x32_bf16 v[116:119], v[184:187], v[196:199], v[116:119]\n"
"ds_read_b64_tr_b16 v[182:183], v228 offset:37376\n"
"v_mfma_f32_16x16x32_bf16 v[120:123], v[184:187], v[200:203], v[120:123]\n"
"s_add_u32 m0, s84, 0x26000\n"
"v_mfma_f32_16x16x32_bf16 v[124:127], v[184:187], v[204:207], v[124:127]\n"
"global_load_lds_dwordx4 v240, s[80:81] sc1\n"
"s_add_u32 s80, s80, 0x1000\n"
"s_addc_u32 s81, s81, 0\n"
"s_waitcnt lgkmcnt(4)\n"
"v_mfma_f32_16x16x32_bf16 v[128:131], v[188:191], v[192:195], v[128:131]\n"
"v_mfma_f32_16x16x32_bf16 v[132:135], v[188:191], v[196:199], v[132:135]\n"
"v_mfma_f32_16x16x32_bf16 v[136:139], v[188:191], v[200:203], v[136:139]\n"
"v_mfma_f32_16x16x32_bf16 v[140:143], v[188:191], v[204:207], v[140:143]\n"
"s_waitcnt vmcnt(9) lgkmcnt(0)\n"
"s_barrier\n"
"ds_read_b128 v[208:211], v234 offset:4096\n"
"ds_read_b64_tr_b16 v[184:185], v229 offset:0\n"
"v_mfma_f32_16x16x32_bf16 v[144:147], v[176:179], v[192:195], v[144:147]\n"
"ds_read_b64_tr_b16 v[186:187], v229 offset:512\n"
"s_add_u32 m0, s82, 0x9000\n"
"v_mfma_f32_16x16x32_bf16 v[148:151], v[176:179], v[196:199], v[148:151]\n"
"ds_read_b128 v[212:215], v234 offset:5120\n"
"global_load_lds_dwordx4 v235, s[90:91]\n"
"v_mfma_f32_16x16x32_bf16 v[152:155], v[176:179], v[200:203], v[152:155]\n"
"ds_read_b128 v[216:219], v234 offset:6144\n"
"ds_read_b128 v[220:223], v234 offset:7168\n"
"v_mfma_f32_16x16x32_bf16 v[156:159], v[176:179], v[204:207], v[156:159]\n"
"ds_read_b64_tr_b16 v[188:189], v230 offset:0\n"
"v_mfma_f32_16x16x32_bf16 v[160:163], v[180:183], v[192:195], v[160:163]\n"
"ds_read_b64_tr_b16 v[190:191], v230 offset:512\n"
"global_load_lds_dwordx4 v236, s[90:91] offset:1024\n"
"v_mfma_f32_16x16x32_bf16 v[164:167], v[180:183], v[196:199], v[164:167]\n"
"v_mfma_f32_16x16x32_bf16 v[168:171], v[180:183], v[200:203], v[168:171]\n"
"ds_read_b64_tr_b16 v[176:177], v231 offset:0\n"
"ds_read_b64_tr_b16 v[178:179], v231 offset:512\n"
"v_mfma_f32_16x16x32_bf16 v[172:175], v[180:183], v[204:207], v[172:175]\n"
"s_waitcnt lgkmcnt(7)\n"
"v_mfma_f32_16x16x32_bf16 v[16:19], v[184:187], v[208:211], v[16:19]\n"
"ds_read_b64_tr_b16 v[180:181], v232 offset:0\n"
"s_waitcnt lgkmcnt(7)\n"
"v_mfma_f32_16x16x32_bf16 v[20:23], v[184:187], v[212:215], v[20:23]\n"
"ds_read_b64_tr_b16 v[182:183], v232 offset:512\n"
"global_load_lds_dwordx4 v237, s[90:91] offset:2048\n"
"s_waitcnt lgkmcnt(7)\n"
"v_mfma_f32_16x16x32_bf16 v[24:27], v[184:187], v[216:219], v[24:27]\n"
"s_waitcnt lgkmcnt(6)\n"
"v_mfma_f32_16x16x32_bf16 v[28:31], v[184:187], v[220:223], v[28:31]\n"
"s_waitcnt lgkmcnt(4)\n"
"v_mfma_f32_16x16x32_bf16 v[32:35], v[188:191], v[208:211], v[32:35]\n"
"ds_read_b64_tr_b16 v[184:185], v233 offset:0\n"
"global_load_lds_dwordx4 v238, s[90:91] offset:3072\n"
"s_add_u32 s90, s90, 0x48000\n"
"s_addc_u32 s91, s91, 0\n"
"v_mfma_f32_16x16x32_bf16 v[36:39], v[188:191], v[212:215], v[36:39]\n"
"ds_read_b64_tr_b16 v[186:187], v233 offset:512\n"
"v_mfma_f32_16x16x32_bf16 v[40:43], v[188:191], v[216:219], v[40:43]\n"
"v_mfma_f32_16x16x32_bf16 v[44:47], v[188:191], v[220:223], v[44:47]\n"
"s_waitcnt lgkmcnt(4)\n"
"v_mfma_f32_16x16x32_bf16 v[48:51], v[176:179], v[208:211], v[48:51]\n"
"v_mfma_f32_16x16x32_bf16 v[52:55], v[176:179], v[212:215], v[52:55]\n"
"v_mfma_f32_16x16x32_bf16 v[56:59], v[176:179], v[216:219], v[56:59]\n"
"v_mfma_f32_16x16x32_bf16 v[60:63], v[176:179], v[220:223], v[60:63]\n"
"s_waitcnt vmcnt(9) lgkmcnt(0)\n"
"s_barrier\n"
"ds_read_b64_tr_b16 v[188:189], v229 offset:36864\n"
"v_mfma_f32_16x16x32_bf16 v[64:67], v[180:183], v[208:211], v[64:67]\n"
"ds_read_b64_tr_b16 v[190:191], v229 offset:37376\n"
"s_add_u32 m0, s82, 0x12000\n"
"v_mfma_f32_16x16x32_bf16 v[68:71], v[180:183], v[212:215], v[68:71]\n"
"global_load_lds_dwordx4 v235, s[88:89]\n"
"v_mfma_f32_16x16x32_bf16 v[72:75], v[180:183], v[216:219], v[72:75]\n"
"ds_read_b64_tr_b16 v[176:177], v230 offset:36864\n"
"v_mfma_f32_16x16x32_bf16 v[76:79], v[180:183], v[220:223], v[76:79]\n"
"ds_read_b64_tr_b16 v[178:179], v230 offset:37376\n"
"v_mfma_f32_16x16x32_bf16 v[80:83], v[184:187], v[208:211], v[80:83]\n"
"global_load_lds_dwordx4 v236, s[88:89] offset:1024\n"
"v_mfma_f32_16x16x32_bf16 v[84:87], v[184:187], v[212:215], v[84:87]\n"
"v_mfma_f32_16x16x32_bf16 v[88:91], v[184:187], v[216:219], v[88:91]\n"
"ds_read_b64_tr_b16 v[180:181], v231 offset:36864\n"
"ds_read_b64_tr_b16 v[182:183], v231 offset:37376\n"
"v_mfma_f32_16x16x32_bf16 v[92:95], v[184:187], v[220:223], v[92:95]\n"
"s_waitcnt lgkmcnt(4)\n"
"v_mfma_f32_16x16x32_bf16 v[96:99], v[188:191], v[208:211], v[96:99]\n"
"ds_read_b64_tr_b16 v[184:185], v232 offset:36864\n"
"v_mfma_f32_16x16x32_bf16 v[100:103], v[188:191], v[212:215], v[100:103]\n"
"ds_read_b64_tr_b16 v[186:187], v232 offset:37376\n"
"global_load_lds_dwordx4 v237, s[88:89] offset:2048\n"
"v_mfma_f32_16x16x32_bf16 v[104:107], v[188:191], v[216:219], v[104:107]\n"
"v_mfma_f32_16x16x32_bf16 v[108:111], v[188:191], v[220:223], v[108:111]\n"
"s_waitcnt lgkmcnt(4)\n"
"v_mfma_f32_16x16x32_bf16 v[112:115], v[176:179], v[208:211], v[112:115]\n"
"ds_read_b64_tr_b16 v[188:189], v233 offset:36864\n"
"global_load_lds_dwordx4 v238, s[88:89] offset:3072\n"
"s_add_u32 s88, s88, 0x48000\n"
"s_addc_u32 s89, s89, 0\n"
"v_mfma_f32_16x16x32_bf16 v[116:119], v[176:179], v[212:215], v[116:119]\n"
"ds_read_b64_tr_b16 v[190:191], v233 offset:37376\n"
"v_mfma_f32_16x16x32_bf16 v[120:123], v[176:179], v[216:219], v[120:123]\n"
"s_add_u32 m0, s84, 0x24000\n"
"v_mfma_f32_16x16x32_bf16 v[124:127], v[176:179], v[220:223], v[124:127]\n"
"global_load_lds_dwordx4 v240, s[80:81] sc1\n"
"s_add_u32 s80, s80, 0x1000\n"
"s_addc_u32 s81, s81, 0\n"
"s_waitcnt lgkmcnt(4)\n"
"v_mfma_f32_16x16x32_bf16 v[128:131], v[180:183], v[208:211], v[128:131]\n"
"v_mfma_f32_16x16x32_bf16 v[132:135], v[180:183], v[212:215], v[132:135]\n"
"v_mfma_f32_16x16x32_bf16 v[136:139], v[180:183], v[216:219], v[136:139]\n"
"v_mfma_f32_16x16x32_bf16 v[140:143], v[180:183], v[220:223], v[140:143]\n"
"s_waitcnt vmcnt(9) lgkmcnt(0)\n"
"s_barrier\n"
"ds_read_b128 v[192:195], v234 offset:8192\n"
"ds_read_b64_tr_b16 v[176:177], v224 offset:0\n"
"v_mfma_f32_16x16x32_bf16 v[144:147], v[184:187], v[208:211], v[144:147]\n"
"ds_read_b64_tr_b16 v[178:179], v224 offset:512\n"
"s_add_u32 m0, s82, 0x1b000\n"
"v_mfma_f32_16x16x32_bf16 v[148:151], v[184:187], v[212:215], v[148:151]\n"
"ds_read_b128 v[196:199], v234 offset:9216\n"
"global_load_lds_dwordx4 v235, s[90:91]\n"
"v_mfma_f32_16x16x32_bf16 v[152:155], v[184:187], v[216:219], v[152:155]\n"
"ds_read_b128 v[200:203], v234 offset:10240\n"
"ds_read_b128 v[204:207], v234 offset:11264\n"
"v_mfma_f32_16x16x32_bf16 v[156:159], v[184:187], v[220:223], v[156:159]\n"
"ds_read_b64_tr_b16 v[180:181], v225 offset:0\n"
"v_mfma_f32_16x16x32_bf16 v[160:163], v[188:191], v[208:211], v[160:163]\n"
"ds_read_b64_tr_b16 v[182:183], v225 offset:512\n"
"global_load_lds_dwordx4 v236, s[90:91] offset:1024\n"
"v_mfma_f32_16x16x32_bf16 v[164:167], v[188:191], v[212:215], v[164:167]\n"
"v_mfma_f32_16x16x32_bf16 v[168:171], v[188:191], v[216:219], v[168:171]\n"
"ds_read_b64_tr_b16 v[184:185], v226 offset:0\n"
"ds_read_b64_tr_b16 v[186:187], v226 offset:512\n"
"v_mfma_f32_16x16x32_bf16 v[172:175], v[188:191], v[220:223], v[172:175]\n"
"s_waitcnt lgkmcnt(7)\n"
"v_mfma_f32_16x16x32_bf16 v[16:19], v[176:179], v[192:195], v[16:19]\n"
"ds_read_b64_tr_b16 v[188:189], v227 offset:0\n"
"s_waitcnt lgkmcnt(7)\n"
"v_mfma_f32_16x16x32_bf16 v[20:23], v[176:179], v[196:199], v[20:23]\n"
"ds_read_b64_tr_b16 v[190:191], v227 offset:512\n"
"global_load_lds_dwordx4 v237, s[90:91] offset:2048\n"
"s_waitcnt lgkmcnt(7)\n"
"v_mfma_f32_16x16x32_bf16 v[24:27], v[176:179], v[200:203], v[24:27]\n"
"s_waitcnt lgkmcnt(6)\n"
"v_mfma_f32_16x16x32_bf16 v[28:31], v[176:179], v[204:207], v[28:31]\n"
"s_waitcnt lgkmcnt(4)\n"
"v_mfma_f32_16x16x32_bf16 v[32:35], v[180:183], v[192:195], v[32:35]\n"
"ds_read_b64_tr_b16 v[176:177], v228 offset:0\n"
"global_load_lds_dwordx4 v238, s[90:91] offset:3072\n"
"s_add_u32 s90, s90, 0x48000\n"
"s_addc_u32 s91, s91, 0\n"
"v_mfma_f32_16x16x32_bf16 v[36:39], v[180:183], v[196:199], v[36:39]\n"
"ds_read_b64_tr_b16 v[178:179], v228 offset:512\n"
"v_mfma_f32_16x16x32_bf16 v[40:43], v[180:183], v[200:203], v[40:43]\n"
"v_mfma_f32_16x16x32_bf16 v[44:47], v[180:183], v[204:207], v[44:47]\n"
"s_waitcnt lgkmcnt(4)\n"
"v_mfma_f32_16x16x32_bf16 v[48:51], v[184:187], v[192:195], v[48:51]\n"
"v_mfma_f32_16x16x32_bf16 v[52:55], v[184:187], v[196:199], v[52:55]\n"
"v_mfma_f32_16x16x32_bf16 v[56:59], v[184:187], v[200:203], v[56:59]\n"
"v_mfma_f32_16x16x32_bf16 v[60:63], v[184:187], v[204:207], v[60:63]\n"
"s_waitcnt vmcnt(9) lgkmcnt(0)\n"
"s_barrier\n"
"ds_read_b64_tr_b16 v[180:181], v224 offset:36864\n"
"v_mfma_f32_16x16x32_bf16 v[64:67], v[188:191], v[192:195], v[64:67]\n"
"ds_read_b64_tr_b16 v[182:183], v224 offset:37376\n"
"s_mov_b32 m0, s82\n"
"v_mfma_f32_16x16x32_bf16 v[68:71], v[188:191], v[196:199], v[68:71]\n"
"global_load_lds_dwordx4 v235, s[88:89]\n"
"v_mfma_f32_16x16x32_bf16 v[72:75], v[188:191], v[200:203], v[72:75]\n"
"ds_read_b64_tr_b16 v[184:185], v225 offset:36864\n"
"v_mfma_f32_16x16x32_bf16 v[76:79], v[188:191], v[204:207], v[76:79]\n"
"ds_read_b64_tr_b16 v[186:187], v225 offset:37376\n"
"v_mfma_f32_16x16x32_bf16 v[80:83], v[176:179], v[192:195], v[80:83]\n"
"global_load_lds_dwordx4 v236, s[88:89] offset:1024\n"
"v_mfma_f32_16x16x32_bf16 v[84:87], v[176:179], v[196:199], v[84:87]\n"
"v_mfma_f32_16x16x32_bf16 v[88:91], v[176:179], v[200:203], v[88:91]\n"
"ds_read_b64_tr_b16 v[188:189], v226 offset:36864\n"
"ds_read_b64_tr_b16 v[190:191], v226 offset:37376\n"
"v_mfma_f32_16x16x32_bf16 v[92:95], v[176:179], v[204:207], v[92:95]\n"
"s_waitcnt lgkmcnt(4)\n"
"v_mfma_f32_16x16x32_bf16 v[96:99], v[180:183], v[192:195], v[96:99]\n"
"ds_read_b64_tr_b16 v[176:177], v227 offset:36864\n"
"v_mfma_f32_16x16x32_bf16 v[100:103], v[180:183], v[196:199], v[100:103]\n"
"ds_read_b64_tr_b16 v[178:179], v227 offset:37376\n"
"global_load_lds_dwordx4 v237, s[88:89] offset:2048\n"
"v_mfma_f32_16x16x32_bf16 v[104:107], v[180:183], v[200:203], v[104:107]\n"
"v_mfma_f32_16x16x32_bf16 v[108:111], v[180:183], v[204:207], v[108:111]\n"
"s_waitcnt lgkmcnt(4)\n"
"v_mfma_f32_16x16x32_bf16 v[112:115], v[184:187], v[192:195], v[112:115]\n"
"ds_read_b64_tr_b16 v[180:181], v228 offset:36864\n"
"global_load_lds_dwordx4 v238, s[88:89] offset:3072\n"
"s_add_u32 s88, s88, 0x48000\n"
"s_addc_u32 s89, s89, 0\n"
"v_mfma_f32_16x16x32_bf16 v[116:119], v[184:187], v[196:199], v[116:119]\n"
"ds_read_b64_tr_b16 v[182:183], v228 offset:37376\n"
"v_mfma_f32_16x16x32_bf16 v[120:123], v[184:187], v[200:203], v[120:123]\n"
"s_add_u32 m0, s84, 0x25000\n"
"v_mfma_f32_16x16x32_bf16 v[124:127], v[184:187], v[204:207], v[124:127]\n"
"global_load_lds_dwordx4 v240, s[80:81] sc1\n"
"s_add_u32 s80, s80, 0x1000\n"
"s_addc_u32 s81, s81, 0\n"
"s_waitcnt lgkmcnt(4)\n"
"v_mfma_f32_16x16x32_bf16 v[128:131], v[188:191], v[192:195], v[128:131]\n"
"v_mfma_f32_16x16x32_bf16 v[132:135], v[188:191], v[196:199], v[132:135]\n"
"v_mfma_f32_16x16x32_bf16 v[136:139], v[188:191], v[200:203], v[136:139]\n"
"v_mfma_f32_16x16x32_bf16 v[140:143], v[188:191], v[204:207], v[140:143]\n"
"s_waitcnt vmcnt(9) lgkmcnt(0)\n"
"s_barrier\n"
"ds_read_b128 v[208:211], v234 offset:0\n"
"ds_read_b64_tr_b16 v[184:185], v229 offset:0\n"
"v_mfma_f32_16x16x32_bf16 v[144:147], v[176:179], v[192:195], v[144:147]\n"
"ds_read_b64_tr_b16 v[186:187], v229 offset:512\n"
"s_add_u32 m0, s82, 0x9000\n"
"v_mfma_f32_16x16x32_bf16 v[148:151], v[176:179], v[196:199], v[148:151]\n"
"ds_read_b128 v[212:215], v234 offset:1024\n"
"global_load_lds_dwordx4 v235, s[90:91]\n"
"v_mfma_f32_16x16x32_bf16 v[152:155], v[176:179], v[200:203], v[152:155]\n"
"ds_read_b128 v[216:219], v234 offset:2048\n"
"ds_read_b128 v[220:223], v234 offset:3072\n"
"v_mfma_f32_16x16x32_bf16 v[156:159], v[176:179], v[204:207], v[156:159]\n"
"ds_read_b64_tr_b16 v[188:189], v230 offset:0\n"
"v_mfma_f32_16x16x32_bf16 v[160:163], v[180:183], v[192:195], v[160:163]\n"
"ds_read_b64_tr_b16 v[190:191], v230 offset:512\n"
"global_load_lds_dwordx4 v236, s[90:91] offset:1024\n"
"v_mfma_f32_16x16x32_bf16 v[164:167], v[180:183], v[196:199], v[164:167]\n"
"v_mfma_f32_16x16x32_bf16 v[168:171], v[180:183], v[200:203], v[168:171]\n"
"ds_read_b64_tr_b16 v[176:177], v231 offset:0\n"
"ds_read_b64_tr_b16 v[178:179], v231 offset:512\n"
"v_mfma_f32_16x16x32_bf16 v[172:175], v[180:183], v[204:207], v[172:175]\n"
"s_waitcnt lgkmcnt(7)\n"
"v_mfma_f32_16x16x32_bf16 v[16:19], v[184:187], v[208:211], v[16:19]\n"
"ds_read_b64_tr_b16 v[180:181], v232 offset:0\n"
"s_waitcnt lgkmcnt(7)\n"
"v_mfma_f32_16x16x32_bf16 v[20:23], v[184:187], v[212:215], v[20:23]\n"
"ds_read_b64_tr_b16 v[182:183], v232 offset:512\n"
"global_load_lds_dwordx4 v237, s[90:91] offset:2048\n"
"s_waitcnt lgkmcnt(7)\n"
"v_mfma_f32_16x16x32_bf16 v[24:27], v[184:187], v[216:219], v[24:27]\n"
"s_waitcnt lgkmcnt(6)\n"
"v_mfma_f32_16x16x32_bf16 v[28:31], v[184:187], v[220:223], v[28:31]\n"
"s_waitcnt lgkmcnt(4)\n"
"v_mfma_f32_16x16x32_bf16 v[32:35], v[188:191], v[208:211], v[32:35]\n"
"ds_read_b64_tr_b16 v[184:185], v233 offset:0\n"
"global_load_lds_dwordx4 v238, s[90:91] offset:3072\n"
"s_add_u32 s90, s90, 0x48000\n"
"s_addc_u32 s91, s91, 0\n"
"v_mfma_f32_16x16x32_bf16 v[36:39], v[188:191], v[212:215], v[36:39]\n"
"ds_read_b64_tr_b16 v[186:187], v233 offset:512\n"
"v_mfma_f32_16x16x32_bf16 v[40:43], v[188:191], v[216:219], v[40:43]\n"
"v_mfma_f32_16x16x32_bf16 v[44:47], v[188:191], v[220:223], v[44:47]\n"
"s_waitcnt lgkmcnt(4)\n"
"v_mfma_f32_16x16x32_bf16 v[48:51], v[176:179], v[208:211], v[48:51]\n"
"v_mfma_f32_16x16x32_bf16 v[52:55], v[176:179], v[212:215], v[52:55]\n"
"v_mfma_f32_16x16x32_bf16 v[56:59], v[176:179], v[216:219], v[56:59]\n"
"v_mfma_f32_16x16x32_bf16 v[60:63], v[176:179], v[220:223], v[60:63]\n"
"s_waitcnt vmcnt(9) lgkmcnt(0)\n"
"s_barrier\n"
"ds_read_b64_tr_b16 v[188:189], v229 offset:36864\n"
"v_mfma_f32_16x16x32_bf16 v[64:67], v[180:183], v[208:211], v[64:67]\n"
"ds_read_b64_tr_b16 v[190:191], v229 offset:37376\n"
"s_add_u32 m0, s82, 0x12000\n"
"v_mfma_f32_16x16x32_bf16 v[68:71], v[180:183], v[212:215], v[68:71]\n"
"global_load_lds_dwordx4 v235, s[88:89]\n"
"v_mfma_f32_16x16x32_bf16 v[72:75], v[180:183], v[216:219], v[72:75]\n"
"ds_read_b64_tr_b16 v[176:177], v230 offset:36864\n"
"v_mfma_f32_16x16x32_bf16 v[76:79], v[180:183], v[220:223], v[76:79]\n"
"ds_read_b64_tr_b16 v[178:179], v230 offset:37376\n"
"v_mfma_f32_16x16x32_bf16 v[80:83], v[184:187], v[208:211], v[80:83]\n"
"global_load_lds_dwordx4 v236, s[88:89] offset:1024\n"
"v_mfma_f32_16x16x32_bf16 v[84:87], v[184:187], v[212:215], v[84:87]\n"
"v_mfma_f32_16x16x32_bf16 v[88:91], v[184:187], v[216:219], v[88:91]\n"
"ds_read_b64_tr_b16 v[180:181], v231 offset:36864\n"
"ds_read_b64_tr_b16 v[182:183], v231 offset:37376\n"
"v_mfma_f32_16x16x32_bf16 v[92:95], v[184:187], v[220:223], v[92:95]\n"
"s_waitcnt lgkmcnt(4)\n"
"v_mfma_f32_16x16x32_bf16 v[96:99], v[188:191], v[208:211], v[96:99]\n"
"ds_read_b64_tr_b16 v[184:185], v232 offset:36864\n"
"v_mfma_f32_16x16x32_bf16 v[100:103], v[188:191], v[212:215], v[100:103]\n"
"ds_read_b64_tr_b16 v[186:187], v232 offset:37376\n"
"global_load_lds_dwordx4 v237, s[88:89] offset:2048\n"
"v_mfma_f32_16x16x32_bf16 v[104:107], v[188:191], v[216:219], v[104:107]\n"
"v_mfma_f32_16x16x32_bf16 v[108:111], v[188:191], v[220:223], v[108:111]\n"
"s_waitcnt lgkmcnt(4)\n"
"v_mfma_f32_16x16x32_bf16 v[112:115], v[176:179], v[208:211], v[112:115]\n"
"ds_read_b64_tr_b16 v[188:189], v233 offset:36864\n"
"global_load_lds_dwordx4 v238, s[88:89] offset:3072\n"
"s_add_u32 s88, s88, 0x48000\n"
"s_addc_u32 s89, s89, 0\n"
"v_mfma_f32_16x16x32_bf16 v[116:119], v[176:179], v[212:215], v[116:119]\n"
"ds_read_b64_tr_b16 v[190:191], v233 offset:37376\n"
"v_mfma_f32_16x16x32_bf16 v[120:123], v[176:179], v[216:219], v[120:123]\n"
"s_add_u32 m0, s84, 0x26000\n"
"v_mfma_f32_16x16x32_bf16 v[124:127], v[176:179], v[220:223], v[124:127]\n"
"global_load_lds_dwordx4 v240, s[80:81] sc1\n"
"s_add_u32 s80, s80, 0x1000\n"
"s_addc_u32 s81, s81, 0\n"
"s_waitcnt lgkmcnt(4)\n"
"v_mfma_f32_16x16x32_bf16 v[128:131], v[180:183], v[208:211], v[128:131]\n"
"v_mfma_f32_16x16x32_bf16 v[132:135], v[180:183], v[212:215], v[132:135]\n"
"v_mfma_f32_16x16x32_bf16 v[136:139], v[180:183], v[216:219], v[136:139]\n"
"v_mfma_f32_16x16x32_bf16 v[140:143], v[180:183], v[220:223], v[140:143]\n"
"s_waitcnt vmcnt(9) lgkmcnt(0)\n"
"s_barrier\n"
"ds_read_b128 v[192:195], v234 offset:4096\n"
"ds_read_b64_tr_b16 v[176:177], v224 offset:0\n"
"v_mfma_f32_16x16x32_bf16 v[144:147], v[184:187], v[208:211], v[144:147]\n"
"ds_read_b64_tr_b16 v[178:179], v224 offset:512\n"
"s_add_u32 m0, s82, 0x1b000\n"
"v_mfma_f32_16x16x32_bf16 v[148:151], v[184:187], v[212:215], v[148:151]\n"
"ds_read_b128 v[196:199], v234 offset:5120\n"
"global_load_lds_dwordx4 v235, s[90:91]\n"
"v_mfma_f32_16x16x32_bf16 v[152:155], v[184:187], v[216:219], v[152:155]\n"
"ds_read_b128 v[200:203], v234 offset:6144\n"
"ds_read_b128 v[204:207], v234 offset:7168\n"
"v_mfma_f32_16x16x32_bf16 v[156:159], v[184:187], v[220:223], v[156:159]\n"
"ds_read_b64_tr_b16 v[180:181], v225 offset:0\n"
"v_mfma_f32_16x16x32_bf16 v[160:163], v[188:191], v[208:211], v[160:163]\n"
"ds_read_b64_tr_b16 v[182:183], v225 offset:512\n"
"global_load_lds_dwordx4 v236, s[90:91] offset:1024\n"
"v_mfma_f32_16x16x32_bf16 v[164:167], v[188:191], v[212:215], v[164:167]\n"
"v_mfma_f32_16x16x32_bf16 v[168:171], v[188:191], v[216:219], v[168:171]\n"
"ds_read_b64_tr_b16 v[184:185], v226 offset:0\n"
"ds_read_b64_tr_b16 v[186:187], v226 offset:512\n"
"v_mfma_f32_16x16x32_bf16 v[172:175], v[188:191], v[220:223], v[172:175]\n"
"s_waitcnt lgkmcnt(7)\n"
"v_mfma_f32_16x16x32_bf16 v[16:19], v[176:179], v[192:195], v[16:19]\n"
"ds_read_b64_tr_b16 v[188:189], v227 offset:0\n"
"s_waitcnt lgkmcnt(7)\n"
"v_mfma_f32_16x16x32_bf16 v[20:23], v[176:179], v[196:199], v[20:23]\n"
"ds_read_b64_tr_b16 v[190:191], v227 offset:512\n"
"global_load_lds_dwordx4 v237, s[90:91] offset:2048\n"
"s_waitcnt lgkmcnt(7)\n"
"v_mfma_f32_16x16x32_bf16 v[24:27], v[176:179], v[200:203], v[24:27]\n"
"s_waitcnt lgkmcnt(6)\n"
"v_mfma_f32_16x16x32_bf16 v[28:31], v[176:179], v[204:207], v[28:31]\n"
"s_waitcnt lgkmcnt(4)\n"
"v_mfma_f32_16x16x32_bf16 v[32:35], v[180:183], v[192:195], v[32:35]\n"
"ds_read_b64_tr_b16 v[176:177], v228 offset:0\n"
"global_load_lds_dwordx4 v238, s[90:91] offset:3072\n"
"s_add_u32 s90, s90, 0x48000\n"
"s_addc_u32 s91, s91, 0\n"
"v_mfma_f32_16x16x32_bf16 v[36:39], v[180:183], v[196:199], v[36:39]\n"
"ds_read_b64_tr_b16 v[178:179], v228 offset:512\n"
"v_mfma_f32_16x16x32_bf16 v[40:43], v[180:183], v[200:203], v[40:43]\n"
"v_mfma_f32_16x16x32_bf16 v[44:47], v[180:183], v[204:207], v[44:47]\n"
"s_waitcnt lgkmcnt(4)\n"
"v_mfma_f32_16x16x32_bf16 v[48:51], v[184:187], v[192:195], v[48:51]\n"
"v_mfma_f32_16x16x32_bf16 v[52:55], v[184:187], v[196:199], v[52:55]\n"
"v_mfma_f32_16x16x32_bf16 v[56:59], v[184:187], v[200:203], v[56:59]\n"
"v_mfma_f32_16x16x32_bf16 v[60:63], v[184:187], v[204:207], v[60:63]\n"
"s_waitcnt vmcnt(9) lgkmcnt(0)\n"
"s_barrier\n"
"ds_read_b64_tr_b16 v[180:181], v224 offset:36864\n"
"v_mfma_f32_16x16x32_bf16 v[64:67], v[188:191], v[192:195], v[64:67]\n"
"ds_read_b64_tr_b16 v[182:183], v224 offset:37376\n"
"v_mfma_f32_16x16x32_bf16 v[68:71], v[188:191], v[196:199], v[68:71]\n"
"v_mfma_f32_16x16x32_bf16 v[72:75], v[188:191], v[200:203], v[72:75]\n"
"ds_read_b64_tr_b16 v[184:185], v225 offset:36864\n"
"v_mfma_f32_16x16x32_bf16 v[76:79], v[188:191], v[204:207], v[76:79]\n"
"ds_read_b64_tr_b16 v[186:187], v225 offset:37376\n"
"v_mfma_f32_16x16x32_bf16 v[80:83], v[176:179], v[192:195], v[80:83]\n"
"v_mfma_f32_16x16x32_bf16 v[84:87], v[176:179], v[196:199], v[84:87]\n"
"v_mfma_f32_16x16x32_bf16 v[88:91], v[176:179], v[200:203], v[88:91]\n"
"ds_read_b64_tr_b16 v[188:189], v226 offset:36864\n"
"ds_read_b64_tr_b16 v[190:191], v226 offset:37376\n"
"v_mfma_f32_16x16x32_bf16 v[92:95], v[176:179], v[204:207], v[92:95]\n"
"s_waitcnt lgkmcnt(4)\n"
"v_mfma_f32_16x16x32_bf16 v[96:99], v[180:183], v[192:195], v[96:99]\n"
"ds_read_b64_tr_b16 v[176:177], v227 offset:36864\n"
"v_mfma_f32_16x16x32_bf16 v[100:103], v[180:183], v[196:199], v[100:103]\n"
"ds_read_b64_tr_b16 v[178:179], v227 offset:37376\n"
"v_mfma_f32_16x16x32_bf16 v[104:107], v[180:183], v[200:203], v[104:107]\n"
"v_mfma_f32_16x16x32_bf16 v[108:111], v[180:183], v[204:207], v[108:111]\n"
"s_waitcnt lgkmcnt(4)\n"
"v_mfma_f32_16x16x32_bf16 v[112:115], v[184:187], v[192:195], v[112:115]\n"
"ds_read_b64_tr_b16 v[180:181], v228 offset:36864\n"
"v_mfma_f32_16x16x32_bf16 v[116:119], v[184:187], v[196:199], v[116:119]\n"
"ds_read_b64_tr_b16 v[182:183], v228 offset:37376\n"
"v_mfma_f32_16x16x32_bf16 v[120:123], v[184:187], v[200:203], v[120:123]\n"
"v_mfma_f32_16x16x32_bf16 v[124:127], v[184:187], v[204:207], v[124:127]\n"
"s_waitcnt lgkmcnt(4)\n"
"v_mfma_f32_16x16x32_bf16 v[128:131], v[188:191], v[192:195], v[128:131]\n"
"v_mfma_f32_16x16x32_bf16 v[132:135], v[188:191], v[196:199], v[132:135]\n"
"v_mfma_f32_16x16x32_bf16 v[136:139], v[188:191], v[200:203], v[136:139]\n"
"v_mfma_f32_16x16x32_bf16 v[140:143], v[188:191], v[204:207], v[140:143]\n"
"s_waitcnt vmcnt(4) lgkmcnt(0)\n"
"s_barrier\n"
"ds_read_b128 v[208:211], v234 offset:8192\n"
"ds_read_b64_tr_b16 v[184:185], v229 offset:0\n"
"v_mfma_f32_16x16x32_bf16 v[144:147], v[176:179], v[192:195], v[144:147]\n"
"ds_read_b64_tr_b16 v[186:187], v229 offset:512\n"
"s_add_u32 s48, s50, 0\n"
"s_addc_u32 s49, s51, 0\n"
"s_mov_b32 m0, s44\n"
"v_mfma_f32_16x16x32_bf16 v[148:151], v[176:179], v[196:199], v[148:151]\n"
"ds_read_b128 v[212:215], v234 offset:9216\n"
"global_load_lds_dwordx4 v250, s[48:49]\n"
"v_mfma_f32_16x16x32_bf16 v[152:155], v[176:179], v[200:203], v[152:155]\n"
"ds_read_b128 v[216:219], v234 offset:10240\n"
"ds_read_b128 v[220:223], v234 offset:11264\n"
"v_mfma_f32_16x16x32_bf16 v[156:159], v[176:179], v[204:207], v[156:159]\n"
"ds_read_b64_tr_b16 v[188:189], v230 offset:0\n"
"v_mfma_f32_16x16x32_bf16 v[160:163], v[180:183], v[192:195], v[160:163]\n"
"ds_read_b64_tr_b16 v[190:191], v230 offset:512\n"
"global_load_lds_dwordx4 v251, s[48:49] offset:1024\n"
"v_mfma_f32_16x16x32_bf16 v[164:167], v[180:183], v[196:199], v[164:167]\n"
"v_mfma_f32_16x16x32_bf16 v[168:171], v[180:183], v[200:203], v[168:171]\n"
"ds_read_b64_tr_b16 v[176:177], v231 offset:0\n"
"ds_read_b64_tr_b16 v[178:179], v231 offset:512\n"
"v_mfma_f32_16x16x32_bf16 v[172:175], v[180:183], v[204:207], v[172:175]\n"
"s_waitcnt lgkmcnt(7)\n"
"v_mfma_f32_16x16x32_bf16 v[16:19], v[184:187], v[208:211], v[16:19]\n"
"ds_read_b64_tr_b16 v[180:181], v232 offset:0\n"
"s_waitcnt lgkmcnt(7)\n"
"v_mfma_f32_16x16x32_bf16 v[20:23], v[184:187], v[212:215], v[20:23]\n"
"ds_read_b64_tr_b16 v[182:183], v232 offset:512\n"
"global_load_lds_dwordx4 v252, s[48:49] offset:2048\n"
"s_waitcnt lgkmcnt(7)\n"
"v_mfma_f32_16x16x32_bf16 v[24:27], v[184:187], v[216:219], v[24:27]\n"
"s_add_u32 m0, s44, 0xc00\n"
"s_waitcnt lgkmcnt(6)\n"
"v_mfma_f32_16x16x32_bf16 v[28:31], v[184:187], v[220:223], v[28:31]\n"
"global_load_lds_dwordx4 v253, s[48:49]\n"
"s_waitcnt lgkmcnt(4)\n"
"v_mfma_f32_16x16x32_bf16 v[32:35], v[188:191], v[208:211], v[32:35]\n"
"ds_read_b64_tr_b16 v[184:185], v233 offset:0\n"
"v_mfma_f32_16x16x32_bf16 v[36:39], v[188:191], v[212:215], v[36:39]\n"
"ds_read_b64_tr_b16 v[186:187], v233 offset:512\n"
"v_mfma_f32_16x16x32_bf16 v[40:43], v[188:191], v[216:219], v[40:43]\n"
"global_load_lds_dwordx4 v254, s[48:49] offset:1024\n"
"v_mfma_f32_16x16x32_bf16 v[44:47], v[188:191], v[220:223], v[44:47]\n"
"s_waitcnt lgkmcnt(4)\n"
"v_mfma_f32_16x16x32_bf16 v[48:51], v[176:179], v[208:211], v[48:51]\n"
"global_load_lds_dwordx4 v255, s[48:49] offset:2048\n"
"v_mfma_f32_16x16x32_bf16 v[52:55], v[176:179], v[212:215], v[52:55]\n"
"v_mfma_f32_16x16x32_bf16 v[56:59], v[176:179], v[216:219], v[56:59]\n"
"v_mfma_f32_16x16x32_bf16 v[60:63], v[176:179], v[220:223], v[60:63]\n"
"s_waitcnt vmcnt(6) lgkmcnt(0)\n"
"s_barrier\n"
"ds_read_b64_tr_b16 v[188:189], v229 offset:36864\n"
"v_mfma_f32_16x16x32_bf16 v[64:67], v[180:183], v[208:211], v[64:67]\n"
"ds_read_b64_tr_b16 v[190:191], v229 offset:37376\n"
"s_mov_b32 s46, 0x24000\n"
"s_cmp_ge_u32 s16, 6\n"
"s_cselect_b32 s46, 0x1b000, s46\n"
"s_ashr_i32 s47, s46, 31\n"
"s_add_u32 s48, s50, s46\n"
"s_addc_u32 s49, s51, s47\n"
"s_add_u32 m0, s45, 0xc000\n"
"v_mfma_f32_16x16x32_bf16 v[68:71], v[180:183], v[212:215], v[68:71]\n"
"global_load_lds_dwordx4 v250, s[48:49]\n"
"v_mfma_f32_16x16x32_bf16 v[72:75], v[180:183], v[216:219], v[72:75]\n"
"ds_read_b64_tr_b16 v[176:177], v230 offset:36864\n"
"v_mfma_f32_16x16x32_bf16 v[76:79], v[180:183], v[220:223], v[76:79]\n"
"ds_read_b64_tr_b16 v[178:179], v230 offset:37376\n"
"v_mfma_f32_16x16x32_bf16 v[80:83], v[184:187], v[208:211], v[80:83]\n"
"global_load_lds_dwordx4 v251, s[48:49] offset:1024\n"
"v_mfma_f32_16x16x32_bf16 v[84:87], v[184:187], v[212:215], v[84:87]\n"
"v_mfma_f32_16x16x32_bf16 v[88:91], v[184:187], v[216:219], v[88:91]\n"
"ds_read_b64_tr_b16 v[180:181], v231 offset:36864\n"
"ds_read_b64_tr_b16 v[182:183], v231 offset:37376\n"
"v_mfma_f32_16x16x32_bf16 v[92:95], v[184:187], v[220:223], v[92:95]\n"
"s_waitcnt lgkmcnt(4)\n"
"v_mfma_f32_16x16x32_bf16 v[96:99], v[188:191], v[208:211], v[96:99]\n"
"ds_read_b64_tr_b16 v[184:185], v232 offset:36864\n"
"v_max_f32 v16, 0, v16\n"
"v_max_f32 v17, 0, v17\n"
"v_max_f32 v18, 0, v18\n"
"v_max_f32 v19, 0, v19\n"
"v_mfma_f32_16x16x32_bf16 v[100:103], v[188:191], v[212:215], v[100:103]\n"
"ds_read_b64_tr_b16 v[186:187], v232 offset:37376\n"
"global_load_lds_dwordx4 v252, s[48:49] offset:2048\n"
"v_max_f32 v20, 0, v20\n"
"v_max_f32 v21, 0, v21\n"
"v_max_f32 v22, 0, v22\n"
"v_max_f32 v23, 0, v23\n"
"v_mfma_f32_16x16x32_bf16 v[104:107], v[188:191], v[216:219], v[104:107]\n"
"s_add_u32 m0, s45, 0xcc00\n"
"v_max_f32 v24, 0, v24\n"
"v_max_f32 v25, 0, v25\n"
"v_max_f32 v26, 0, v26\n"
"v_max_f32 v27, 0, v27\n"
"v_mfma_f32_16x16x32_bf16 v[108:111], v[188:191], v[220:223], v[108:111]\n"
"global_load_lds_dwordx4 v253, s[48:49]\n"
"v_max_f32 v28, 0, v28\n"
"v_max_f32 v29, 0, v29\n"
"v_max_f32 v30, 0, v30\n"
"v_max_f32 v31, 0, v31\n"
"s_waitcnt lgkmcnt(4)\n"
"v_mfma_f32_16x16x32_bf16 v[112:115], v[176:179], v[208:211], v[112:115]\n"
"ds_read_b64_tr_b16 v[188:189], v233 offset:36864\n"
"v_max_f32 v32, 0, v32\n"
"v_max_f32 v33, 0, v33\n"
"v_max_f32 v34, 0, v34\n"
"v_max_f32 v35, 0, v35\n"
"v_mfma_f32_16x16x32_bf16 v[116:119], v[176:179], v[212:215], v[116:119]\n"
"ds_read_b64_tr_b16 v[190:191], v233 offset:37376\n"
"v_max_f32 v36, 0, v36\n"
"v_max_f32 v37, 0, v37\n"
"v_max_f32 v38, 0, v38\n"
"v_max_f32 v39, 0, v39\n"
"v_mfma_f32_16x16x32_bf16 v[120:123], v[176:179], v[216:219], v[120:123]\n"
"global_load_lds_dwordx4 v254, s[48:49] offset:1024\n"
"v_max_f32 v40, 0, v40\n"
"v_max_f32 v41, 0, v41\n"
"v_max_f32 v42, 0, v42\n"
"v_max_f32 v43, 0, v43\n"
"v_mfma_f32_16x16x32_bf16 v[124:127], v[176:179], v[220:223], v[124:127]\n"
"v_max_f32 v44, 0, v44\n"
"v_max_f32 v45, 0, v45\n"
"v_max_f32 v46, 0, v46\n"
"v_max_f32 v47, 0, v47\n"
"s_waitcnt lgkmcnt(4)\n"
"v_mfma_f32_16x16x32_bf16 v[128:131], v[180:183], v[208:211], v[128:131]\n"
"global_load_lds_dwordx4 v255, s[48:49] offset:2048\n"
"v_max_f32 v48, 0, v48\n"
"v_max_f32 v49, 0, v49\n"
"v_max_f32 v50, 0, v50\n"
"v_max_f32 v51, 0, v51\n"
"v_mfma_f32_16x16x32_bf16 v[132:135], v[180:183], v[212:215], v[132:135]\n"
"v_max_f32 v52, 0, v52\n"
"v_max_f32 v53, 0, v53\n"
"v_max_f32 v54, 0, v54\n"
"v_max_f32 v55, 0, v55\n"
"v_mfma_f32_16x16x32_bf16 v[136:139], v[180:183], v[216:219], v[136:139]\n"
"v_max_f32 v56, 0, v56\n"
"v_max_f32 v57, 0, v57\n"
"v_max_f32 v58, 0, v58\n"
"v_max_f32 v59, 0, v59\n"
"v_mul_f32 v16, v16, v96\n"
"v_mul_f32 v17, v17, v97\n"
"v_mul_f32 v18, v18, v98\n"
"v_mul_f32 v19, v19, v99\n"
"v_cvt_pk_bf16_f32 v16, v16, v17\n"
"v_cvt_pk_bf16_f32 v17, v18, v19\n"
"v_mfma_f32_16x16x32_bf16 v[140:143], v[180:183], v[220:223], v[140:143]\n"
"s_waitcnt lgkmcnt(0)\n"
"s_barrier\n"
"v_max_f32 v60, 0, v60\n"
"v_max_f32 v61, 0, v61\n"
"v_max_f32 v62, 0, v62\n"
"v_max_f32 v63, 0, v63\n"
"v_mul_f32 v20, v20, v100\n"
"v_mul_f32 v21, v21, v101\n"
"v_mul_f32 v22, v22, v102\n"
"v_mul_f32 v23, v23, v103\n"
"v_cvt_pk_bf16_f32 v20, v20, v21\n"
"v_cvt_pk_bf16_f32 v21, v22, v23\n"
"v_mfma_f32_16x16x32_bf16 v[144:147], v[184:187], v[208:211], v[144:147]\n"
"v_max_f32 v64, 0, v64\n"
"v_max_f32 v65, 0, v65\n"
"v_max_f32 v66, 0, v66\n"
"v_max_f32 v67, 0, v67\n"
"v_mul_f32 v24, v24, v104\n"
"v_mul_f32 v25, v25, v105\n"
"v_mul_f32 v26, v26, v106\n"
"v_mul_f32 v27, v27, v107\n"
"v_cvt_pk_bf16_f32 v24, v24, v25\n"
"v_cvt_pk_bf16_f32 v25, v26, v27\n"
"v_mfma_f32_16x16x32_bf16 v[148:151], v[184:187], v[212:215], v[148:151]\n"
"v_max_f32 v68, 0, v68\n"
"v_max_f32 v69, 0, v69\n"
"v_max_f32 v70, 0, v70\n"
"v_max_f32 v71, 0, v71\n"
"v_mul_f32 v28, v28, v108\n"
"v_mul_f32 v29, v29, v109\n"
"v_mul_f32 v30, v30, v110\n"
"v_mul_f32 v31, v31, v111\n"
"v_cvt_pk_bf16_f32 v28, v28, v29\n"
"v_cvt_pk_bf16_f32 v29, v30, v31\n"
"v_mfma_f32_16x16x32_bf16 v[152:155], v[184:187], v[216:219], v[152:155]\n"
"v_max_f32 v72, 0, v72\n"
"v_max_f32 v73, 0, v73\n"
"v_max_f32 v74, 0, v74\n"
"v_max_f32 v75, 0, v75\n"
"v_mul_f32 v32, v32, v112\n"
"v_mul_f32 v33, v33, v113\n"
"v_mul_f32 v34, v34, v114\n"
"v_mul_f32 v35, v35, v115\n"
"v_cvt_pk_bf16_f32 v32, v32, v33\n"
"v_cvt_pk_bf16_f32 v33, v34, v35\n"
"v_mfma_f32_16x16x32_bf16 v[156:159], v[184:187], v[220:223], v[156:159]\n"
"v_max_f32 v76, 0, v76\n"
"v_max_f32 v77, 0, v77\n"
"v_max_f32 v78, 0, v78\n"
"v_max_f32 v79, 0, v79\n"
"v_mul_f32 v36, v36, v116\n"
"v_mul_f32 v37, v37, v117\n"
"v_mul_f32 v38, v38, v118\n"
"v_mul_f32 v39, v39, v119\n"
"v_cvt_pk_bf16_f32 v36, v36, v37\n"
"v_cvt_pk_bf16_f32 v37, v38, v39\n"
"v_mfma_f32_16x16x32_bf16 v[160:163], v[188:191], v[208:211], v[160:163]\n"
"v_max_f32 v80, 0, v80\n"
"v_max_f32 v81, 0, v81\n"
"v_max_f32 v82, 0, v82\n"
"v_max_f32 v83, 0, v83\n"
"v_mul_f32 v40, v40, v120\n"
"v_mul_f32 v41, v41, v121\n"
"v_mul_f32 v42, v42, v122\n"
"v_mul_f32 v43, v43, v123\n"
"v_cvt_pk_bf16_f32 v40, v40, v41\n"
"v_cvt_pk_bf16_f32 v41, v42, v43\n"
"v_mfma_f32_16x16x32_bf16 v[164:167], v[188:191], v[212:215], v[164:167]\n"
"v_max_f32 v84, 0, v84\n"
"v_max_f32 v85, 0, v85\n"
"v_max_f32 v86, 0, v86\n"
"v_max_f32 v87, 0, v87\n"
"v_mul_f32 v44, v44, v124\n"
"v_mul_f32 v45, v45, v125\n"
"v_mul_f32 v46, v46, v126\n"
"v_mul_f32 v47, v47, v127\n"
"v_cvt_pk_bf16_f32 v44, v44, v45\n"
"v_cvt_pk_bf16_f32 v45, v46, v47\n"
"v_mfma_f32_16x16x32_bf16 v[168:171], v[188:191], v[216:219], v[168:171]\n"
"v_max_f32 v88, 0, v88\n"
"v_max_f32 v89, 0, v89\n"
"v_max_f32 v90, 0, v90\n"
"v_max_f32 v91, 0, v91\n"
"v_mul_f32 v48, v48, v128\n"
"v_mul_f32 v49, v49, v129\n"
"v_mul_f32 v50, v50, v130\n"
"v_mul_f32 v51, v51, v131\n"
"v_cvt_pk_bf16_f32 v48, v48, v49\n"
"v_cvt_pk_bf16_f32 v49, v50, v51\n"
"v_mfma_f32_16x16x32_bf16 v[172:175], v[188:191], v[220:223], v[172:175]\n"
"v_max_f32 v92, 0, v92\n"
"v_max_f32 v93, 0, v93\n"
"v_max_f32 v94, 0, v94\n"
"v_max_f32 v95, 0, v95\n"
"v_mul_f32 v52, v52, v132\n"
"v_mul_f32 v53, v53, v133\n"
"v_mul_f32 v54, v54, v134\n"
"v_mul_f32 v55, v55, v135\n"
"v_cvt_pk_bf16_f32 v52, v52, v53\n"
"v_cvt_pk_bf16_f32 v53, v54, v55\n"
"v_mul_f32 v56, v56, v136\n"
"v_mul_f32 v57, v57, v137\n"
"v_mul_f32 v58, v58, v138\n"
"v_mul_f32 v59, v59, v139\n"
"v_cvt_pk_bf16_f32 v56, v56, v57\n"
"v_cvt_pk_bf16_f32 v57, v58, v59\n"
"v_mul_f32 v60, v60, v140\n"
"v_mul_f32 v61, v61, v141\n"
"v_mul_f32 v62, v62, v142\n"
"v_mul_f32 v63, v63, v143\n"
"v_cvt_pk_bf16_f32 v60, v60, v61\n"
"v_cvt_pk_bf16_f32 v61, v62, v63\n"
"ds_write_b64 v13, v[16:17] offset:0\n"
"ds_write_b64 v13, v[20:21] offset:1024\n"
"ds_write_b64 v13, v[24:25] offset:2048\n"
"ds_write_b64 v13, v[28:29] offset:3072\n"
"ds_write_b64 v14, v[32:33] offset:0\n"
"ds_write_b64 v14, v[36:37] offset:1024\n"
"ds_write_b64 v14, v[40:41] offset:2048\n"
"ds_write_b64 v14, v[44:45] offset:3072\n"
"ds_write_b64 v13, v[48:49] offset:4096\n"
"ds_write_b64 v13, v[52:53] offset:5120\n"
"ds_write_b64 v13, v[56:57] offset:6144\n"
"ds_write_b64 v13, v[60:61] offset:7168\n"
"v_mul_f32 v64, v64, v144\n"
"v_mul_f32 v65, v65, v145\n"
"v_mul_f32 v66, v66, v146\n"
"v_mul_f32 v67, v67, v147\n"
"v_cvt_pk_bf16_f32 v64, v64, v65\n"
"v_cvt_pk_bf16_f32 v65, v66, v67\n"
"ds_write_b64 v14, v[64:65] offset:4096\n"
"v_mul_f32 v68, v68, v148\n"
"v_mul_f32 v69, v69, v149\n"
"v_mul_f32 v70, v70, v150\n"
"v_mul_f32 v71, v71, v151\n"
"v_cvt_pk_bf16_f32 v68, v68, v69\n"
"v_cvt_pk_bf16_f32 v69, v70, v71\n"
"ds_write_b64 v14, v[68:69] offset:5120\n"
"v_mul_f32 v72, v72, v152\n"
"v_mul_f32 v73, v73, v153\n"
"v_mul_f32 v74, v74, v154\n"
"v_mul_f32 v75, v75, v155\n"
"v_cvt_pk_bf16_f32 v72, v72, v73\n"
"v_cvt_pk_bf16_f32 v73, v74, v75\n"
"ds_write_b64 v14, v[72:73] offset:6144\n"
"v_mul_f32 v76, v76, v156\n"
"v_mul_f32 v77, v77, v157\n"
"v_mul_f32 v78, v78, v158\n"
"v_mul_f32 v79, v79, v159\n"
"v_cvt_pk_bf16_f32 v76, v76, v77\n"
"v_cvt_pk_bf16_f32 v77, v78, v79\n"
"ds_write_b64 v14, v[76:77] offset:7168\n"
"v_mul_f32 v80, v80, v160\n"
"v_mul_f32 v81, v81, v161\n"
"v_mul_f32 v82, v82, v162\n"
"v_mul_f32 v83, v83, v163\n"
"v_cvt_pk_bf16_f32 v80, v80, v81\n"
"v_cvt_pk_bf16_f32 v81, v82, v83\n"
"ds_write_b64 v13, v[80:81] offset:8192\n"
"v_mul_f32 v84, v84, v164\n"
"v_mul_f32 v85, v85, v165\n"
"v_mul_f32 v86, v86, v166\n"
"v_mul_f32 v87, v87, v167\n"
"v_cvt_pk_bf16_f32 v84, v84, v85\n"
"v_cvt_pk_bf16_f32 v85, v86, v87\n"
"ds_write_b64 v13, v[84:85] offset:9216\n"
"v_mul_f32 v88, v88, v168\n"
"v_mul_f32 v89, v89, v169\n"
"v_mul_f32 v90, v90, v170\n"
"v_mul_f32 v91, v91, v171\n"
"v_cvt_pk_bf16_f32 v88, v88, v89\n"
"v_cvt_pk_bf16_f32 v89, v90, v91\n"
"ds_write_b64 v13, v[88:89] offset:10240\n"
"v_mul_f32 v92, v92, v172\n"
"v_mul_f32 v93, v93, v173\n"
"v_mul_f32 v94, v94, v174\n"
"v_mul_f32 v95, v95, v175\n"
"v_cvt_pk_bf16_f32 v92, v92, v93\n"
"v_cvt_pk_bf16_f32 v93, v94, v95\n"
"ds_write_b64 v13, v[92:93] offset:11264\n"
"s_nop 1\n"
"s_branch F2_JOIN\n"
"F2_CLSB:\n"
"ds_read_b128 v[192:195], v234 offset:0\n"
"ds_read_b64_tr_b16 v[176:177], v224 offset:0\n"
"ds_read_b64_tr_b16 v[178:179], v224 offset:512\n"
"ds_read_b128 v[196:199], v234 offset:1024\n"
"ds_read_b128 v[200:203], v234 offset:2048\n"
"ds_read_b128 v[204:207], v234 offset:3072\n"
"ds_read_b64_tr_b16 v[180:181], v225 offset:0\n"
"ds_read_b64_tr_b16 v[182:183], v225 offset:512\n"
"ds_read_b64_tr_b16 v[184:185], v226 offset:0\n"
"ds_read_b64_tr_b16 v[186:187], v226 offset:512\n"
"s_waitcnt lgkmcnt(7)\n"
"v_mfma_f32_16x16x32_bf16 v[16:19], v[176:179], v[192:195], 0\n"
"ds_read_b64_tr_b16 v[188:189], v227 offset:0\n"
"s_waitcnt lgkmcnt(7)\n"
"v_mfma_f32_16x16x32_bf16 v[20:23], v[176:179], v[196:199], 0\n"
"ds_read_b64_tr_b16 v[190:191], v227 offset:512\n"
"s_waitcnt lgkmcnt(7)\n"
"v_mfma_f32_16x16x32_bf16 v[24:27], v[176:179], v[200:203], 0\n"
"s_waitcnt lgkmcnt(6)\n"
"v_mfma_f32_16x16x32_bf16 v[28:31], v[176:179], v[204:207], 0\n"
"s_waitcnt lgkmcnt(4)\n"
"v_mfma_f32_16x16x32_bf16 v[32:35], v[180:183], v[192:195], 0\n"
"v_mfma_f32_16x16x32_bf16 v[36:39], v[180:183], v[196:199], 0\n"
"v_mfma_f32_16x16x32_bf16 v[40:43], v[180:183], v[200:203], 0\n"
"v_mfma_f32_16x16x32_bf16 v[44:47], v[180:183], v[204:207], 0\n"
"s_waitcnt vmcnt(11) lgkmcnt(0)\n"
"s_barrier\n"
"ds_read_b64_tr_b16 v[176:177], v224 offset:36864\n"
"v_mfma_f32_16x16x32_bf16 v[48:51], v[184:187], v[192:195], 0\n"
"ds_read_b64_tr_b16 v[178:179], v224 offset:37376\n"
"s_mov_b32 m0, s82\n"
"v_mfma_f32_16x16x32_bf16 v[52:55], v[184:187], v[196:199], 0\n"
"global_load_lds_dwordx4 v235, s[88:89]\n"
"v_mfma_f32_16x16x32_bf16 v[56:59], v[184:187], v[200:203], 0\n"
"ds_read_b64_tr_b16 v[180:181], v225 offset:36864\n"
"v_mfma_f32_16x16x32_bf16 v[60:63], v[184:187], v[204:207], 0\n"
"ds_read_b64_tr_b16 v[182:183], v225 offset:37376\n"
"v_mfma_f32_16x16x32_bf16 v[64:67], v[188:191], v[192:195], 0\n"
"global_load_lds_dwordx4 v236, s[88:89] offset:1024\n"
"v_mfma_f32_16x16x32_bf16 v[68:71], v[188:191], v[196:199], 0\n"
"v_mfma_f32_16x16x32_bf16 v[72:75], v[188:191], v[200:203], 0\n"
"ds_read_b64_tr_b16 v[184:185], v226 offset:36864\n"
"ds_read_b64_tr_b16 v[186:187], v226 offset:37376\n"
"v_mfma_f32_16x16x32_bf16 v[76:79], v[188:191], v[204:207], 0\n"
"s_waitcnt lgkmcnt(4)\n"
"v_mfma_f32_16x16x32_bf16 v[96:99], v[176:179], v[192:195], 0\n"
"ds_read_b64_tr_b16 v[188:189], v227 offset:36864\n"
"v_mfma_f32_16x16x32_bf16 v[100:103], v[176:179], v[196:199], 0\n"
"ds_read_b64_tr_b16 v[190:191], v227 offset:37376\n"
"global_load_lds_dwordx4 v237, s[88:89] offset:2048\n"
"v_mfma_f32_16x16x32_bf16 v[104:107], v[176:179], v[200:203], 0\n"
"v_mfma_f32_16x16x32_bf16 v[108:111], v[176:179], v[204:207], 0\n"
"global_load_lds_dwordx4 v238, s[88:89] offset:3072\n"
"s_waitcnt lgkmcnt(4)\n"
"v_mfma_f32_16x16x32_bf16 v[112:115], v[180:183], v[192:195], 0\n"
"s_add_u32 m0, s82, 0x1000\n"
"v_mfma_f32_16x16x32_bf16 v[116:119], v[180:183], v[196:199], 0\n"
"global_load_lds_dwordx4 v239, s[88:89]\n"
"s_add_u32 s88, s88, 0x48000\n"
"s_addc_u32 s89, s89, 0\n"
"v_mfma_f32_16x16x32_bf16 v[120:123], v[180:183], v[200:203], 0\n"
"v_mfma_f32_16x16x32_bf16 v[124:127], v[180:183], v[204:207], 0\n"
"s_waitcnt vmcnt(12) lgkmcnt(0)\n"
"s_barrier\n"
"ds_read_b128 v[208:211], v234 offset:4096\n"
"ds_read_b64_tr_b16 v[176:177], v229 offset:0\n"
"v_mfma_f32_16x16x32_bf16 v[128:131], v[184:187], v[192:195], 0\n"
"ds_read_b64_tr_b16 v[178:179], v229 offset:512\n"
"s_add_u32 m0, s82, 0x9000\n"
"v_mfma_f32_16x16x32_bf16 v[132:135], v[184:187], v[196:199], 0\n"
"ds_read_b128 v[212:215], v234 offset:5120\n"
"global_load_lds_dwordx4 v235, s[90:91]\n"
"v_mfma_f32_16x16x32_bf16 v[136:139], v[184:187], v[200:203], 0\n"
"ds_read_b128 v[216:219], v234 offset:6144\n"
"ds_read_b128 v[220:223], v234 offset:7168\n"
"v_mfma_f32_16x16x32_bf16 v[140:143], v[184:187], v[204:207], 0\n"
"ds_read_b64_tr_b16 v[180:181], v230 offset:0\n"
"v_mfma_f32_16x16x32_bf16 v[144:147], v[188:191], v[192:195], 0\n"
"ds_read_b64_tr_b16 v[182:183], v230 offset:512\n"
"global_load_lds_dwordx4 v236, s[90:91] offset:1024\n"
"v_mfma_f32_16x16x32_bf16 v[148:151], v[188:191], v[196:199], 0\n"
"v_mfma_f32_16x16x32_bf16 v[152:155], v[188:191], v[200:203], 0\n"
"ds_read_b64_tr_b16 v[184:185], v231 offset:0\n"
"ds_read_b64_tr_b16 v[186:187], v231 offset:512\n"
"v_mfma_f32_16x16x32_bf16 v[156:159], v[188:191], v[204:207], 0\n"
"s_waitcnt lgkmcnt(7)\n"
"v_mfma_f32_16x16x32_bf16 v[16:19], v[176:179], v[208:211], v[16:19]\n"
"ds_read_b64_tr_b16 v[188:189], v232 offset:0\n"
"s_waitcnt lgkmcnt(7)\n"
"v_mfma_f32_16x16x32_bf16 v[20:23], v[176:179], v[212:215], v[20:23]\n"
"ds_read_b64_tr_b16 v[190:191], v232 offset:512\n"
"global_load_lds_dwordx4 v237, s[90:91] offset:2048\n"
"s_waitcnt lgkmcnt(7)\n"
"v_mfma_f32_16x16x32_bf16 v[24:27], v[176:179], v[216:219], v[24:27]\n"
"s_waitcnt lgkmcnt(6)\n"
"v_mfma_f32_16x16x32_bf16 v[28:31], v[176:179], v[220:223], v[28:31]\n"
"global_load_lds_dwordx4 v238, s[90:91] offset:3072\n"
"s_waitcnt lgkmcnt(4)\n"
"v_mfma_f32_16x16x32_bf16 v[32:35], v[180:183], v[208:211], v[32:35]\n"
"s_add_u32 m0, s82, 0xa000\n"
"v_mfma_f32_16x16x32_bf16 v[36:39], v[180:183], v[212:215], v[36:39]\n"
"global_load_lds_dwordx4 v239, s[90:91]\n"
"s_add_u32 s90, s90, 0x48000\n"
"s_addc_u32 s91, s91, 0\n"
"v_mfma_f32_16x16x32_bf16 v[40:43], v[180:183], v[216:219], v[40:43]\n"
"v_mfma_f32_16x16x32_bf16 v[44:47], v[180:183], v[220:223], v[44:47]\n"
"s_waitcnt vmcnt(13) lgkmcnt(0)\n"
"s_barrier\n"
"ds_read_b64_tr_b16 v[176:177], v229 offset:36864\n"
"v_mfma_f32_16x16x32_bf16 v[48:51], v[184:187], v[208:211], v[48:51]\n"
"ds_read_b64_tr_b16 v[178:179], v229 offset:37376\n"
"s_add_u32 m0, s82, 0x12000\n"
"v_mfma_f32_16x16x32_bf16 v[52:55], v[184:187], v[212:215], v[52:55]\n"
"global_load_lds_dwordx4 v235, s[88:89]\n"
"v_mfma_f32_16x16x32_bf16 v[56:59], v[184:187], v[216:219], v[56:59]\n"
"ds_read_b64_tr_b16 v[180:181], v230 offset:36864\n"
"v_mfma_f32_16x16x32_bf16 v[60:63], v[184:187], v[220:223], v[60:63]\n"
"ds_read_b64_tr_b16 v[182:183], v230 offset:37376\n"
"v_mfma_f32_16x16x32_bf16 v[64:67], v[188:191], v[208:211], v[64:67]\n"
"global_load_lds_dwordx4 v236, s[88:89] offset:1024\n"
"v_mfma_f32_16x16x32_bf16 v[68:71], v[188:191], v[212:215], v[68:71]\n"
"v_mfma_f32_16x16x32_bf16 v[72:75], v[188:191], v[216:219], v[72:75]\n"
"ds_read_b64_tr_b16 v[184:185], v231 offset:36864\n"
"ds_read_b64_tr_b16 v[186:187], v231 offset:37376\n"
"v_mfma_f32_16x16x32_bf16 v[76:79], v[188:191], v[220:223], v[76:79]\n"
"s_waitcnt lgkmcnt(4)\n"
"v_mfma_f32_16x16x32_bf16 v[96:99], v[176:179], v[208:211], v[96:99]\n"
"ds_read_b64_tr_b16 v[188:189], v232 offset:36864\n"
"v_mfma_f32_16x16x32_bf16 v[100:103], v[176:179], v[212:215], v[100:103]\n"
"ds_read_b64_tr_b16 v[190:191], v232 offset:37376\n"
"global_load_lds_dwordx4 v237, s[88:89] offset:2048\n"
"v_mfma_f32_16x16x32_bf16 v[104:107], v[176:179], v[216:219], v[104:107]\n"
"v_mfma_f32_16x16x32_bf16 v[108:111], v[176:179], v[220:223], v[108:111]\n"
"global_load_lds_dwordx4 v238, s[88:89] offset:3072\n"
"s_waitcnt lgkmcnt(4)\n"
"v_mfma_f32_16x16x32_bf16 v[112:115], v[180:183], v[208:211], v[112:115]\n"
"s_add_u32 m0, s82, 0x13000\n"
"v_mfma_f32_16x16x32_bf16 v[116:119], v[180:183], v[212:215], v[116:119]\n"
"global_load_lds_dwordx4 v239, s[88:89]\n"
"s_add_u32 s88, s88, 0x48000\n"
"s_addc_u32 s89, s89, 0\n"
"v_mfma_f32_16x16x32_bf16 v[120:123], v[180:183], v[216:219], v[120:123]\n"
"v_mfma_f32_16x16x32_bf16 v[124:127], v[180:183], v[220:223], v[124:127]\n"
"s_waitcnt vmcnt(10) lgkmcnt(0)\n"
"s_barrier\n"
"ds_read_b128 v[192:195], v234 offset:8192\n"
"ds_read_b64_tr_b16 v[176:177], v224 offset:0\n"
"v_mfma_f32_16x16x32_bf16 v[128:131], v[184:187], v[208:211], v[128:131]\n"
"ds_read_b64_tr_b16 v[178:179], v224 offset:512\n"
"s_add_u32 m0, s82, 0x1b000\n"
"v_mfma_f32_16x16x32_bf16 v[132:135], v[184:187], v[212:215], v[132:135]\n"
"ds_read_b128 v[196:199], v234 offset:9216\n"
"global_load_lds_dwordx4 v235, s[90:91]\n"
"v_mfma_f32_16x16x32_bf16 v[136:139], v[184:187], v[216:219], v[136:139]\n"
"ds_read_b128 v[200:203], v234 offset:10240\n"
"ds_read_b128 v[204:207], v234 offset:11264\n"
"v_mfma_f32_16x16x32_bf16 v[140:143], v[184:187], v[220:223], v[140:143]\n"
"ds_read_b64_tr_b16 v[180:181], v225 offset:0\n"
"v_mfma_f32_16x16x32_bf16 v[144:147], v[188:191], v[208:211], v[144:147]\n"
"ds_read_b64_tr_b16 v[182:183], v225 offset:512\n"
"global_load_lds_dwordx4 v236, s[90:91] offset:1024\n"
"v_mfma_f32_16x16x32_bf16 v[148:151], v[188:191], v[212:215], v[148:151]\n"
"v_mfma_f32_16x16x32_bf16 v[152:155], v[188:191], v[216:219], v[152:155]\n"
"ds_read_b64_tr_b16 v[184:185], v226 offset:0\n"
"ds_read_b64_tr_b16 v[186:187], v226 offset:512\n"
"v_mfma_f32_16x16x32_bf16 v[156:159], v[188:191], v[220:223], v[156:159]\n"
"s_waitcnt lgkmcnt(7)\n"
"v_mfma_f32_16x16x32_bf16 v[16:19], v[176:179], v[192:195], v[16:19]\n"
"ds_read_b64_tr_b16 v[188:189], v227 offset:0\n"
"s_waitcnt lgkmcnt(7)\n"
"v_mfma_f32_16x16x32_bf16 v[20:23], v[176:179], v[196:199], v[20:23]\n"
"ds_read_b64_tr_b16 v[190:191], v227 offset:512\n"
"global_load_lds_dwordx4 v237, s[90:91] offset:2048\n"
"s_waitcnt lgkmcnt(7)\n"
"v_mfma_f32_16x16x32_bf16 v[24:27], v[176:179], v[200:203], v[24:27]\n"
"s_waitcnt lgkmcnt(6)\n"
"v_mfma_f32_16x16x32_bf16 v[28:31], v[176:179], v[204:207], v[28:31]\n"
"global_load_lds_dwordx4 v238, s[90:91] offset:3072\n"
"s_waitcnt lgkmcnt(4)\n"
"v_mfma_f32_16x16x32_bf16 v[32:35], v[180:183], v[192:195], v[32:35]\n"
"s_add_u32 m0, s82, 0x1c000\n"
"v_mfma_f32_16x16x32_bf16 v[36:39], v[180:183], v[196:199], v[36:39]\n"
"global_load_lds_dwordx4 v239, s[90:91]\n"
"s_add_u32 s90, s90, 0x48000\n"
"s_addc_u32 s91, s91, 0\n"
"v_mfma_f32_16x16x32_bf16 v[40:43], v[180:183], v[200:203], v[40:43]\n"
"v_mfma_f32_16x16x32_bf16 v[44:47], v[180:183], v[204:207], v[44:47]\n"
"s_waitcnt vmcnt(10) lgkmcnt(0)\n"
"s_barrier\n"
"ds_read_b64_tr_b16 v[176:177], v224 offset:36864\n"
"v_mfma_f32_16x16x32_bf16 v[48:51], v[184:187], v[192:195], v[48:51]\n"
"ds_read_b64_tr_b16 v[178:179], v224 offset:37376\n"
"s_mov_b32 m0, s82\n"
"v_mfma_f32_16x16x32_bf16 v[52:55], v[184:187], v[196:199], v[52:55]\n"
"global_load_lds_dwordx4 v235, s[88:89]\n"
"v_mfma_f32_16x16x32_bf16 v[56:59], v[184:187], v[200:203], v[56:59]\n"
"ds_read_b64_tr_b16 v[180:181], v225 offset:36864\n"
"v_mfma_f32_16x16x32_bf16 v[60:63], v[184:187], v[204:207], v[60:63]\n"
"ds_read_b64_tr_b16 v[182:183], v225 offset:37376\n"
"v_mfma_f32_16x16x32_bf16 v[64:67], v[188:191], v[192:195], v[64:67]\n"
"global_load_lds_dwordx4 v236, s[88:89] offset:1024\n"
"v_mfma_f32_16x16x32_bf16 v[68:71], v[188:191], v[196:199], v[68:71]\n"
"v_mfma_f32_16x16x32_bf16 v[72:75], v[188:191], v[200:203], v[72:75]\n"
"ds_read_b64_tr_b16 v[184:185], v226 offset:36864\n"
"ds_read_b64_tr_b16 v[186:187], v226 offset:37376\n"
"v_mfma_f32_16x16x32_bf16 v[76:79], v[188:191], v[204:207], v[76:79]\n"
"s_waitcnt lgkmcnt(4)\n"
"v_mfma_f32_16x16x32_bf16 v[96:99], v[176:179], v[192:195], v[96:99]\n"
"ds_read_b64_tr_b16 v[188:189], v227 offset:36864\n"
"v_mfma_f32_16x16x32_bf16 v[100:103], v[176:179], v[196:199], v[100:103]\n"
"ds_read_b64_tr_b16 v[190:191], v227 offset:37376\n"
"global_load_lds_dwordx4 v237, s[88:89] offset:2048\n"
"v_mfma_f32_16x16x32_bf16 v[104:107], v[176:179], v[200:203], v[104:107]\n"
"v_mfma_f32_16x16x32_bf16 v[108:111], v[176:179], v[204:207], v[108:111]\n"
"global_load_lds_dwordx4 v238, s[88:89] offset:3072\n"
"s_waitcnt lgkmcnt(4)\n"
"v_mfma_f32_16x16x32_bf16 v[112:115], v[180:183], v[192:195], v[112:115]\n"
"s_add_u32 m0, s82, 0x1000\n"
"v_mfma_f32_16x16x32_bf16 v[116:119], v[180:183], v[196:199], v[116:119]\n"
"global_load_lds_dwordx4 v239, s[88:89]\n"
"s_add_u32 s88, s88, 0x48000\n"
"s_addc_u32 s89, s89, 0\n"
"v_mfma_f32_16x16x32_bf16 v[120:123], v[180:183], v[200:203], v[120:123]\n"
"v_mfma_f32_16x16x32_bf16 v[124:127], v[180:183], v[204:207], v[124:127]\n"
"s_waitcnt vmcnt(10) lgkmcnt(0)\n"
"s_barrier\n"
"ds_read_b128 v[208:211], v234 offset:0\n"
"ds_read_b64_tr_b16 v[176:177], v229 offset:0\n"
"v_mfma_f32_16x16x32_bf16 v[128:131], v[184:187], v[192:195], v[128:131]\n"
"ds_read_b64_tr_b16 v[178:179], v229 offset:512\n"
"s_add_u32 m0, s82, 0x9000\n"
"v_mfma_f32_16x16x32_bf16 v[132:135], v[184:187], v[196:199], v[132:135]\n"
"ds_read_b128 v[212:215], v234 offset:1024\n"
"global_load_lds_dwordx4 v235, s[90:91]\n"
"v_mfma_f32_16x16x32_bf16 v[136:139], v[184:187], v[200:203], v[136:139]\n"
"ds_read_b128 v[216:219], v234 offset:2048\n"
"ds_read_b128 v[220:223], v234 offset:3072\n"
"v_mfma_f32_16x16x32_bf16 v[140:143], v[184:187], v[204:207], v[140:143]\n"
"ds_read_b64_tr_b16 v[180:181], v230 offset:0\n"
"v_mfma_f32_16x16x32_bf16 v[144:147], v[188:191], v[192:195], v[144:147]\n"
"ds_read_b64_tr_b16 v[182:183], v230 offset:512\n"
"global_load_lds_dwordx4 v236, s[90:91] offset:1024\n"
"v_mfma_f32_16x16x32_bf16 v[148:151], v[188:191], v[196:199], v[148:151]\n"
"v_mfma_f32_16x16x32_bf16 v[152:155], v[188:191], v[200:203], v[152:155]\n"
"ds_read_b64_tr_b16 v[184:185], v231 offset:0\n"
"ds_read_b64_tr_b16 v[186:187], v231 offset:512\n"
"v_mfma_f32_16x16x32_bf16 v[156:159], v[188:191], v[204:207], v[156:159]\n"
"s_waitcnt lgkmcnt(7)\n"
"v_mfma_f32_16x16x32_bf16 v[16:19], v[176:179], v[208:211], v[16:19]\n"
"ds_read_b64_tr_b16 v[188:189], v232 offset:0\n"
"s_waitcnt lgkmcnt(7)\n"
"v_mfma_f32_16x16x32_bf16 v[20:23], v[176:179], v[212:215], v[20:23]\n"
"ds_read_b64_tr_b16 v[190:191], v232 offset:512\n"
"global_load_lds_dwordx4 v237, s[90:91] offset:2048\n"
"s_waitcnt lgkmcnt(7)\n"
"v_mfma_f32_16x16x32_bf16 v[24:27], v[176:179], v[216:219], v[24:27]\n"
"s_waitcnt lgkmcnt(6)\n"
"v_mfma_f32_16x16x32_bf16 v[28:31], v[176:179], v[220:223], v[28:31]\n"
"global_load_lds_dwordx4 v238, s[90:91] offset:3072\n"
"s_waitcnt lgkmcnt(4)\n"
"v_mfma_f32_16x16x32_bf16 v[32:35], v[180:183], v[208:211], v[32:35]\n"
"s_add_u32 m0, s82, 0xa000\n"
"v_mfma_f32_16x16x32_bf16 v[36:39], v[180:183], v[212:215], v[36:39]\n"
"global_load_lds_dwordx4 v239, s[90:91]\n"
"s_add_u32 s90, s90, 0x48000\n"
"s_addc_u32 s91, s91, 0\n"
"v_mfma_f32_16x16x32_bf16 v[40:43], v[180:183], v[216:219], v[40:43]\n"
"v_mfma_f32_16x16x32_bf16 v[44:47], v[180:183], v[220:223], v[44:47]\n"
"s_waitcnt vmcnt(10) lgkmcnt(0)\n"
"s_barrier\n"
"ds_read_b64_tr_b16 v[176:177], v229 offset:36864\n"
"v_mfma_f32_16x16x32_bf16 v[48:51], v[184:187], v[208:211], v[48:51]\n"
"ds_read_b64_tr_b16 v[178:179], v229 offset:37376\n"
"s_add_u32 m0, s82, 0x12000\n"
"v_mfma_f32_16x16x32_bf16 v[52:55], v[184:187], v[212:215], v[52:55]\n"
"global_load_lds_dwordx4 v235, s[88:89]\n"
"v_mfma_f32_16x16x32_bf16 v[56:59], v[184:187], v[216:219], v[56:59]\n"
"ds_read_b64_tr_b16 v[180:181], v230 offset:36864\n"
"v_mfma_f32_16x16x32_bf16 v[60:63], v[184:187], v[220:223], v[60:63]\n"
"ds_read_b64_tr_b16 v[182:183], v230 offset:37376\n"
"v_mfma_f32_16x16x32_bf16 v[64:67], v[188:191], v[208:211], v[64:67]\n"
"global_load_lds_dwordx4 v236, s[88:89] offset:1024\n"
"v_mfma_f32_16x16x32_bf16 v[68:71], v[188:191], v[212:215], v[68:71]\n"
"v_mfma_f32_16x16x32_bf16 v[72:75], v[188:191], v[216:219], v[72:75]\n"
"ds_read_b64_tr_b16 v[184:185], v231 offset:36864\n"
"ds_read_b64_tr_b16 v[186:187], v231 offset:37376\n"
"v_mfma_f32_16x16x32_bf16 v[76:79], v[188:191], v[220:223], v[76:79]\n"
"s_waitcnt lgkmcnt(4)\n"
"v_mfma_f32_16x16x32_bf16 v[96:99], v[176:179], v[208:211], v[96:99]\n"
"ds_read_b64_tr_b16 v[188:189], v232 offset:36864\n"
"v_mfma_f32_16x16x32_bf16 v[100:103], v[176:179], v[212:215], v[100:103]\n"
"ds_read_b64_tr_b16 v[190:191], v232 offset:37376\n"
"global_load_lds_dwordx4 v237, s[88:89] offset:2048\n"
"v_mfma_f32_16x16x32_bf16 v[104:107], v[176:179], v[216:219], v[104:107]\n"
"v_mfma_f32_16x16x32_bf16 v[108:111], v[176:179], v[220:223], v[108:111]\n"
"global_load_lds_dwordx4 v238, s[88:89] offset:3072\n"
"s_waitcnt lgkmcnt(4)\n"
"v_mfma_f32_16x16x32_bf16 v[112:115], v[180:183], v[208:211], v[112:115]\n"
"s_add_u32 m0, s82, 0x13000\n"
"v_mfma_f32_16x16x32_bf16 v[116:119], v[180:183], v[212:215], v[116:119]\n"
"global_load_lds_dwordx4 v239, s[88:89]\n"
"s_add_u32 s88, s88, 0x48000\n"
"s_addc_u32 s89, s89, 0\n"
"v_mfma_f32_16x16x32_bf16 v[120:123], v[180:183], v[216:219], v[120:123]\n"
"v_mfma_f32_16x16x32_bf16 v[124:127], v[180:183], v[220:223], v[124:127]\n"
"s_waitcnt vmcnt(10) lgkmcnt(0)\n"
"s_barrier\n"
"ds_read_b128 v[192:195], v234 offset:4096\n"
"ds_read_b64_tr_b16 v[176:177], v224 offset:0\n"
"v_mfma_f32_16x16x32_bf16 v[128:131], v[184:187], v[208:211], v[128:131]\n"
"ds_read_b64_tr_b16 v[178:179], v224 offset:512\n"
"s_add_u32 m0, s82, 0x1b000\n"
"v_mfma_f32_16x16x32_bf16 v[132:135], v[184:187], v[212:215], v[132:135]\n"
"ds_read_b128 v[196:199], v234 offset:5120\n"
"global_load_lds_dwordx4 v235, s[90:91]\n"
"v_mfma_f32_16x16x32_bf16 v[136:139], v[184:187], v[216:219], v[136:139]\n"
"ds_read_b128 v[200:203], v234 offset:6144\n"
"ds_read_b128 v[204:207], v234 offset:7168\n"
"v_mfma_f32_16x16x32_bf16 v[140:143], v[184:187], v[220:223], v[140:143]\n"
"ds_read_b64_tr_b16 v[180:181], v225 offset:0\n"
"v_mfma_f32_16x16x32_bf16 v[144:147], v[188:191], v[208:211], v[144:147]\n"
"ds_read_b64_tr_b16 v[182:183], v225 offset:512\n"
"global_load_lds_dwordx4 v236, s[90:91] offset:1024\n"
"v_mfma_f32_16x16x32_bf16 v[148:151], v[188:191], v[212:215], v[148:151]\n"
"v_mfma_f32_16x16x32_bf16 v[152:155], v[188:191], v[216:219], v[152:155]\n"
"ds_read_b64_tr_b16 v[184:185], v226 offset:0\n"
"ds_read_b64_tr_b16 v[186:187], v226 offset:512\n"
"v_mfma_f32_16x16x32_bf16 v[156:159], v[188:191], v[220:223], v[156:159]\n"
"s_waitcnt lgkmcnt(7)\n"
"v_mfma_f32_16x16x32_bf16 v[16:19], v[176:179], v[192:195], v[16:19]\n"
"ds_read_b64_tr_b16 v[188:189], v227 offset:0\n"
"s_waitcnt lgkmcnt(7)\n"
"v_mfma_f32_16x16x32_bf16 v[20:23], v[176:179], v[196:199], v[20:23]\n"
"ds_read_b64_tr_b16 v[190:191], v227 offset:512\n"
"global_load_lds_dwordx4 v237, s[90:91] offset:2048\n"
"s_waitcnt lgkmcnt(7)\n"
"v_mfma_f32_16x16x32_bf16 v[24:27], v[176:179], v[200:203], v[24:27]\n"
"s_waitcnt lgkmcnt(6)\n"
"v_mfma_f32_16x16x32_bf16 v[28:31], v[176:179], v[204:207], v[28:31]\n"
"global_load_lds_dwordx4 v238, s[90:91] offset:3072\n"
"s_waitcnt lgkmcnt(4)\n"
"v_mfma_f32_16x16x32_bf16 v[32:35], v[180:183], v[192:195], v[32:35]\n"
"s_add_u32 m0, s82, 0x1c000\n"
"v_mfma_f32_16x16x32_bf16 v[36:39], v[180:183], v[196:199], v[36:39]\n"
"global_load_lds_dwordx4 v239, s[90:91]\n"
"s_add_u32 s90, s90, 0x48000\n"
"s_addc_u32 s91, s91, 0\n"
"v_mfma_f32_16x16x32_bf16 v[40:43], v[180:183], v[200:203], v[40:43]\n"
"v_mfma_f32_16x16x32_bf16 v[44:47], v[180:183], v[204:207], v[44:47]\n"
"s_waitcnt vmcnt(10) lgkmcnt(0)\n"
"s_barrier\n"
"ds_read_b64_tr_b16 v[176:177], v224 offset:36864\n"
"v_mfma_f32_16x16x32_bf16 v[48:51], v[184:187], v[192:195], v[48:51]\n"
"ds_read_b64_tr_b16 v[178:179], v224 offset:37376\n"
"s_mov_b32 m0, s82\n"
"v_mfma_f32_16x16x32_bf16 v[52:55], v[184:187], v[196:199], v[52:55]\n"
"global_load_lds_dwordx4 v235, s[88:89]\n"
"v_mfma_f32_16x16x32_bf16 v[56:59], v[184:187], v[200:203], v[56:59]\n"
"ds_read_b64_tr_b16 v[180:181], v225 offset:36864\n"
"v_mfma_f32_16x16x32_bf16 v[60:63], v[184:187], v[204:207], v[60:63]\n"
"ds_read_b64_tr_b16 v[182:183], v225 offset:37376\n"
"v_mfma_f32_16x16x32_bf16 v[64:67], v[188:191], v[192:195], v[64:67]\n"
"global_load_lds_dwordx4 v236, s[88:89] offset:1024\n"
"v_mfma_f32_16x16x32_bf16 v[68:71], v[188:191], v[196:199], v[68:71]\n"
"v_mfma_f32_16x16x32_bf16 v[72:75], v[188:191], v[200:203], v[72:75]\n"
"ds_read_b64_tr_b16 v[184:185], v226 offset:36864\n"
"ds_read_b64_tr_b16 v[186:187], v226 offset:37376\n"
"v_mfma_f32_16x16x32_bf16 v[76:79], v[188:191], v[204:207], v[76:79]\n"
"s_waitcnt lgkmcnt(4)\n"
"v_mfma_f32_16x16x32_bf16 v[96:99], v[176:179], v[192:195], v[96:99]\n"
"ds_read_b64_tr_b16 v[188:189], v227 offset:36864\n"
"v_mfma_f32_16x16x32_bf16 v[100:103], v[176:179], v[196:199], v[100:103]\n"
"ds_read_b64_tr_b16 v[190:191], v227 offset:37376\n"
"global_load_lds_dwordx4 v237, s[88:89] offset:2048\n"
"v_mfma_f32_16x16x32_bf16 v[104:107], v[176:179], v[200:203], v[104:107]\n"
"v_mfma_f32_16x16x32_bf16 v[108:111], v[176:179], v[204:207], v[108:111]\n"
"global_load_lds_dwordx4 v238, s[88:89] offset:3072\n"
"s_waitcnt lgkmcnt(4)\n"
"v_mfma_f32_16x16x32_bf16 v[112:115], v[180:183], v[192:195], v[112:115]\n"
"s_add_u32 m0, s82, 0x1000\n"
"v_mfma_f32_16x16x32_bf16 v[116:119], v[180:183], v[196:199], v[116:119]\n"
"global_load_lds_dwordx4 v239, s[88:89]\n"
"s_add_u32 s88, s88, 0x48000\n"
"s_addc_u32 s89, s89, 0\n"
"v_mfma_f32_16x16x32_bf16 v[120:123], v[180:183], v[200:203], v[120:123]\n"
"v_mfma_f32_16x16x32_bf16 v[124:127], v[180:183], v[204:207], v[124:127]\n"
"s_waitcnt vmcnt(10) lgkmcnt(0)\n"
"s_barrier\n"
"ds_read_b128 v[208:211], v234 offset:8192\n"
"ds_read_b64_tr_b16 v[176:177], v229 offset:0\n"
"v_mfma_f32_16x16x32_bf16 v[128:131], v[184:187], v[192:195], v[128:131]\n"
"ds_read_b64_tr_b16 v[178:179], v229 offset:512\n"
"s_add_u32 m0, s82, 0x9000\n"
"v_mfma_f32_16x16x32_bf16 v[132:135], v[184:187], v[196:199], v[132:135]\n"
"ds_read_b128 v[212:215], v234 offset:9216\n"
"global_load_lds_dwordx4 v235, s[90:91]\n"
"v_mfma_f32_16x16x32_bf16 v[136:139], v[184:187], v[200:203], v[136:139]\n"
"ds_read_b128 v[216:219], v234 offset:10240\n"
"ds_read_b128 v[220:223], v234 offset:11264\n"
"v_mfma_f32_16x16x32_bf16 v[140:143], v[184:187], v[204:207], v[140:143]\n"
"ds_read_b64_tr_b16 v[180:181], v230 offset:0\n"
"v_mfma_f32_16x16x32_bf16 v[144:147], v[188:191], v[192:195], v[144:147]\n"
"ds_read_b64_tr_b16 v[182:183], v230 offset:512\n"
"global_load_lds_dwordx4 v236, s[90:91] offset:1024\n"
"v_mfma_f32_16x16x32_bf16 v[148:151], v[188:191], v[196:199], v[148:151]\n"
"v_mfma_f32_16x16x32_bf16 v[152:155], v[188:191], v[200:203], v[152:155]\n"
"ds_read_b64_tr_b16 v[184:185], v231 offset:0\n"
"ds_read_b64_tr_b16 v[186:187], v231 offset:512\n"
"v_mfma_f32_16x16x32_bf16 v[156:159], v[188:191], v[204:207], v[156:159]\n"
"s_waitcnt lgkmcnt(7)\n"
"v_mfma_f32_16x16x32_bf16 v[16:19], v[176:179], v[208:211], v[16:19]\n"
"ds_read_b64_tr_b16 v[188:189], v232 offset:0\n"
"s_waitcnt lgkmcnt(7)\n"
"v_mfma_f32_16x16x32_bf16 v[20:23], v[176:179], v[212:215], v[20:23]\n"
"ds_read_b64_tr_b16 v[190:191], v232 offset:512\n"
"global_load_lds_dwordx4 v237, s[90:91] offset:2048\n"
"s_waitcnt lgkmcnt(7)\n"
"v_mfma_f32_16x16x32_bf16 v[24:27], v[176:179], v[216:219], v[24:27]\n"
"s_waitcnt lgkmcnt(6)\n"
"v_mfma_f32_16x16x32_bf16 v[28:31], v[176:179], v[220:223], v[28:31]\n"
"global_load_lds_dwordx4 v238, s[90:91] offset:3072\n"
"s_waitcnt lgkmcnt(4)\n"
"v_mfma_f32_16x16x32_bf16 v[32:35], v[180:183], v[208:211], v[32:35]\n"
"s_add_u32 m0, s82, 0xa000\n"
"v_mfma_f32_16x16x32_bf16 v[36:39], v[180:183], v[212:215], v[36:39]\n"
"global_load_lds_dwordx4 v239, s[90:91]\n"
"s_add_u32 s90, s90, 0x48000\n"
"s_addc_u32 s91, s91, 0\n"
"v_mfma_f32_16x16x32_bf16 v[40:43], v[180:183], v[216:219], v[40:43]\n"
"v_mfma_f32_16x16x32_bf16 v[44:47], v[180:183], v[220:223], v[44:47]\n"
"s_waitcnt vmcnt(10) lgkmcnt(0)\n"
"s_barrier\n"
"ds_read_b64_tr_b16 v[176:177], v229 offset:36864\n"
"v_mfma_f32_16x16x32_bf16 v[48:51], v[184:187], v[208:211], v[48:51]\n"
"ds_read_b64_tr_b16 v[178:179], v229 offset:37376\n"
"s_add_u32 m0, s82, 0x12000\n"
"v_mfma_f32_16x16x32_bf16 v[52:55], v[184:187], v[212:215], v[52:55]\n"
"global_load_lds_dwordx4 v235, s[88:89]\n"
"v_mfma_f32_16x16x32_bf16 v[56:59], v[184:187], v[216:219], v[56:59]\n"
"ds_read_b64_tr_b16 v[180:181], v230 offset:36864\n"
"v_mfma_f32_16x16x32_bf16 v[60:63], v[184:187], v[220:223], v[60:63]\n"
"ds_read_b64_tr_b16 v[182:183], v230 offset:37376\n"
"v_mfma_f32_16x16x32_bf16 v[64:67], v[188:191], v[208:211], v[64:67]\n"
"global_load_lds_dwordx4 v236, s[88:89] offset:1024\n"
"v_mfma_f32_16x16x32_bf16 v[68:71], v[188:191], v[212:215], v[68:71]\n"
"v_mfma_f32_16x16x32_bf16 v[72:75], v[188:191], v[216:219], v[72:75]\n"
"ds_read_b64_tr_b16 v[184:185], v231 offset:36864\n"
"ds_read_b64_tr_b16 v[186:187], v231 offset:37376\n"
"v_mfma_f32_16x16x32_bf16 v[76:79], v[188:191], v[220:223], v[76:79]\n"
"s_waitcnt lgkmcnt(4)\n"
"v_mfma_f32_16x16x32_bf16 v[96:99], v[176:179], v[208:211], v[96:99]\n"
"ds_read_b64_tr_b16 v[188:189], v232 offset:36864\n"
"v_mfma_f32_16x16x32_bf16 v[100:103], v[176:179], v[212:215], v[100:103]\n"
"ds_read_b64_tr_b16 v[190:191], v232 offset:37376\n"
"global_load_lds_dwordx4 v237, s[88:89] offset:2048\n"
"v_mfma_f32_16x16x32_bf16 v[104:107], v[176:179], v[216:219], v[104:107]\n"
"v_mfma_f32_16x16x32_bf16 v[108:111], v[176:179], v[220:223], v[108:111]\n"
"global_load_lds_dwordx4 v238, s[88:89] offset:3072\n"
"s_waitcnt lgkmcnt(4)\n"
"v_mfma_f32_16x16x32_bf16 v[112:115], v[180:183], v[208:211], v[112:115]\n"
"s_add_u32 m0, s82, 0x13000\n"
"v_mfma_f32_16x16x32_bf16 v[116:119], v[180:183], v[212:215], v[116:119]\n"
"global_load_lds_dwordx4 v239, s[88:89]\n"
"s_add_u32 s88, s88, 0x48000\n"
"s_addc_u32 s89, s89, 0\n"
"v_mfma_f32_16x16x32_bf16 v[120:123], v[180:183], v[216:219], v[120:123]\n"
"v_mfma_f32_16x16x32_bf16 v[124:127], v[180:183], v[220:223], v[124:127]\n"
"s_waitcnt vmcnt(10) lgkmcnt(0)\n"
"s_barrier\n"
"ds_read_b128 v[192:195], v234 offset:0\n"
"ds_read_b64_tr_b16 v[176:177], v224 offset:0\n"
"v_mfma_f32_16x16x32_bf16 v[128:131], v[184:187], v[208:211], v[128:131]\n"
"ds_read_b64_tr_b16 v[178:179], v224 offset:512\n"
"s_add_u32 m0, s82, 0x1b000\n"
"v_mfma_f32_16x16x32_bf16 v[132:135], v[184:187], v[212:215], v[132:135]\n"
"ds_read_b128 v[196:199], v234 offset:1024\n"
"global_load_lds_dwordx4 v235, s[90:91]\n"
"v_mfma_f32_16x16x32_bf16 v[136:139], v[184:187], v[216:219], v[136:139]\n"
"ds_read_b128 v[200:203], v234 offset:2048\n"
"ds_read_b128 v[204:207], v234 offset:3072\n"
"v_mfma_f32_16x16x32_bf16 v[140:143], v[184:187], v[220:223], v[140:143]\n"
"ds_read_b64_tr_b16 v[180:181], v225 offset:0\n"
"v_mfma_f32_16x16x32_bf16 v[144:147], v[188:191], v[208:211], v[144:147]\n"
"ds_read_b64_tr_b16 v[182:183], v225 offset:512\n"
"global_load_lds_dwordx4 v236, s[90:91] offset:1024\n"
"v_mfma_f32_16x16x32_bf16 v[148:151], v[188:191], v[212:215], v[148:151]\n"
"v_mfma_f32_16x16x32_bf16 v[152:155], v[188:191], v[216:219], v[152:155]\n"
"ds_read_b64_tr_b16 v[184:185], v226 offset:0\n"
"ds_read_b64_tr_b16 v[186:187], v226 offset:512\n"
"v_mfma_f32_16x16x32_bf16 v[156:159], v[188:191], v[220:223], v[156:159]\n"
"s_waitcnt lgkmcnt(7)\n"
"v_mfma_f32_16x16x32_bf16 v[16:19], v[176:179], v[192:195], v[16:19]\n"
"ds_read_b64_tr_b16 v[188:189], v227 offset:0\n"
"s_waitcnt lgkmcnt(7)\n"
"v_mfma_f32_16x16x32_bf16 v[20:23], v[176:179], v[196:199], v[20:23]\n"
"ds_read_b64_tr_b16 v[190:191], v227 offset:512\n"
"global_load_lds_dwordx4 v237, s[90:91] offset:2048\n"
"s_waitcnt lgkmcnt(7)\n"
"v_mfma_f32_16x16x32_bf16 v[24:27], v[176:179], v[200:203], v[24:27]\n"
"s_waitcnt lgkmcnt(6)\n"
"v_mfma_f32_16x16x32_bf16 v[28:31], v[176:179], v[204:207], v[28:31]\n"
"global_load_lds_dwordx4 v238, s[90:91] offset:3072\n"
"s_waitcnt lgkmcnt(4)\n"
"v_mfma_f32_16x16x32_bf16 v[32:35], v[180:183], v[192:195], v[32:35]\n"
"s_add_u32 m0, s82, 0x1c000\n"
"v_mfma_f32_16x16x32_bf16 v[36:39], v[180:183], v[196:199], v[36:39]\n"
"global_load_lds_dwordx4 v239, s[90:91]\n"
"s_add_u32 s90, s90, 0x48000\n"
"s_addc_u32 s91, s91, 0\n"
"v_mfma_f32_16x16x32_bf16 v[40:43], v[180:183], v[200:203], v[40:43]\n"
"v_mfma_f32_16x16x32_bf16 v[44:47], v[180:183], v[204:207], v[44:47]\n"
"s_waitcnt vmcnt(10) lgkmcnt(0)\n"
"s_barrier\n"
"ds_read_b64_tr_b16 v[176:177], v224 offset:36864\n"
"v_mfma_f32_16x16x32_bf16 v[48:51], v[184:187], v[192:195], v[48:51]\n"
"ds_read_b64_tr_b16 v[178:179], v224 offset:37376\n"
"s_mov_b32 m0, s82\n"
"v_mfma_f32_16x16x32_bf16 v[52:55], v[184:187], v[196:199], v[52:55]\n"
"global_load_lds_dwordx4 v235, s[88:89]\n"
"v_mfma_f32_16x16x32_bf16 v[56:59], v[184:187], v[200:203], v[56:59]\n"
"ds_read_b64_tr_b16 v[180:181], v225 offset:36864\n"
"v_mfma_f32_16x16x32_bf16 v[60:63], v[184:187], v[204:207], v[60:63]\n"
"ds_read_b64_tr_b16 v[182:183], v225 offset:37376\n"
"v_mfma_f32_16x16x32_bf16 v[64:67], v[188:191], v[192:195], v[64:67]\n"
"global_load_lds_dwordx4 v236, s[88:89] offset:1024\n"
"v_mfma_f32_16x16x32_bf16 v[68:71], v[188:191], v[196:199], v[68:71]\n"
"v_mfma_f32_16x16x32_bf16 v[72:75], v[188:191], v[200:203], v[72:75]\n"
"ds_read_b64_tr_b16 v[184:185], v226 offset:36864\n"
"ds_read_b64_tr_b16 v[186:187], v226 offset:37376\n"
"v_mfma_f32_16x16x32_bf16 v[76:79], v[188:191], v[204:207], v[76:79]\n"
"s_waitcnt lgkmcnt(4)\n"
"v_mfma_f32_16x16x32_bf16 v[96:99], v[176:179], v[192:195], v[96:99]\n"
"ds_read_b64_tr_b16 v[188:189], v227 offset:36864\n"
"v_mfma_f32_16x16x32_bf16 v[100:103], v[176:179], v[196:199], v[100:103]\n"
"ds_read_b64_tr_b16 v[190:191], v227 offset:37376\n"
"global_load_lds_dwordx4 v237, s[88:89] offset:2048\n"
"v_mfma_f32_16x16x32_bf16 v[104:107], v[176:179], v[200:203], v[104:107]\n"
"v_mfma_f32_16x16x32_bf16 v[108:111], v[176:179], v[204:207], v[108:111]\n"
"global_load_lds_dwordx4 v238, s[88:89] offset:3072\n"
"s_waitcnt lgkmcnt(4)\n"
"v_mfma_f32_16x16x32_bf16 v[112:115], v[180:183], v[192:195], v[112:115]\n"
"s_add_u32 m0, s82, 0x1000\n"
"v_mfma_f32_16x16x32_bf16 v[116:119], v[180:183], v[196:199], v[116:119]\n"
"global_load_lds_dwordx4 v239, s[88:89]\n"
"s_add_u32 s88, s88, 0x48000\n"
"s_addc_u32 s89, s89, 0\n"
"v_mfma_f32_16x16x32_bf16 v[120:123], v[180:183], v[200:203], v[120:123]\n"
"v_mfma_f32_16x16x32_bf16 v[124:127], v[180:183], v[204:207], v[124:127]\n"
"s_waitcnt vmcnt(10) lgkmcnt(0)\n"
"s_barrier\n"
"ds_read_b128 v[208:211], v234 offset:4096\n"
"ds_read_b64_tr_b16 v[176:177], v229 offset:0\n"
"v_mfma_f32_16x16x32_bf16 v[128:131], v[184:187], v[192:195], v[128:131]\n"
"ds_read_b64_tr_b16 v[178:179], v229 offset:512\n"
"s_add_u32 m0, s82, 0x9000\n"
"v_mfma_f32_16x16x32_bf16 v[132:135], v[184:187], v[196:199], v[132:135]\n"
"ds_read_b128 v[212:215], v234 offset:5120\n"
"global_load_lds_dwordx4 v235, s[90:91]\n"
"v_mfma_f32_16x16x32_bf16 v[136:139], v[184:187], v[200:203], v[136:139]\n"
"ds_read_b128 v[216:219], v234 offset:6144\n"
"ds_read_b128 v[220:223], v234 offset:7168\n"
"v_mfma_f32_16x16x32_bf16 v[140:143], v[184:187], v[204:207], v[140:143]\n"
"ds_read_b64_tr_b16 v[180:181], v230 offset:0\n"
"v_mfma_f32_16x16x32_bf16 v[144:147], v[188:191], v[192:195], v[144:147]\n"
"ds_read_b64_tr_b16 v[182:183], v230 offset:512\n"
"global_load_lds_dwordx4 v236, s[90:91] offset:1024\n"
"v_mfma_f32_16x16x32_bf16 v[148:151], v[188:191], v[196:199], v[148:151]\n"
"v_mfma_f32_16x16x32_bf16 v[152:155], v[188:191], v[200:203], v[152:155]\n"
"ds_read_b64_tr_b16 v[184:185], v231 offset:0\n"
"ds_read_b64_tr_b16 v[186:187], v231 offset:512\n"
"v_mfma_f32_16x16x32_bf16 v[156:159], v[188:191], v[204:207], v[156:159]\n"
"s_waitcnt lgkmcnt(7)\n"
"v_mfma_f32_16x16x32_bf16 v[16:19], v[176:179], v[208:211], v[16:19]\n"
"ds_read_b64_tr_b16 v[188:189], v232 offset:0\n"
"s_waitcnt lgkmcnt(7)\n"
"v_mfma_f32_16x16x32_bf16 v[20:23], v[176:179], v[212:215], v[20:23]\n"
"ds_read_b64_tr_b16 v[190:191], v232 offset:512\n"
"global_load_lds_dwordx4 v237, s[90:91] offset:2048\n"
"s_waitcnt lgkmcnt(7)\n"
"v_mfma_f32_16x16x32_bf16 v[24:27], v[176:179], v[216:219], v[24:27]\n"
"s_waitcnt lgkmcnt(6)\n"
"v_mfma_f32_16x16x32_bf16 v[28:31], v[176:179], v[220:223], v[28:31]\n"
"global_load_lds_dwordx4 v238, s[90:91] offset:3072\n"
"s_waitcnt lgkmcnt(4)\n"
"v_mfma_f32_16x16x32_bf16 v[32:35], v[180:183], v[208:211], v[32:35]\n"
"s_add_u32 m0, s82, 0xa000\n"
"v_mfma_f32_16x16x32_bf16 v[36:39], v[180:183], v[212:215], v[36:39]\n"
"global_load_lds_dwordx4 v239, s[90:91]\n"
"s_add_u32 s90, s90, 0x48000\n"
"s_addc_u32 s91, s91, 0\n"
"v_mfma_f32_16x16x32_bf16 v[40:43], v[180:183], v[216:219], v[40:43]\n"
"v_mfma_f32_16x16x32_bf16 v[44:47], v[180:183], v[220:223], v[44:47]\n"
"s_waitcnt vmcnt(10) lgkmcnt(0)\n"
"s_barrier\n"
"ds_read_b64_tr_b16 v[176:177], v229 offset:36864\n"
"v_mfma_f32_16x16x32_bf16 v[48:51], v[184:187], v[208:211], v[48:51]\n"
"ds_read_b64_tr_b16 v[178:179], v229 offset:37376\n"
"s_add_u32 m0, s82, 0x12000\n"
"v_mfma_f32_16x16x32_bf16 v[52:55], v[184:187], v[212:215], v[52:55]\n"
"global_load_lds_dwordx4 v235, s[88:89]\n"
"v_mfma_f32_16x16x32_bf16 v[56:59], v[184:187], v[216:219], v[56:59]\n"
"ds_read_b64_tr_b16 v[180:181], v230 offset:36864\n"
"v_mfma_f32_16x16x32_bf16 v[60:63], v[184:187], v[220:223], v[60:63]\n"
"ds_read_b64_tr_b16 v[182:183], v230 offset:37376\n"
"v_mfma_f32_16x16x32_bf16 v[64:67], v[188:191], v[208:211], v[64:67]\n"
"global_load_lds_dwordx4 v236, s[88:89] offset:1024\n"
"v_mfma_f32_16x16x32_bf16 v[68:71], v[188:191], v[212:215], v[68:71]\n"
"v_mfma_f32_16x16x32_bf16 v[72:75], v[188:191], v[216:219], v[72:75]\n"
"ds_read_b64_tr_b16 v[184:185], v231 offset:36864\n"
"ds_read_b64_tr_b16 v[186:187], v231 offset:37376\n"
"v_mfma_f32_16x16x32_bf16 v[76:79], v[188:191], v[220:223], v[76:79]\n"
"s_waitcnt lgkmcnt(4)\n"
"v_mfma_f32_16x16x32_bf16 v[96:99], v[176:179], v[208:211], v[96:99]\n"
"ds_read_b64_tr_b16 v[188:189], v232 offset:36864\n"
"v_mfma_f32_16x16x32_bf16 v[100:103], v[176:179], v[212:215], v[100:103]\n"
"ds_read_b64_tr_b16 v[190:191], v232 offset:37376\n"
"global_load_lds_dwordx4 v237, s[88:89] offset:2048\n"
"v_mfma_f32_16x16x32_bf16 v[104:107], v[176:179], v[216:219], v[104:107]\n"
"v_mfma_f32_16x16x32_bf16 v[108:111], v[176:179], v[220:223], v[108:111]\n"
"global_load_lds_dwordx4 v238, s[88:89] offset:3072\n"
"s_waitcnt lgkmcnt(4)\n"
"v_mfma_f32_16x16x32_bf16 v[112:115], v[180:183], v[208:211], v[112:115]\n"
"s_add_u32 m0, s82, 0x13000\n"
"v_mfma_f32_16x16x32_bf16 v[116:119], v[180:183], v[212:215], v[116:119]\n"
"global_load_lds_dwordx4 v239, s[88:89]\n"
"s_add_u32 s88, s88, 0x48000\n"
"s_addc_u32 s89, s89, 0\n"
"v_mfma_f32_16x16x32_bf16 v[120:123], v[180:183], v[216:219], v[120:123]\n"
"v_mfma_f32_16x16x32_bf16 v[124:127], v[180:183], v[220:223], v[124:127]\n"
"s_waitcnt vmcnt(10) lgkmcnt(0)\n"
"s_barrier\n"
"ds_read_b128 v[192:195], v234 offset:8192\n"
"ds_read_b64_tr_b16 v[176:177], v224 offset:0\n"
"v_mfma_f32_16x16x32_bf16 v[128:131], v[184:187], v[208:211], v[128:131]\n"
"ds_read_b64_tr_b16 v[178:179], v224 offset:512\n"
"s_add_u32 m0, s82, 0x1b000\n"
"v_mfma_f32_16x16x32_bf16 v[132:135], v[184:187], v[212:215], v[132:135]\n"
"ds_read_b128 v[196:199], v234 offset:9216\n"
"global_load_lds_dwordx4 v235, s[90:91]\n"
"v_mfma_f32_16x16x32_bf16 v[136:139], v[184:187], v[216:219], v[136:139]\n"
"ds_read_b128 v[200:203], v234 offset:10240\n"
"ds_read_b128 v[204:207], v234 offset:11264\n"
"v_mfma_f32_16x16x32_bf16 v[140:143], v[184:187], v[220:223], v[140:143]\n"
"ds_read_b64_tr_b16 v[180:181], v225 offset:0\n"
"v_mfma_f32_16x16x32_bf16 v[144:147], v[188:191], v[208:211], v[144:147]\n"
"ds_read_b64_tr_b16 v[182:183], v225 offset:512\n"
"global_load_lds_dwordx4 v236, s[90:91] offset:1024\n"
"v_mfma_f32_16x16x32_bf16 v[148:151], v[188:191], v[212:215], v[148:151]\n"
"v_mfma_f32_16x16x32_bf16 v[152:155], v[188:191], v[216:219], v[152:155]\n"
"ds_read_b64_tr_b16 v[184:185], v226 offset:0\n"
"ds_read_b64_tr_b16 v[186:187], v226 offset:512\n"
"v_mfma_f32_16x16x32_bf16 v[156:159], v[188:191], v[220:223], v[156:159]\n"
"s_waitcnt lgkmcnt(7)\n"
"v_mfma_f32_16x16x32_bf16 v[16:19], v[176:179], v[192:195], v[16:19]\n"
"ds_read_b64_tr_b16 v[188:189], v227 offset:0\n"
"s_waitcnt lgkmcnt(7)\n"
"v_mfma_f32_16x16x32_bf16 v[20:23], v[176:179], v[196:199], v[20:23]\n"
"ds_read_b64_tr_b16 v[190:191], v227 offset:512\n"
"global_load_lds_dwordx4 v237, s[90:91] offset:2048\n"
"s_waitcnt lgkmcnt(7)\n"
"v_mfma_f32_16x16x32_bf16 v[24:27], v[176:179], v[200:203], v[24:27]\n"
"s_waitcnt lgkmcnt(6)\n"
"v_mfma_f32_16x16x32_bf16 v[28:31], v[176:179], v[204:207], v[28:31]\n"
"global_load_lds_dwordx4 v238, s[90:91] offset:3072\n"
"s_waitcnt lgkmcnt(4)\n"
"v_mfma_f32_16x16x32_bf16 v[32:35], v[180:183], v[192:195], v[32:35]\n"
"s_add_u32 m0, s82, 0x1c000\n"
"v_mfma_f32_16x16x32_bf16 v[36:39], v[180:183], v[196:199], v[36:39]\n"
"global_load_lds_dwordx4 v239, s[90:91]\n"
"s_add_u32 s90, s90, 0x48000\n"
"s_addc_u32 s91, s91, 0\n"
"v_mfma_f32_16x16x32_bf16 v[40:43], v[180:183], v[200:203], v[40:43]\n"
"v_mfma_f32_16x16x32_bf16 v[44:47], v[180:183], v[204:207], v[44:47]\n"
"s_waitcnt vmcnt(10) lgkmcnt(0)\n"
"s_barrier\n"
"ds_read_b64_tr_b16 v[176:177], v224 offset:36864\n"
"v_mfma_f32_16x16x32_bf16 v[48:51], v[184:187], v[192:195], v[48:51]\n"
"ds_read_b64_tr_b16 v[178:179], v224 offset:37376\n"
"s_mov_b32 m0, s82\n"
"v_mfma_f32_16x16x32_bf16 v[52:55], v[184:187], v[196:199], v[52:55]\n"
"global_load_lds_dwordx4 v235, s[88:89]\n"
"v_mfma_f32_16x16x32_bf16 v[56:59], v[184:187], v[200:203], v[56:59]\n"
"ds_read_b64_tr_b16 v[180:181], v225 offset:36864\n"
"v_mfma_f32_16x16x32_bf16 v[60:63], v[184:187], v[204:207], v[60:63]\n"
"ds_read_b64_tr_b16 v[182:183], v225 offset:37376\n"
"v_mfma_f32_16x16x32_bf16 v[64:67], v[188:191], v[192:195], v[64:67]\n"
"global_load_lds_dwordx4 v236, s[88:89] offset:1024\n"
"v_mfma_f32_16x16x32_bf16 v[68:71], v[188:191], v[196:199], v[68:71]\n"
"v_mfma_f32_16x16x32_bf16 v[72:75], v[188:191], v[200:203], v[72:75]\n"
"ds_read_b64_tr_b16 v[184:185], v226 offset:36864\n"
"ds_read_b64_tr_b16 v[186:187], v226 offset:37376\n"
"v_mfma_f32_16x16x32_bf16 v[76:79], v[188:191], v[204:207], v[76:79]\n"
"s_waitcnt lgkmcnt(4)\n"
"v_mfma_f32_16x16x32_bf16 v[96:99], v[176:179], v[192:195], v[96:99]\n"
"ds_read_b64_tr_b16 v[188:189], v227 offset:36864\n"
"v_mfma_f32_16x16x32_bf16 v[100:103], v[176:179], v[196:199], v[100:103]\n"
"ds_read_b64_tr_b16 v[190:191], v227 offset:37376\n"
"global_load_lds_dwordx4 v237, s[88:89] offset:2048\n"
"v_mfma_f32_16x16x32_bf16 v[104:107], v[176:179], v[200:203], v[104:107]\n"
"v_mfma_f32_16x16x32_bf16 v[108:111], v[176:179], v[204:207], v[108:111]\n"
"global_load_lds_dwordx4 v238, s[88:89] offset:3072\n"
"s_waitcnt lgkmcnt(4)\n"
"v_mfma_f32_16x16x32_bf16 v[112:115], v[180:183], v[192:195], v[112:115]\n"
"s_add_u32 m0, s82, 0x1000\n"
"v_mfma_f32_16x16x32_bf16 v[116:119], v[180:183], v[196:199], v[116:119]\n"
"global_load_lds_dwordx4 v239, s[88:89]\n"
"s_add_u32 s88, s88, 0x48000\n"
"s_addc_u32 s89, s89, 0\n"
"v_mfma_f32_16x16x32_bf16 v[120:123], v[180:183], v[200:203], v[120:123]\n"
"v_mfma_f32_16x16x32_bf16 v[124:127], v[180:183], v[204:207], v[124:127]\n"
"s_waitcnt vmcnt(10) lgkmcnt(0)\n"
"s_barrier\n"
"ds_read_b128 v[208:211], v234 offset:0\n"
"ds_read_b64_tr_b16 v[176:177], v229 offset:0\n"
"v_mfma_f32_16x16x32_bf16 v[128:131], v[184:187], v[192:195], v[128:131]\n"
"ds_read_b64_tr_b16 v[178:179], v229 offset:512\n"
"s_add_u32 m0, s82, 0x9000\n"
"v_mfma_f32_16x16x32_bf16 v[132:135], v[184:187], v[196:199], v[132:135]\n"
"ds_read_b128 v[212:215], v234 offset:1024\n"
"global_load_lds_dwordx4 v235, s[90:91]\n"
"v_mfma_f32_16x16x32_bf16 v[136:139], v[184:187], v[200:203], v[136:139]\n"
"ds_read_b128 v[216:219], v234 offset:2048\n"
"ds_read_b128 v[220:223], v234 offset:3072\n"
"v_mfma_f32_16x16x32_bf16 v[140:143], v[184:187], v[204:207], v[140:143]\n"
"ds_read_b64_tr_b16 v[180:181], v230 offset:0\n"
"v_mfma_f32_16x16x32_bf16 v[144:147], v[188:191], v[192:195], v[144:147]\n"
"ds_read_b64_tr_b16 v[182:183], v230 offset:512\n"
"global_load_lds_dwordx4 v236, s[90:91] offset:1024\n"
"v_mfma_f32_16x16x32_bf16 v[148:151], v[188:191], v[196:199], v[148:151]\n"
"v_mfma_f32_16x16x32_bf16 v[152:155], v[188:191], v[200:203], v[152:155]\n"
"ds_read_b64_tr_b16 v[184:185], v231 offset:0\n"
"ds_read_b64_tr_b16 v[186:187], v231 offset:512\n"
"v_mfma_f32_16x16x32_bf16 v[156:159], v[188:191], v[204:207], v[156:159]\n"
"s_waitcnt lgkmcnt(7)\n"
"v_mfma_f32_16x16x32_bf16 v[16:19], v[176:179], v[208:211], v[16:19]\n"
"ds_read_b64_tr_b16 v[188:189], v232 offset:0\n"
"s_waitcnt lgkmcnt(7)\n"
"v_mfma_f32_16x16x32_bf16 v[20:23], v[176:179], v[212:215], v[20:23]\n"
"ds_read_b64_tr_b16 v[190:191], v232 offset:512\n"
"global_load_lds_dwordx4 v237, s[90:91] offset:2048\n"
"s_waitcnt lgkmcnt(7)\n"
"v_mfma_f32_16x16x32_bf16 v[24:27], v[176:179], v[216:219], v[24:27]\n"
"s_waitcnt lgkmcnt(6)\n"
"v_mfma_f32_16x16x32_bf16 v[28:31], v[176:179], v[220:223], v[28:31]\n"
"global_load_lds_dwordx4 v238, s[90:91] offset:3072\n"
"s_waitcnt lgkmcnt(4)\n"
"v_mfma_f32_16x16x32_bf16 v[32:35], v[180:183], v[208:211], v[32:35]\n"
"s_add_u32 m0, s82, 0xa000\n"
"v_mfma_f32_16x16x32_bf16 v[36:39], v[180:183], v[212:215], v[36:39]\n"
"global_load_lds_dwordx4 v239, s[90:91]\n"
"s_add_u32 s90, s90, 0x48000\n"
"s_addc_u32 s91, s91, 0\n"
"v_mfma_f32_16x16x32_bf16 v[40:43], v[180:183], v[216:219], v[40:43]\n"
"v_mfma_f32_16x16x32_bf16 v[44:47], v[180:183], v[220:223], v[44:47]\n"
"s_waitcnt vmcnt(10) lgkmcnt(0)\n"
"s_barrier\n"
"ds_read_b64_tr_b16 v[176:177], v229 offset:36864\n"
"v_mfma_f32_16x16x32_bf16 v[48:51], v[184:187], v[208:211], v[48:51]\n"
"ds_read_b64_tr_b16 v[178:179], v229 offset:37376\n"
"s_add_u32 m0, s82, 0x12000\n"
"v_mfma_f32_16x16x32_bf16 v[52:55], v[184:187], v[212:215], v[52:55]\n"
"global_load_lds_dwordx4 v235, s[88:89]\n"
"v_mfma_f32_16x16x32_bf16 v[56:59], v[184:187], v[216:219], v[56:59]\n"
"ds_read_b64_tr_b16 v[180:181], v230 offset:36864\n"
"v_mfma_f32_16x16x32_bf16 v[60:63], v[184:187], v[220:223], v[60:63]\n"
"ds_read_b64_tr_b16 v[182:183], v230 offset:37376\n"
"v_mfma_f32_16x16x32_bf16 v[64:67], v[188:191], v[208:211], v[64:67]\n"
"global_load_lds_dwordx4 v236, s[88:89] offset:1024\n"
"v_mfma_f32_16x16x32_bf16 v[68:71], v[188:191], v[212:215], v[68:71]\n"
"v_mfma_f32_16x16x32_bf16 v[72:75], v[188:191], v[216:219], v[72:75]\n"
"ds_read_b64_tr_b16 v[184:185], v231 offset:36864\n"
"ds_read_b64_tr_b16 v[186:187], v231 offset:37376\n"
"v_mfma_f32_16x16x32_bf16 v[76:79], v[188:191], v[220:223], v[76:79]\n"
"s_waitcnt lgkmcnt(4)\n"
"v_mfma_f32_16x16x32_bf16 v[96:99], v[176:179], v[208:211], v[96:99]\n"
"ds_read_b64_tr_b16 v[188:189], v232 offset:36864\n"
"v_mfma_f32_16x16x32_bf16 v[100:103], v[176:179], v[212:215], v[100:103]\n"
"ds_read_b64_tr_b16 v[190:191], v232 offset:37376\n"
"global_load_lds_dwordx4 v237, s[88:89] offset:2048\n"
"v_mfma_f32_16x16x32_bf16 v[104:107], v[176:179], v[216:219], v[104:107]\n"
"v_mfma_f32_16x16x32_bf16 v[108:111], v[176:179], v[220:223], v[108:111]\n"
"global_load_lds_dwordx4 v238, s[88:89] offset:3072\n"
"s_waitcnt lgkmcnt(4)\n"
"v_mfma_f32_16x16x32_bf16 v[112:115], v[180:183], v[208:211], v[112:115]\n"
"s_add_u32 m0, s82, 0x13000\n"
"v_mfma_f32_16x16x32_bf16 v[116:119], v[180:183], v[212:215], v[116:119]\n"
"global_load_lds_dwordx4 v239, s[88:89]\n"
"s_add_u32 s88, s88, 0x48000\n"
"s_addc_u32 s89, s89, 0\n"
"v_mfma_f32_16x16x32_bf16 v[120:123], v[180:183], v[216:219], v[120:123]\n"
"v_mfma_f32_16x16x32_bf16 v[124:127], v[180:183], v[220:223], v[124:127]\n"
"s_waitcnt vmcnt(10) lgkmcnt(0)\n"
"s_barrier\n"
"ds_read_b128 v[192:195], v234 offset:4096\n"
"ds_read_b64_tr_b16 v[176:177], v224 offset:0\n"
"v_mfma_f32_16x16x32_bf16 v[128:131], v[184:187], v[208:211], v[128:131]\n"
"ds_read_b64_tr_b16 v[178:179], v224 offset:512\n"
"s_add_u32 m0, s82, 0x1b000\n"
"v_mfma_f32_16x16x32_bf16 v[132:135], v[184:187], v[212:215], v[132:135]\n"
"ds_read_b128 v[196:199], v234 offset:5120\n"
"global_load_lds_dwordx4 v235, s[90:91]\n"
"v_mfma_f32_16x16x32_bf16 v[136:139], v[184:187], v[216:219], v[136:139]\n"
"ds_read_b128 v[200:203], v234 offset:6144\n"
"ds_read_b128 v[204:207], v234 offset:7168\n"
"v_mfma_f32_16x16x32_bf16 v[140:143], v[184:187], v[220:223], v[140:143]\n"
"ds_read_b64_tr_b16 v[180:181], v225 offset:0\n"
"v_mfma_f32_16x16x32_bf16 v[144:147], v[188:191], v[208:211], v[144:147]\n"
"ds_read_b64_tr_b16 v[182:183], v225 offset:512\n"
"global_load_lds_dwordx4 v236, s[90:91] offset:1024\n"
"v_mfma_f32_16x16x32_bf16 v[148:151], v[188:191], v[212:215], v[148:151]\n"
"v_mfma_f32_16x16x32_bf16 v[152:155], v[188:191], v[216:219], v[152:155]\n"
"ds_read_b64_tr_b16 v[184:185], v226 offset:0\n"
"ds_read_b64_tr_b16 v[186:187], v226 offset:512\n"
"v_mfma_f32_16x16x32_bf16 v[156:159], v[188:191], v[220:223], v[156:159]\n"
"s_waitcnt lgkmcnt(7)\n"
"v_mfma_f32_16x16x32_bf16 v[16:19], v[176:179], v[192:195], v[16:19]\n"
"ds_read_b64_tr_b16 v[188:189], v227 offset:0\n"
"s_waitcnt lgkmcnt(7)\n"
"v_mfma_f32_16x16x32_bf16 v[20:23], v[176:179], v[196:199], v[20:23]\n"
"ds_read_b64_tr_b16 v[190:191], v227 offset:512\n"
"global_load_lds_dwordx4 v237, s[90:91] offset:2048\n"
"s_waitcnt lgkmcnt(7)\n"
"v_mfma_f32_16x16x32_bf16 v[24:27], v[176:179], v[200:203], v[24:27]\n"
"s_waitcnt lgkmcnt(6)\n"
"v_mfma_f32_16x16x32_bf16 v[28:31], v[176:179], v[204:207], v[28:31]\n"
"global_load_lds_dwordx4 v238, s[90:91] offset:3072\n"
"s_waitcnt lgkmcnt(4)\n"
"v_mfma_f32_16x16x32_bf16 v[32:35], v[180:183], v[192:195], v[32:35]\n"
"s_add_u32 m0, s82, 0x1c000\n"
"v_mfma_f32_16x16x32_bf16 v[36:39], v[180:183], v[196:199], v[36:39]\n"
"global_load_lds_dwordx4 v239, s[90:91]\n"
"s_add_u32 s90, s90, 0x48000\n"
"s_addc_u32 s91, s91, 0\n"
"v_mfma_f32_16x16x32_bf16 v[40:43], v[180:183], v[200:203], v[40:43]\n"
"v_mfma_f32_16x16x32_bf16 v[44:47], v[180:183], v[204:207], v[44:47]\n"
"s_waitcnt vmcnt(10) lgkmcnt(0)\n"
"s_barrier\n"
"ds_read_b64_tr_b16 v[176:177], v224 offset:36864\n"
"v_mfma_f32_16x16x32_bf16 v[48:51], v[184:187], v[192:195], v[48:51]\n"
"ds_read_b64_tr_b16 v[178:179], v224 offset:37376\n"
"s_mov_b32 m0, s82\n"
"v_mfma_f32_16x16x32_bf16 v[52:55], v[184:187], v[196:199], v[52:55]\n"
"global_load_lds_dwordx4 v235, s[88:89]\n"
"v_mfma_f32_16x16x32_bf16 v[56:59], v[184:187], v[200:203], v[56:59]\n"
"ds_read_b64_tr_b16 v[180:181], v225 offset:36864\n"
"v_mfma_f32_16x16x32_bf16 v[60:63], v[184:187], v[204:207], v[60:63]\n"
"ds_read_b64_tr_b16 v[182:183], v225 offset:37376\n"
"v_mfma_f32_16x16x32_bf16 v[64:67], v[188:191], v[192:195], v[64:67]\n"
"global_load_lds_dwordx4 v236, s[88:89] offset:1024\n"
"v_mfma_f32_16x16x32_bf16 v[68:71], v[188:191], v[196:199], v[68:71]\n"
"v_mfma_f32_16x16x32_bf16 v[72:75], v[188:191], v[200:203], v[72:75]\n"
"ds_read_b64_tr_b16 v[184:185], v226 offset:36864\n"
"ds_read_b64_tr_b16 v[186:187], v226 offset:37376\n"
"v_mfma_f32_16x16x32_bf16 v[76:79], v[188:191], v[204:207], v[76:79]\n"
"s_waitcnt lgkmcnt(4)\n"
"v_mfma_f32_16x16x32_bf16 v[96:99], v[176:179], v[192:195], v[96:99]\n"
"ds_read_b64_tr_b16 v[188:189], v227 offset:36864\n"
"v_mfma_f32_16x16x32_bf16 v[100:103], v[176:179], v[196:199], v[100:103]\n"
"ds_read_b64_tr_b16 v[190:191], v227 offset:37376\n"
"global_load_lds_dwordx4 v237, s[88:89] offset:2048\n"
"v_mfma_f32_16x16x32_bf16 v[104:107], v[176:179], v[200:203], v[104:107]\n"
"v_mfma_f32_16x16x32_bf16 v[108:111], v[176:179], v[204:207], v[108:111]\n"
"global_load_lds_dwordx4 v238, s[88:89] offset:3072\n"
"s_waitcnt lgkmcnt(4)\n"
"v_mfma_f32_16x16x32_bf16 v[112:115], v[180:183], v[192:195], v[112:115]\n"
"s_add_u32 m0, s82, 0x1000\n"
"v_mfma_f32_16x16x32_bf16 v[116:119], v[180:183], v[196:199], v[116:119]\n"
"global_load_lds_dwordx4 v239, s[88:89]\n"
"s_add_u32 s88, s88, 0x48000\n"
"s_addc_u32 s89, s89, 0\n"
"v_mfma_f32_16x16x32_bf16 v[120:123], v[180:183], v[200:203], v[120:123]\n"
"v_mfma_f32_16x16x32_bf16 v[124:127], v[180:183], v[204:207], v[124:127]\n"
"s_waitcnt vmcnt(10) lgkmcnt(0)\n"
"s_barrier\n"
"ds_read_b128 v[208:211], v234 offset:8192\n"
"ds_read_b64_tr_b16 v[176:177], v229 offset:0\n"
"v_mfma_f32_16x16x32_bf16 v[128:131], v[184:187], v[192:195], v[128:131]\n"
"ds_read_b64_tr_b16 v[178:179], v229 offset:512\n"
"s_add_u32 m0, s82, 0x9000\n"
"v_mfma_f32_16x16x32_bf16 v[132:135], v[184:187], v[196:199], v[132:135]\n"
"ds_read_b128 v[212:215], v234 offset:9216\n"
"global_load_lds_dwordx4 v235, s[90:91]\n"
"v_mfma_f32_16x16x32_bf16 v[136:139], v[184:187], v[200:203], v[136:139]\n"
"ds_read_b128 v[216:219], v234 offset:10240\n"
"ds_read_b128 v[220:223], v234 offset:11264\n"
"v_mfma_f32_16x16x32_bf16 v[140:143], v[184:187], v[204:207], v[140:143]\n"
"ds_read_b64_tr_b16 v[180:181], v230 offset:0\n"
"v_mfma_f32_16x16x32_bf16 v[144:147], v[188:191], v[192:195], v[144:147]\n"
"ds_read_b64_tr_b16 v[182:183], v230 offset:512\n"
"global_load_lds_dwordx4 v236, s[90:91] offset:1024\n"
"v_mfma_f32_16x16x32_bf16 v[148:151], v[188:191], v[196:199], v[148:151]\n"
"v_mfma_f32_16x16x32_bf16 v[152:155], v[188:191], v[200:203], v[152:155]\n"
"ds_read_b64_tr_b16 v[184:185], v231 offset:0\n"
"ds_read_b64_tr_b16 v[186:187], v231 offset:512\n"
"v_mfma_f32_16x16x32_bf16 v[156:159], v[188:191], v[204:207], v[156:159]\n"
"s_waitcnt lgkmcnt(7)\n"
"v_mfma_f32_16x16x32_bf16 v[16:19], v[176:179], v[208:211], v[16:19]\n"
"ds_read_b64_tr_b16 v[188:189], v232 offset:0\n"
"s_waitcnt lgkmcnt(7)\n"
"v_mfma_f32_16x16x32_bf16 v[20:23], v[176:179], v[212:215], v[20:23]\n"
"ds_read_b64_tr_b16 v[190:191], v232 offset:512\n"
"global_load_lds_dwordx4 v237, s[90:91] offset:2048\n"
"s_waitcnt lgkmcnt(7)\n"
"v_mfma_f32_16x16x32_bf16 v[24:27], v[176:179], v[216:219], v[24:27]\n"
"s_waitcnt lgkmcnt(6)\n"
"v_mfma_f32_16x16x32_bf16 v[28:31], v[176:179], v[220:223], v[28:31]\n"
"global_load_lds_dwordx4 v238, s[90:91] offset:3072\n"
"s_waitcnt lgkmcnt(4)\n"
"v_mfma_f32_16x16x32_bf16 v[32:35], v[180:183], v[208:211], v[32:35]\n"
"s_add_u32 m0, s82, 0xa000\n"
"v_mfma_f32_16x16x32_bf16 v[36:39], v[180:183], v[212:215], v[36:39]\n"
"global_load_lds_dwordx4 v239, s[90:91]\n"
"s_add_u32 s90, s90, 0x48000\n"
"s_addc_u32 s91, s91, 0\n"
"v_mfma_f32_16x16x32_bf16 v[40:43], v[180:183], v[216:219], v[40:43]\n"
"v_mfma_f32_16x16x32_bf16 v[44:47], v[180:183], v[220:223], v[44:47]\n"
"s_waitcnt vmcnt(10) lgkmcnt(0)\n"
"s_barrier\n"
"ds_read_b64_tr_b16 v[176:177], v229 offset:36864\n"
"v_mfma_f32_16x16x32_bf16 v[48:51], v[184:187], v[208:211], v[48:51]\n"
"ds_read_b64_tr_b16 v[178:179], v229 offset:37376\n"
"s_add_u32 m0, s82, 0x12000\n"
"v_mfma_f32_16x16x32_bf16 v[52:55], v[184:187], v[212:215], v[52:55]\n"
"global_load_lds_dwordx4 v235, s[88:89]\n"
"v_mfma_f32_16x16x32_bf16 v[56:59], v[184:187], v[216:219], v[56:59]\n"
"ds_read_b64_tr_b16 v[180:181], v230 offset:36864\n"
"v_mfma_f32_16x16x32_bf16 v[60:63], v[184:187], v[220:223], v[60:63]\n"
"ds_read_b64_tr_b16 v[182:183], v230 offset:37376\n"
"v_mfma_f32_16x16x32_bf16 v[64:67], v[188:191], v[208:211], v[64:67]\n"
"global_load_lds_dwordx4 v236, s[88:89] offset:1024\n"
"v_mfma_f32_16x16x32_bf16 v[68:71], v[188:191], v[212:215], v[68:71]\n"
"v_mfma_f32_16x16x32_bf16 v[72:75], v[188:191], v[216:219], v[72:75]\n"
"ds_read_b64_tr_b16 v[184:185], v231 offset:36864\n"
"ds_read_b64_tr_b16 v[186:187], v231 offset:37376\n"
"v_mfma_f32_16x16x32_bf16 v[76:79], v[188:191], v[220:223], v[76:79]\n"
"s_waitcnt lgkmcnt(4)\n"
"v_mfma_f32_16x16x32_bf16 v[96:99], v[176:179], v[208:211], v[96:99]\n"
"ds_read_b64_tr_b16 v[188:189], v232 offset:36864\n"
"v_mfma_f32_16x16x32_bf16 v[100:103], v[176:179], v[212:215], v[100:103]\n"
"ds_read_b64_tr_b16 v[190:191], v232 offset:37376\n"
"global_load_lds_dwordx4 v237, s[88:89] offset:2048\n"
"v_mfma_f32_16x16x32_bf16 v[104:107], v[176:179], v[216:219], v[104:107]\n"
"v_mfma_f32_16x16x32_bf16 v[108:111], v[176:179], v[220:223], v[108:111]\n"
"global_load_lds_dwordx4 v238, s[88:89] offset:3072\n"
"s_waitcnt lgkmcnt(4)\n"
"v_mfma_f32_16x16x32_bf16 v[112:115], v[180:183], v[208:211], v[112:115]\n"
"s_add_u32 m0, s82, 0x13000\n"
"v_mfma_f32_16x16x32_bf16 v[116:119], v[180:183], v[212:215], v[116:119]\n"
"global_load_lds_dwordx4 v239, s[88:89]\n"
"s_add_u32 s88, s88, 0x48000\n"
"s_addc_u32 s89, s89, 0\n"
"v_mfma_f32_16x16x32_bf16 v[120:123], v[180:183], v[216:219], v[120:123]\n"
"v_mfma_f32_16x16x32_bf16 v[124:127], v[180:183], v[220:223], v[124:127]\n"
"s_waitcnt vmcnt(10) lgkmcnt(0)\n"
"s_barrier\n"
"ds_read_b128 v[192:195], v234 offset:0\n"
"ds_read_b64_tr_b16 v[176:177], v224 offset:0\n"
"v_mfma_f32_16x16x32_bf16 v[128:131], v[184:187], v[208:211], v[128:131]\n"
"ds_read_b64_tr_b16 v[178:179], v224 offset:512\n"
"s_add_u32 m0, s82, 0x1b000\n"
"v_mfma_f32_16x16x32_bf16 v[132:135], v[184:187], v[212:215], v[132:135]\n"
"ds_read_b128 v[196:199], v234 offset:1024\n"
"global_load_lds_dwordx4 v235, s[90:91]\n"
"v_mfma_f32_16x16x32_bf16 v[136:139], v[184:187], v[216:219], v[136:139]\n"
"ds_read_b128 v[200:203], v234 offset:2048\n"
"ds_read_b128 v[204:207], v234 offset:3072\n"
"v_mfma_f32_16x16x32_bf16 v[140:143], v[184:187], v[220:223], v[140:143]\n"
"ds_read_b64_tr_b16 v[180:181], v225 offset:0\n"
"v_mfma_f32_16x16x32_bf16 v[144:147], v[188:191], v[208:211], v[144:147]\n"
"ds_read_b64_tr_b16 v[182:183], v225 offset:512\n"
"global_load_lds_dwordx4 v236, s[90:91] offset:1024\n"
"v_mfma_f32_16x16x32_bf16 v[148:151], v[188:191], v[212:215], v[148:151]\n"
"v_mfma_f32_16x16x32_bf16 v[152:155], v[188:191], v[216:219], v[152:155]\n"
"ds_read_b64_tr_b16 v[184:185], v226 offset:0\n"
"ds_read_b64_tr_b16 v[186:187], v226 offset:512\n"
"v_mfma_f32_16x16x32_bf16 v[156:159], v[188:191], v[220:223], v[156:159]\n"
"s_waitcnt lgkmcnt(7)\n"
"v_mfma_f32_16x16x32_bf16 v[16:19], v[176:179], v[192:195], v[16:19]\n"
"ds_read_b64_tr_b16 v[188:189], v227 offset:0\n"
"s_waitcnt lgkmcnt(7)\n"
"v_mfma_f32_16x16x32_bf16 v[20:23], v[176:179], v[196:199], v[20:23]\n"
"ds_read_b64_tr_b16 v[190:191], v227 offset:512\n"
"global_load_lds_dwordx4 v237, s[90:91] offset:2048\n"
"s_waitcnt lgkmcnt(7)\n"
"v_mfma_f32_16x16x32_bf16 v[24:27], v[176:179], v[200:203], v[24:27]\n"
"s_waitcnt lgkmcnt(6)\n"
"v_mfma_f32_16x16x32_bf16 v[28:31], v[176:179], v[204:207], v[28:31]\n"
"global_load_lds_dwordx4 v238, s[90:91] offset:3072\n"
"s_waitcnt lgkmcnt(4)\n"
"v_mfma_f32_16x16x32_bf16 v[32:35], v[180:183], v[192:195], v[32:35]\n"
"s_add_u32 m0, s82, 0x1c000\n"
"v_mfma_f32_16x16x32_bf16 v[36:39], v[180:183], v[196:199], v[36:39]\n"
"global_load_lds_dwordx4 v239, s[90:91]\n"
"s_add_u32 s90, s90, 0x48000\n"
"s_addc_u32 s91, s91, 0\n"
"v_mfma_f32_16x16x32_bf16 v[40:43], v[180:183], v[200:203], v[40:43]\n"
"v_mfma_f32_16x16x32_bf16 v[44:47], v[180:183], v[204:207], v[44:47]\n"
"s_waitcnt vmcnt(10) lgkmcnt(0)\n"
"s_barrier\n"
"ds_read_b64_tr_b16 v[176:177], v224 offset:36864\n"
"v_mfma_f32_16x16x32_bf16 v[48:51], v[184:187], v[192:195], v[48:51]\n"
"ds_read_b64_tr_b16 v[178:179], v224 offset:37376\n"
"s_mov_b32 m0, s82\n"
"v_mfma_f32_16x16x32_bf16 v[52:55], v[184:187], v[196:199], v[52:55]\n"
"global_load_lds_dwordx4 v235, s[88:89]\n"
"v_mfma_f32_16x16x32_bf16 v[56:59], v[184:187], v[200:203], v[56:59]\n"
"ds_read_b64_tr_b16 v[180:181], v225 offset:36864\n"
"v_mfma_f32_16x16x32_bf16 v[60:63], v[184:187], v[204:207], v[60:63]\n"
"ds_read_b64_tr_b16 v[182:183], v225 offset:37376\n"
"v_mfma_f32_16x16x32_bf16 v[64:67], v[188:191], v[192:195], v[64:67]\n"
"global_load_lds_dwordx4 v236, s[88:89] offset:1024\n"
"v_mfma_f32_16x16x32_bf16 v[68:71], v[188:191], v[196:199], v[68:71]\n"
"v_mfma_f32_16x16x32_bf16 v[72:75], v[188:191], v[200:203], v[72:75]\n"
"ds_read_b64_tr_b16 v[184:185], v226 offset:36864\n"
"ds_read_b64_tr_b16 v[186:187], v226 offset:37376\n"
"v_mfma_f32_16x16x32_bf16 v[76:79], v[188:191], v[204:207], v[76:79]\n"
"s_waitcnt lgkmcnt(4)\n"
"v_mfma_f32_16x16x32_bf16 v[96:99], v[176:179], v[192:195], v[96:99]\n"
"ds_read_b64_tr_b16 v[188:189], v227 offset:36864\n"
"v_mfma_f32_16x16x32_bf16 v[100:103], v[176:179], v[196:199], v[100:103]\n"
"ds_read_b64_tr_b16 v[190:191], v227 offset:37376\n"
"global_load_lds_dwordx4 v237, s[88:89] offset:2048\n"
"v_mfma_f32_16x16x32_bf16 v[104:107], v[176:179], v[200:203], v[104:107]\n"
"v_mfma_f32_16x16x32_bf16 v[108:111], v[176:179], v[204:207], v[108:111]\n"
"global_load_lds_dwordx4 v238, s[88:89] offset:3072\n"
"s_waitcnt lgkmcnt(4)\n"
"v_mfma_f32_16x16x32_bf16 v[112:115], v[180:183], v[192:195], v[112:115]\n"
"s_add_u32 m0, s82, 0x1000\n"
"v_mfma_f32_16x16x32_bf16 v[116:119], v[180:183], v[196:199], v[116:119]\n"
"global_load_lds_dwordx4 v239, s[88:89]\n"
"s_add_u32 s88, s88, 0x48000\n"
"s_addc_u32 s89, s89, 0\n"
"v_mfma_f32_16x16x32_bf16 v[120:123], v[180:183], v[200:203], v[120:123]\n"
"v_mfma_f32_16x16x32_bf16 v[124:127], v[180:183], v[204:207], v[124:127]\n"
"s_waitcnt vmcnt(10) lgkmcnt(0)\n"
"s_barrier\n"
"ds_read_b128 v[208:211], v234 offset:4096\n"
"ds_read_b64_tr_b16 v[176:177], v229 offset:0\n"
"v_mfma_f32_16x16x32_bf16 v[128:131], v[184:187], v[192:195], v[128:131]\n"
"ds_read_b64_tr_b16 v[178:179], v229 offset:512\n"
"s_add_u32 m0, s82, 0x9000\n"
"v_mfma_f32_16x16x32_bf16 v[132:135], v[184:187], v[196:199], v[132:135]\n"
"ds_read_b128 v[212:215], v234 offset:5120\n"
"global_load_lds_dwordx4 v235, s[90:91]\n"
"v_mfma_f32_16x16x32_bf16 v[136:139], v[184:187], v[200:203], v[136:139]\n"
"ds_read_b128 v[216:219], v234 offset:6144\n"
"ds_read_b128 v[220:223], v234 offset:7168\n"
"v_mfma_f32_16x16x32_bf16 v[140:143], v[184:187], v[204:207], v[140:143]\n"
"ds_read_b64_tr_b16 v[180:181], v230 offset:0\n"
"v_mfma_f32_16x16x32_bf16 v[144:147], v[188:191], v[192:195], v[144:147]\n"
"ds_read_b64_tr_b16 v[182:183], v230 offset:512\n"
"global_load_lds_dwordx4 v236, s[90:91] offset:1024\n"
"v_mfma_f32_16x16x32_bf16 v[148:151], v[188:191], v[196:199], v[148:151]\n"
"v_mfma_f32_16x16x32_bf16 v[152:155], v[188:191], v[200:203], v[152:155]\n"
"ds_read_b64_tr_b16 v[184:185], v231 offset:0\n"
"ds_read_b64_tr_b16 v[186:187], v231 offset:512\n"
"v_mfma_f32_16x16x32_bf16 v[156:159], v[188:191], v[204:207], v[156:159]\n"
"s_waitcnt lgkmcnt(7)\n"
"v_mfma_f32_16x16x32_bf16 v[16:19], v[176:179], v[208:211], v[16:19]\n"
"ds_read_b64_tr_b16 v[188:189], v232 offset:0\n"
"s_waitcnt lgkmcnt(7)\n"
"v_mfma_f32_16x16x32_bf16 v[20:23], v[176:179], v[212:215], v[20:23]\n"
"ds_read_b64_tr_b16 v[190:191], v232 offset:512\n"
"global_load_lds_dwordx4 v237, s[90:91] offset:2048\n"
"s_waitcnt lgkmcnt(7)\n"
"v_mfma_f32_16x16x32_bf16 v[24:27], v[176:179], v[216:219], v[24:27]\n"
"s_waitcnt lgkmcnt(6)\n"
"v_mfma_f32_16x16x32_bf16 v[28:31], v[176:179], v[220:223], v[28:31]\n"
"global_load_lds_dwordx4 v238, s[90:91] offset:3072\n"
"s_waitcnt lgkmcnt(4)\n"
"v_mfma_f32_16x16x32_bf16 v[32:35], v[180:183], v[208:211], v[32:35]\n"
"s_add_u32 m0, s82, 0xa000\n"
"v_mfma_f32_16x16x32_bf16 v[36:39], v[180:183], v[212:215], v[36:39]\n"
"global_load_lds_dwordx4 v239, s[90:91]\n"
"s_add_u32 s90, s90, 0x48000\n"
"s_addc_u32 s91, s91, 0\n"
"v_mfma_f32_16x16x32_bf16 v[40:43], v[180:183], v[216:219], v[40:43]\n"
"v_mfma_f32_16x16x32_bf16 v[44:47], v[180:183], v[220:223], v[44:47]\n"
"s_waitcnt vmcnt(10) lgkmcnt(0)\n"
"s_barrier\n"
"ds_read_b64_tr_b16 v[176:177], v229 offset:36864\n"
"v_mfma_f32_16x16x32_bf16 v[48:51], v[184:187], v[208:211], v[48:51]\n"
"ds_read_b64_tr_b16 v[178:179], v229 offset:37376\n"
"s_add_u32 m0, s82, 0x12000\n"
"v_mfma_f32_16x16x32_bf16 v[52:55], v[184:187], v[212:215], v[52:55]\n"
"global_load_lds_dwordx4 v235, s[88:89]\n"
"v_mfma_f32_16x16x32_bf16 v[56:59], v[184:187], v[216:219], v[56:59]\n"
"ds_read_b64_tr_b16 v[180:181], v230 offset:36864\n"
"v_mfma_f32_16x16x32_bf16 v[60:63], v[184:187], v[220:223], v[60:63]\n"
"ds_read_b64_tr_b16 v[182:183], v230 offset:37376\n"
"v_mfma_f32_16x16x32_bf16 v[64:67], v[188:191], v[208:211], v[64:67]\n"
"global_load_lds_dwordx4 v236, s[88:89] offset:1024\n"
"v_mfma_f32_16x16x32_bf16 v[68:71], v[188:191], v[212:215], v[68:71]\n"
"v_mfma_f32_16x16x32_bf16 v[72:75], v[188:191], v[216:219], v[72:75]\n"
"ds_read_b64_tr_b16 v[184:185], v231 offset:36864\n"
"ds_read_b64_tr_b16 v[186:187], v231 offset:37376\n"
"v_mfma_f32_16x16x32_bf16 v[76:79], v[188:191], v[220:223], v[76:79]\n"
"s_waitcnt lgkmcnt(4)\n"
"v_mfma_f32_16x16x32_bf16 v[96:99], v[176:179], v[208:211], v[96:99]\n"
"ds_read_b64_tr_b16 v[188:189], v232 offset:36864\n"
"v_mfma_f32_16x16x32_bf16 v[100:103], v[176:179], v[212:215], v[100:103]\n"
"ds_read_b64_tr_b16 v[190:191], v232 offset:37376\n"
"global_load_lds_dwordx4 v237, s[88:89] offset:2048\n"
"v_mfma_f32_16x16x32_bf16 v[104:107], v[176:179], v[216:219], v[104:107]\n"
"v_mfma_f32_16x16x32_bf16 v[108:111], v[176:179], v[220:223], v[108:111]\n"
"global_load_lds_dwordx4 v238, s[88:89] offset:3072\n"
"s_waitcnt lgkmcnt(4)\n"
"v_mfma_f32_16x16x32_bf16 v[112:115], v[180:183], v[208:211], v[112:115]\n"
"s_add_u32 m0, s82, 0x13000\n"
"v_mfma_f32_16x16x32_bf16 v[116:119], v[180:183], v[212:215], v[116:119]\n"
"global_load_lds_dwordx4 v239, s[88:89]\n"
"s_add_u32 s88, s88, 0x48000\n"
"s_addc_u32 s89, s89, 0\n"
"v_mfma_f32_16x16x32_bf16 v[120:123], v[180:183], v[216:219], v[120:123]\n"
"v_mfma_f32_16x16x32_bf16 v[124:127], v[180:183], v[220:223], v[124:127]\n"
"s_waitcnt vmcnt(10) lgkmcnt(0)\n"
"s_barrier\n"
"ds_read_b128 v[192:195], v234 offset:8192\n"
"ds_read_b64_tr_b16 v[176:177], v224 offset:0\n"
"v_mfma_f32_16x16x32_bf16 v[128:131], v[184:187], v[208:211], v[128:131]\n"
"ds_read_b64_tr_b16 v[178:179], v224 offset:512\n"
"s_add_u32 m0, s82, 0x1b000\n"
"v_mfma_f32_16x16x32_bf16 v[132:135], v[184:187], v[212:215], v[132:135]\n"
"ds_read_b128 v[196:199], v234 offset:9216\n"
"global_load_lds_dwordx4 v235, s[90:91]\n"
"v_mfma_f32_16x16x32_bf16 v[136:139], v[184:187], v[216:219], v[136:139]\n"
"ds_read_b128 v[200:203], v234 offset:10240\n"
"ds_read_b128 v[204:207], v234 offset:11264\n"
"v_mfma_f32_16x16x32_bf16 v[140:143], v[184:187], v[220:223], v[140:143]\n"
"ds_read_b64_tr_b16 v[180:181], v225 offset:0\n"
"v_mfma_f32_16x16x32_bf16 v[144:147], v[188:191], v[208:211], v[144:147]\n"
"ds_read_b64_tr_b16 v[182:183], v225 offset:512\n"
"global_load_lds_dwordx4 v236, s[90:91] offset:1024\n"
"v_mfma_f32_16x16x32_bf16 v[148:151], v[188:191], v[212:215], v[148:151]\n"
"v_mfma_f32_16x16x32_bf16 v[152:155], v[188:191], v[216:219], v[152:155]\n"
"ds_read_b64_tr_b16 v[184:185], v226 offset:0\n"
"ds_read_b64_tr_b16 v[186:187], v226 offset:512\n"
"v_mfma_f32_16x16x32_bf16 v[156:159], v[188:191], v[220:223], v[156:159]\n"
"s_waitcnt lgkmcnt(7)\n"
"v_mfma_f32_16x16x32_bf16 v[16:19], v[176:179], v[192:195], v[16:19]\n"
"ds_read_b64_tr_b16 v[188:189], v227 offset:0\n"
"s_waitcnt lgkmcnt(7)\n"
"v_mfma_f32_16x16x32_bf16 v[20:23], v[176:179], v[196:199], v[20:23]\n"
"ds_read_b64_tr_b16 v[190:191], v227 offset:512\n"
"global_load_lds_dwordx4 v237, s[90:91] offset:2048\n"
"s_waitcnt lgkmcnt(7)\n"
"v_mfma_f32_16x16x32_bf16 v[24:27], v[176:179], v[200:203], v[24:27]\n"
"s_waitcnt lgkmcnt(6)\n"
"v_mfma_f32_16x16x32_bf16 v[28:31], v[176:179], v[204:207], v[28:31]\n"
"global_load_lds_dwordx4 v238, s[90:91] offset:3072\n"
"s_waitcnt lgkmcnt(4)\n"
"v_mfma_f32_16x16x32_bf16 v[32:35], v[180:183], v[192:195], v[32:35]\n"
"s_add_u32 m0, s82, 0x1c000\n"
"v_mfma_f32_16x16x32_bf16 v[36:39], v[180:183], v[196:199], v[36:39]\n"
"global_load_lds_dwordx4 v239, s[90:91]\n"
"s_add_u32 s90, s90, 0x48000\n"
"s_addc_u32 s91, s91, 0\n"
"v_mfma_f32_16x16x32_bf16 v[40:43], v[180:183], v[200:203], v[40:43]\n"
"v_mfma_f32_16x16x32_bf16 v[44:47], v[180:183], v[204:207], v[44:47]\n"
"s_waitcnt vmcnt(10) lgkmcnt(0)\n"
"s_barrier\n"
"ds_read_b64_tr_b16 v[176:177], v224 offset:36864\n"
"v_mfma_f32_16x16x32_bf16 v[48:51], v[184:187], v[192:195], v[48:51]\n"
"ds_read_b64_tr_b16 v[178:179], v224 offset:37376\n"
"s_mov_b32 m0, s82\n"
"v_mfma_f32_16x16x32_bf16 v[52:55], v[184:187], v[196:199], v[52:55]\n"
"global_load_lds_dwordx4 v235, s[88:89]\n"
"v_mfma_f32_16x16x32_bf16 v[56:59], v[184:187], v[200:203], v[56:59]\n"
"ds_read_b64_tr_b16 v[180:181], v225 offset:36864\n"
"v_mfma_f32_16x16x32_bf16 v[60:63], v[184:187], v[204:207], v[60:63]\n"
"ds_read_b64_tr_b16 v[182:183], v225 offset:37376\n"
"v_mfma_f32_16x16x32_bf16 v[64:67], v[188:191], v[192:195], v[64:67]\n"
"global_load_lds_dwordx4 v236, s[88:89] offset:1024\n"
"v_mfma_f32_16x16x32_bf16 v[68:71], v[188:191], v[196:199], v[68:71]\n"
"v_mfma_f32_16x16x32_bf16 v[72:75], v[188:191], v[200:203], v[72:75]\n"
"ds_read_b64_tr_b16 v[184:185], v226 offset:36864\n"
"ds_read_b64_tr_b16 v[186:187], v226 offset:37376\n"
"v_mfma_f32_16x16x32_bf16 v[76:79], v[188:191], v[204:207], v[76:79]\n"
"s_waitcnt lgkmcnt(4)\n"
"v_mfma_f32_16x16x32_bf16 v[96:99], v[176:179], v[192:195], v[96:99]\n"
"ds_read_b64_tr_b16 v[188:189], v227 offset:36864\n"
"v_mfma_f32_16x16x32_bf16 v[100:103], v[176:179], v[196:199], v[100:103]\n"
"ds_read_b64_tr_b16 v[190:191], v227 offset:37376\n"
"global_load_lds_dwordx4 v237, s[88:89] offset:2048\n"
"v_mfma_f32_16x16x32_bf16 v[104:107], v[176:179], v[200:203], v[104:107]\n"
"v_mfma_f32_16x16x32_bf16 v[108:111], v[176:179], v[204:207], v[108:111]\n"
"global_load_lds_dwordx4 v238, s[88:89] offset:3072\n"
"s_waitcnt lgkmcnt(4)\n"
"v_mfma_f32_16x16x32_bf16 v[112:115], v[180:183], v[192:195], v[112:115]\n"
"s_add_u32 m0, s82, 0x1000\n"
"v_mfma_f32_16x16x32_bf16 v[116:119], v[180:183], v[196:199], v[116:119]\n"
"global_load_lds_dwordx4 v239, s[88:89]\n"
"s_add_u32 s88, s88, 0x48000\n"
"s_addc_u32 s89, s89, 0\n"
"v_mfma_f32_16x16x32_bf16 v[120:123], v[180:183], v[200:203], v[120:123]\n"
"v_mfma_f32_16x16x32_bf16 v[124:127], v[180:183], v[204:207], v[124:127]\n"
"s_waitcnt vmcnt(10) lgkmcnt(0)\n"
"s_barrier\n"
"ds_read_b128 v[208:211], v234 offset:0\n"
"ds_read_b64_tr_b16 v[176:177], v229 offset:0\n"
"v_mfma_f32_16x16x32_bf16 v[128:131], v[184:187], v[192:195], v[128:131]\n"
"ds_read_b64_tr_b16 v[178:179], v229 offset:512\n"
"s_add_u32 m0, s82, 0x9000\n"
"v_mfma_f32_16x16x32_bf16 v[132:135], v[184:187], v[196:199], v[132:135]\n"
"ds_read_b128 v[212:215], v234 offset:1024\n"
"global_load_lds_dwordx4 v235, s[90:91]\n"
"v_mfma_f32_16x16x32_bf16 v[136:139], v[184:187], v[200:203], v[136:139]\n"
"ds_read_b128 v[216:219], v234 offset:2048\n"
"ds_read_b128 v[220:223], v234 offset:3072\n"
"v_mfma_f32_16x16x32_bf16 v[140:143], v[184:187], v[204:207], v[140:143]\n"
"ds_read_b64_tr_b16 v[180:181], v230 offset:0\n"
"v_mfma_f32_16x16x32_bf16 v[144:147], v[188:191], v[192:195], v[144:147]\n"
"ds_read_b64_tr_b16 v[182:183], v230 offset:512\n"
"global_load_lds_dwordx4 v236, s[90:91] offset:1024\n"
"v_mfma_f32_16x16x32_bf16 v[148:151], v[188:191], v[196:199], v[148:151]\n"
"v_mfma_f32_16x16x32_bf16 v[152:155], v[188:191], v[200:203], v[152:155]\n"
"ds_read_b64_tr_b16 v[184:185], v231 offset:0\n"
"ds_read_b64_tr_b16 v[186:187], v231 offset:512\n"
"v_mfma_f32_16x16x32_bf16 v[156:159], v[188:191], v[204:207], v[156:159]\n"
"s_waitcnt lgkmcnt(7)\n"
"v_mfma_f32_16x16x32_bf16 v[16:19], v[176:179], v[208:211], v[16:19]\n"
"ds_read_b64_tr_b16 v[188:189], v232 offset:0\n"
"s_waitcnt lgkmcnt(7)\n"
"v_mfma_f32_16x16x32_bf16 v[20:23], v[176:179], v[212:215], v[20:23]\n"
"ds_read_b64_tr_b16 v[190:191], v232 offset:512\n"
"global_load_lds_dwordx4 v237, s[90:91] offset:2048\n"
"s_waitcnt lgkmcnt(7)\n"
"v_mfma_f32_16x16x32_bf16 v[24:27], v[176:179], v[216:219], v[24:27]\n"
"s_waitcnt lgkmcnt(6)\n"
"v_mfma_f32_16x16x32_bf16 v[28:31], v[176:179], v[220:223], v[28:31]\n"
"global_load_lds_dwordx4 v238, s[90:91] offset:3072\n"
"s_waitcnt lgkmcnt(4)\n"
"v_mfma_f32_16x16x32_bf16 v[32:35], v[180:183], v[208:211], v[32:35]\n"
"s_add_u32 m0, s82, 0xa000\n"
"v_mfma_f32_16x16x32_bf16 v[36:39], v[180:183], v[212:215], v[36:39]\n"
"global_load_lds_dwordx4 v239, s[90:91]\n"
"s_add_u32 s90, s90, 0x48000\n"
"s_addc_u32 s91, s91, 0\n"
"v_mfma_f32_16x16x32_bf16 v[40:43], v[180:183], v[216:219], v[40:43]\n"
"v_mfma_f32_16x16x32_bf16 v[44:47], v[180:183], v[220:223], v[44:47]\n"
"s_waitcnt vmcnt(10) lgkmcnt(0)\n"
"s_barrier\n"
"ds_read_b64_tr_b16 v[176:177], v229 offset:36864\n"
"v_mfma_f32_16x16x32_bf16 v[48:51], v[184:187], v[208:211], v[48:51]\n"
"ds_read_b64_tr_b16 v[178:179], v229 offset:37376\n"
"s_add_u32 m0, s82, 0x12000\n"
"v_mfma_f32_16x16x32_bf16 v[52:55], v[184:187], v[212:215], v[52:55]\n"
"global_load_lds_dwordx4 v235, s[88:89]\n"
"v_mfma_f32_16x16x32_bf16 v[56:59], v[184:187], v[216:219], v[56:59]\n"
"ds_read_b64_tr_b16 v[180:181], v230 offset:36864\n"
"v_mfma_f32_16x16x32_bf16 v[60:63], v[184:187], v[220:223], v[60:63]\n"
"ds_read_b64_tr_b16 v[182:183], v230 offset:37376\n"
"v_mfma_f32_16x16x32_bf16 v[64:67], v[188:191], v[208:211], v[64:67]\n"
"global_load_lds_dwordx4 v236, s[88:89] offset:1024\n"
"v_mfma_f32_16x16x32_bf16 v[68:71], v[188:191], v[212:215], v[68:71]\n"
"v_mfma_f32_16x16x32_bf16 v[72:75], v[188:191], v[216:219], v[72:75]\n"
"ds_read_b64_tr_b16 v[184:185], v231 offset:36864\n"
"ds_read_b64_tr_b16 v[186:187], v231 offset:37376\n"
"v_mfma_f32_16x16x32_bf16 v[76:79], v[188:191], v[220:223], v[76:79]\n"
"s_waitcnt lgkmcnt(4)\n"
"v_mfma_f32_16x16x32_bf16 v[96:99], v[176:179], v[208:211], v[96:99]\n"
"ds_read_b64_tr_b16 v[188:189], v232 offset:36864\n"
"v_mfma_f32_16x16x32_bf16 v[100:103], v[176:179], v[212:215], v[100:103]\n"
"ds_read_b64_tr_b16 v[190:191], v232 offset:37376\n"
"global_load_lds_dwordx4 v237, s[88:89] offset:2048\n"
"v_mfma_f32_16x16x32_bf16 v[104:107], v[176:179], v[216:219], v[104:107]\n"
"v_mfma_f32_16x16x32_bf16 v[108:111], v[176:179], v[220:223], v[108:111]\n"
"global_load_lds_dwordx4 v238, s[88:89] offset:3072\n"
"s_waitcnt lgkmcnt(4)\n"
"v_mfma_f32_16x16x32_bf16 v[112:115], v[180:183], v[208:211], v[112:115]\n"
"s_add_u32 m0, s82, 0x13000\n"
"v_mfma_f32_16x16x32_bf16 v[116:119], v[180:183], v[212:215], v[116:119]\n"
"global_load_lds_dwordx4 v239, s[88:89]\n"
"s_add_u32 s88, s88, 0x48000\n"
"s_addc_u32 s89, s89, 0\n"
"v_mfma_f32_16x16x32_bf16 v[120:123], v[180:183], v[216:219], v[120:123]\n"
"v_mfma_f32_16x16x32_bf16 v[124:127], v[180:183], v[220:223], v[124:127]\n"
"s_waitcnt vmcnt(10) lgkmcnt(0)\n"
"s_barrier\n"
"ds_read_b128 v[192:195], v234 offset:4096\n"
"ds_read_b64_tr_b16 v[176:177], v224 offset:0\n"
"v_mfma_f32_16x16x32_bf16 v[128:131], v[184:187], v[208:211], v[128:131]\n"
"ds_read_b64_tr_b16 v[178:179], v224 offset:512\n"
"s_add_u32 m0, s82, 0x1b000\n"
"v_mfma_f32_16x16x32_bf16 v[132:135], v[184:187], v[212:215], v[132:135]\n"
"ds_read_b128 v[196:199], v234 offset:5120\n"
"global_load_lds_dwordx4 v235, s[90:91]\n"
"v_mfma_f32_16x16x32_bf16 v[136:139], v[184:187], v[216:219], v[136:139]\n"
"ds_read_b128 v[200:203], v234 offset:6144\n"
"ds_read_b128 v[204:207], v234 offset:7168\n"
"v_mfma_f32_16x16x32_bf16 v[140:143], v[184:187], v[220:223], v[140:143]\n"
"ds_read_b64_tr_b16 v[180:181], v225 offset:0\n"
"v_mfma_f32_16x16x32_bf16 v[144:147], v[188:191], v[208:211], v[144:147]\n"
"ds_read_b64_tr_b16 v[182:183], v225 offset:512\n"
"global_load_lds_dwordx4 v236, s[90:91] offset:1024\n"
"v_mfma_f32_16x16x32_bf16 v[148:151], v[188:191], v[212:215], v[148:151]\n"
"v_mfma_f32_16x16x32_bf16 v[152:155], v[188:191], v[216:219], v[152:155]\n"
"ds_read_b64_tr_b16 v[184:185], v226 offset:0\n"
"ds_read_b64_tr_b16 v[186:187], v226 offset:512\n"
"v_mfma_f32_16x16x32_bf16 v[156:159], v[188:191], v[220:223], v[156:159]\n"
"s_waitcnt lgkmcnt(7)\n"
"v_mfma_f32_16x16x32_bf16 v[16:19], v[176:179], v[192:195], v[16:19]\n"
"ds_read_b64_tr_b16 v[188:189], v227 offset:0\n"
"s_waitcnt lgkmcnt(7)\n"
"v_mfma_f32_16x16x32_bf16 v[20:23], v[176:179], v[196:199], v[20:23]\n"
"ds_read_b64_tr_b16 v[190:191], v227 offset:512\n"
"global_load_lds_dwordx4 v237, s[90:91] offset:2048\n"
"s_waitcnt lgkmcnt(7)\n"
"v_mfma_f32_16x16x32_bf16 v[24:27], v[176:179], v[200:203], v[24:27]\n"
"s_waitcnt lgkmcnt(6)\n"
"v_mfma_f32_16x16x32_bf16 v[28:31], v[176:179], v[204:207], v[28:31]\n"
"global_load_lds_dwordx4 v238, s[90:91] offset:3072\n"
"s_waitcnt lgkmcnt(4)\n"
"v_mfma_f32_16x16x32_bf16 v[32:35], v[180:183], v[192:195], v[32:35]\n"
"s_add_u32 m0, s82, 0x1c000\n"
"v_mfma_f32_16x16x32_bf16 v[36:39], v[180:183], v[196:199], v[36:39]\n"
"global_load_lds_dwordx4 v239, s[90:91]\n"
"s_add_u32 s90, s90, 0x48000\n"
"s_addc_u32 s91, s91, 0\n"
"v_mfma_f32_16x16x32_bf16 v[40:43], v[180:183], v[200:203], v[40:43]\n"
"v_mfma_f32_16x16x32_bf16 v[44:47], v[180:183], v[204:207], v[44:47]\n"
"s_waitcnt vmcnt(10) lgkmcnt(0)\n"
"s_barrier\n"
"ds_read_b64_tr_b16 v[176:177], v224 offset:36864\n"
"v_mfma_f32_16x16x32_bf16 v[48:51], v[184:187], v[192:195], v[48:51]\n"
"ds_read_b64_tr_b16 v[178:179], v224 offset:37376\n"
"v_mfma_f32_16x16x32_bf16 v[52:55], v[184:187], v[196:199], v[52:55]\n"
"v_mfma_f32_16x16x32_bf16 v[56:59], v[184:187], v[200:203], v[56:59]\n"
"ds_read_b64_tr_b16 v[180:181], v225 offset:36864\n"
"v_mfma_f32_16x16x32_bf16 v[60:63], v[184:187], v[204:207], v[60:63]\n"
"ds_read_b64_tr_b16 v[182:183], v225 offset:37376\n"
"v_mfma_f32_16x16x32_bf16 v[64:67], v[188:191], v[192:195], v[64:67]\n"
"v_mfma_f32_16x16x32_bf16 v[68:71], v[188:191], v[196:199], v[68:71]\n"
"v_mfma_f32_16x16x32_bf16 v[72:75], v[188:191], v[200:203], v[72:75]\n"
"ds_read_b64_tr_b16 v[184:185], v226 offset:36864\n"
"ds_read_b64_tr_b16 v[186:187], v226 offset:37376\n"
"v_mfma_f32_16x16x32_bf16 v[76:79], v[188:191], v[204:207], v[76:79]\n"
"s_waitcnt lgkmcnt(4)\n"
"v_mfma_f32_16x16x32_bf16 v[96:99], v[176:179], v[192:195], v[96:99]\n"
"ds_read_b64_tr_b16 v[188:189], v227 offset:36864\n"
"v_mfma_f32_16x16x32_bf16 v[100:103], v[176:179], v[196:199], v[100:103]\n"
"ds_read_b64_tr_b16 v[190:191], v227 offset:37376\n"
"v_mfma_f32_16x16x32_bf16 v[104:107], v[176:179], v[200:203], v[104:107]\n"
"v_mfma_f32_16x16x32_bf16 v[108:111], v[176:179], v[204:207], v[108:111]\n"
"s_waitcnt lgkmcnt(4)\n"
"v_mfma_f32_16x16x32_bf16 v[112:115], v[180:183], v[192:195], v[112:115]\n"
"v_mfma_f32_16x16x32_bf16 v[116:119], v[180:183], v[196:199], v[116:119]\n"
"v_mfma_f32_16x16x32_bf16 v[120:123], v[180:183], v[200:203], v[120:123]\n"
"v_mfma_f32_16x16x32_bf16 v[124:127], v[180:183], v[204:207], v[124:127]\n"
"s_waitcnt vmcnt(5) lgkmcnt(0)\n"
"s_barrier\n"
"ds_read_b128 v[208:211], v234 offset:8192\n"
"ds_read_b64_tr_b16 v[176:177], v229 offset:0\n"
"v_mfma_f32_16x16x32_bf16 v[128:131], v[184:187], v[192:195], v[128:131]\n"
"ds_read_b64_tr_b16 v[178:179], v229 offset:512\n"
"s_add_u32 s48, s50, 0\n"
"s_addc_u32 s49, s51, 0\n"
"s_mov_b32 m0, s44\n"
"v_mfma_f32_16x16x32_bf16 v[132:135], v[184:187], v[196:199], v[132:135]\n"
"ds_read_b128 v[212:215], v234 offset:9216\n"
"global_load_lds_dwordx4 v250, s[48:49]\n"
"v_mfma_f32_16x16x32_bf16 v[136:139], v[184:187], v[200:203], v[136:139]\n"
"ds_read_b128 v[216:219], v234 offset:10240\n"
"ds_read_b128 v[220:223], v234 offset:11264\n"
"v_mfma_f32_16x16x32_bf16 v[140:143], v[184:187], v[204:207], v[140:143]\n"
"ds_read_b64_tr_b16 v[180:181], v230 offset:0\n"
"v_mfma_f32_16x16x32_bf16 v[144:147], v[188:191], v[192:195], v[144:147]\n"
"ds_read_b64_tr_b16 v[182:183], v230 offset:512\n"
"global_load_lds_dwordx4 v251, s[48:49] offset:1024\n"
"v_mfma_f32_16x16x32_bf16 v[148:151], v[188:191], v[196:199], v[148:151]\n"
"v_mfma_f32_16x16x32_bf16 v[152:155], v[188:191], v[200:203], v[152:155]\n"
"ds_read_b64_tr_b16 v[184:185], v231 offset:0\n"
"ds_read_b64_tr_b16 v[186:187], v231 offset:512\n"
"v_mfma_f32_16x16x32_bf16 v[156:159], v[188:191], v[204:207], v[156:159]\n"
"s_waitcnt lgkmcnt(7)\n"
"v_mfma_f32_16x16x32_bf16 v[16:19], v[176:179], v[208:211], v[16:19]\n"
"ds_read_b64_tr_b16 v[188:189], v232 offset:0\n"
"s_waitcnt lgkmcnt(7)\n"
"v_mfma_f32_16x16x32_bf16 v[20:23], v[176:179], v[212:215], v[20:23]\n"
"ds_read_b64_tr_b16 v[190:191], v232 offset:512\n"
"global_load_lds_dwordx4 v252, s[48:49] offset:2048\n"
"s_add_u32 m0, s44, 0xc00\n"
"s_waitcnt lgkmcnt(7)\n"
"v_mfma_f32_16x16x32_bf16 v[24:27], v[176:179], v[216:219], v[24:27]\n"
"global_load_lds_dwordx4 v253, s[48:49]\n"
"s_waitcnt lgkmcnt(6)\n"
"v_mfma_f32_16x16x32_bf16 v[28:31], v[176:179], v[220:223], v[28:31]\n"
"s_waitcnt lgkmcnt(4)\n"
"v_mfma_f32_16x16x32_bf16 v[32:35], v[180:183], v[208:211], v[32:35]\n"
"global_load_lds_dwordx4 v254, s[48:49] offset:1024\n"
"v_mfma_f32_16x16x32_bf16 v[36:39], v[180:183], v[212:215], v[36:39]\n"
"global_load_lds_dwordx4 v255, s[48:49] offset:2048\n"
"v_mfma_f32_16x16x32_bf16 v[40:43], v[180:183], v[216:219], v[40:43]\n"
"v_mfma_f32_16x16x32_bf16 v[44:47], v[180:183], v[220:223], v[44:47]\n"
"s_waitcnt vmcnt(6) lgkmcnt(0)\n"
"s_barrier\n"
"ds_read_b64_tr_b16 v[176:177], v229 offset:36864\n"
"v_mfma_f32_16x16x32_bf16 v[48:51], v[184:187], v[208:211], v[48:51]\n"
"ds_read_b64_tr_b16 v[178:179], v229 offset:37376\n"
"s_mov_b32 s46, 0x24000\n"
"s_cmp_ge_u32 s16, 6\n"
"s_cselect_b32 s46, 0x1b000, s46\n"
"s_ashr_i32 s47, s46, 31\n"
"s_add_u32 s48, s50, s46\n"
"s_addc_u32 s49, s51, s47\n"
"s_add_u32 m0, s45, 0xc000\n"
"v_mfma_f32_16x16x32_bf16 v[52:55], v[184:187], v[212:215], v[52:55]\n"
"global_load_lds_dwordx4 v250, s[48:49]\n"
"v_mfma_f32_16x16x32_bf16 v[56:59], v[184:187], v[216:219], v[56:59]\n"
"ds_read_b64_tr_b16 v[180:181], v230 offset:36864\n"
"v_mfma_f32_16x16x32_bf16 v[60:63], v[184:187], v[220:223], v[60:63]\n"
"ds_read_b64_tr_b16 v[182:183], v230 offset:37376\n"
"v_mfma_f32_16x16x32_bf16 v[64:67], v[188:191], v[208:211], v[64:67]\n"
"global_load_lds_dwordx4 v251, s[48:49] offset:1024\n"
"v_mfma_f32_16x16x32_bf16 v[68:71], v[188:191], v[212:215], v[68:71]\n"
"v_mfma_f32_16x16x32_bf16 v[72:75], v[188:191], v[216:219], v[72:75]\n"
"ds_read_b64_tr_b16 v[184:185], v231 offset:36864\n"
"ds_read_b64_tr_b16 v[186:187], v231 offset:37376\n"
"v_mfma_f32_16x16x32_bf16 v[76:79], v[188:191], v[220:223], v[76:79]\n"
"s_waitcnt lgkmcnt(4)\n"
"v_mfma_f32_16x16x32_bf16 v[96:99], v[176:179], v[208:211], v[96:99]\n"
"ds_read_b64_tr_b16 v[188:189], v232 offset:36864\n"
"v_max_f32 v16, 0, v16\n"
"v_max_f32 v17, 0, v17\n"
"v_max_f32 v18, 0, v18\n"
"v_max_f32 v19, 0, v19\n"
"v_mfma_f32_16x16x32_bf16 v[100:103], v[176:179], v[212:215], v[100:103]\n"
"ds_read_b64_tr_b16 v[190:191], v232 offset:37376\n"
"global_load_lds_dwordx4 v252, s[48:49] offset:2048\n"
"s_add_u32 m0, s45, 0xcc00\n"
"v_max_f32 v20, 0, v20\n"
"v_max_f32 v21, 0, v21\n"
"v_max_f32 v22, 0, v22\n"
"v_max_f32 v23, 0, v23\n"
"v_mfma_f32_16x16x32_bf16 v[104:107], v[176:179], v[216:219], v[104:107]\n"
"global_load_lds_dwordx4 v253, s[48:49]\n"
"v_max_f32 v24, 0, v24\n"
"v_max_f32 v25, 0, v25\n"
"v_max_f32 v26, 0, v26\n"
"v_max_f32 v27, 0, v27\n"
"v_mfma_f32_16x16x32_bf16 v[108:111], v[176:179], v[220:223], v[108:111]\n"
"v_max_f32 v28, 0, v28\n"
"v_max_f32 v29, 0, v29\n"
"v_max_f32 v30, 0, v30\n"
"v_max_f32 v31, 0, v31\n"
"s_waitcnt lgkmcnt(4)\n"
"v_mfma_f32_16x16x32_bf16 v[112:115], v[180:183], v[208:211], v[112:115]\n"
"global_load_lds_dwordx4 v254, s[48:49] offset:1024\n"
"v_max_f32 v32, 0, v32\n"
"v_max_f32 v33, 0, v33\n"
"v_max_f32 v34, 0, v34\n"
"v_max_f32 v35, 0, v35\n"
"v_mfma_f32_16x16x32_bf16 v[116:119], v[180:183], v[212:215], v[116:119]\n"
"global_load_lds_dwordx4 v255, s[48:49] offset:2048\n"
"v_max_f32 v36, 0, v36\n"
"v_max_f32 v37, 0, v37\n"
"v_max_f32 v38, 0, v38\n"
"v_max_f32 v39, 0, v39\n"
"v_mfma_f32_16x16x32_bf16 v[120:123], v[180:183], v[216:219], v[120:123]\n"
"v_max_f32 v40, 0, v40\n"
"v_max_f32 v41, 0, v41\n"
"v_max_f32 v42, 0, v42\n"
"v_max_f32 v43, 0, v43\n"
"v_mfma_f32_16x16x32_bf16 v[124:127], v[180:183], v[220:223], v[124:127]\n"
"s_waitcnt lgkmcnt(0)\n"
"s_barrier\n"
"v_max_f32 v44, 0, v44\n"
"v_max_f32 v45, 0, v45\n"
"v_max_f32 v46, 0, v46\n"
"v_max_f32 v47, 0, v47\n"
"v_mfma_f32_16x16x32_bf16 v[128:131], v[184:187], v[208:211], v[128:131]\n"
"v_max_f32 v48, 0, v48\n"
"v_max_f32 v49, 0, v49\n"
"v_max_f32 v50, 0, v50\n"
"v_max_f32 v51, 0, v51\n"
"v_mfma_f32_16x16x32_bf16 v[132:135], v[184:187], v[212:215], v[132:135]\n"
"v_max_f32 v52, 0, v52\n"
"v_max_f32 v53, 0, v53\n"
"v_max_f32 v54, 0, v54\n"
"v_max_f32 v55, 0, v55\n"
"v_mfma_f32_16x16x32_bf16 v[136:139], v[184:187], v[216:219], v[136:139]\n"
"v_max_f32 v56, 0, v56\n"
"v_max_f32 v57, 0, v57\n"
"v_max_f32 v58, 0, v58\n"
"v_max_f32 v59, 0, v59\n"
"v_mul_f32 v16, v16, v96\n"
"v_mul_f32 v17, v17, v97\n"
"v_mul_f32 v18, v18, v98\n"
"v_mul_f32 v19, v19, v99\n"
"v_cvt_pk_bf16_f32 v16, v16, v17\n"
"v_cvt_pk_bf16_f32 v17, v18, v19\n"
"v_mfma_f32_16x16x32_bf16 v[140:143], v[184:187], v[220:223], v[140:143]\n"
"v_max_f32 v60, 0, v60\n"
"v_max_f32 v61, 0, v61\n"
"v_max_f32 v62, 0, v62\n"
"v_max_f32 v63, 0, v63\n"
"v_mul_f32 v20, v20, v100\n"
"v_mul_f32 v21, v21, v101\n"
"v_mul_f32 v22, v22, v102\n"
"v_mul_f32 v23, v23, v103\n"
"v_cvt_pk_bf16_f32 v20, v20, v21\n"
"v_cvt_pk_bf16_f32 v21, v22, v23\n"
"v_mfma_f32_16x16x32_bf16 v[144:147], v[188:191], v[208:211], v[144:147]\n"
"v_max_f32 v64, 0, v64\n"
"v_max_f32 v65, 0, v65\n"
"v_max_f32 v66, 0, v66\n"
"v_max_f32 v67, 0, v67\n"
"v_mul_f32 v24, v24, v104\n"
"v_mul_f32 v25, v25, v105\n"
"v_mul_f32 v26, v26, v106\n"
"v_mul_f32 v27, v27, v107\n"
"v_cvt_pk_bf16_f32 v24, v24, v25\n"
"v_cvt_pk_bf16_f32 v25, v26, v27\n"
"v_mfma_f32_16x16x32_bf16 v[148:151], v[188:191], v[212:215], v[148:151]\n"
"v_max_f32 v68, 0, v68\n"
"v_max_f32 v69, 0, v69\n"
"v_max_f32 v70, 0, v70\n"
"v_max_f32 v71, 0, v71\n"
"v_mul_f32 v28, v28, v108\n"
"v_mul_f32 v29, v29, v109\n"
"v_mul_f32 v30, v30, v110\n"
"v_mul_f32 v31, v31, v111\n"
"v_cvt_pk_bf16_f32 v28, v28, v29\n"
"v_cvt_pk_bf16_f32 v29, v30, v31\n"
"v_mfma_f32_16x16x32_bf16 v[152:155], v[188:191], v[216:219], v[152:155]\n"
"v_max_f32 v72, 0, v72\n"
"v_max_f32 v73, 0, v73\n"
"v_max_f32 v74, 0, v74\n"
"v_max_f32 v75, 0, v75\n"
"v_mul_f32 v32, v32, v112\n"
"v_mul_f32 v33, v33, v113\n"
"v_mul_f32 v34, v34, v114\n"
"v_mul_f32 v35, v35, v115\n"
"v_cvt_pk_bf16_f32 v32, v32, v33\n"
"v_cvt_pk_bf16_f32 v33, v34, v35\n"
"v_mfma_f32_16x16x32_bf16 v[156:159], v[188:191], v[220:223], v[156:159]\n"
"v_max_f32 v76, 0, v76\n"
"v_max_f32 v77, 0, v77\n"
"v_max_f32 v78, 0, v78\n"
"v_max_f32 v79, 0, v79\n"
"v_mul_f32 v36, v36, v116\n"
"v_mul_f32 v37, v37, v117\n"
"v_mul_f32 v38, v38, v118\n"
"v_mul_f32 v39, v39, v119\n"
"v_cvt_pk_bf16_f32 v36, v36, v37\n"
"v_cvt_pk_bf16_f32 v37, v38, v39\n"
"v_mul_f32 v40, v40, v120\n"
"v_mul_f32 v41, v41, v121\n"
"v_mul_f32 v42, v42, v122\n"
"v_mul_f32 v43, v43, v123\n"
"v_cvt_pk_bf16_f32 v40, v40, v41\n"
"v_cvt_pk_bf16_f32 v41, v42, v43\n"
"v_mul_f32 v44, v44, v124\n"
"v_mul_f32 v45, v45, v125\n"
"v_mul_f32 v46, v46, v126\n"
"v_mul_f32 v47, v47, v127\n"
"v_cvt_pk_bf16_f32 v44, v44, v45\n"
"v_cvt_pk_bf16_f32 v45, v46, v47\n"
"ds_write_b64 v13, v[16:17] offset:0\n"
"ds_write_b64 v13, v[20:21] offset:1024\n"
"ds_write_b64 v13, v[24:25] offset:2048\n"
"ds_write_b64 v13, v[28:29] offset:3072\n"
"ds_write_b64 v14, v[32:33] offset:0\n"
"ds_write_b64 v14, v[36:37] offset:1024\n"
"ds_write_b64 v14, v[40:41] offset:2048\n"
"ds_write_b64 v14, v[44:45] offset:3072\n"
"v_mul_f32 v48, v48, v128\n"
"v_mul_f32 v49, v49, v129\n"
"v_mul_f32 v50, v50, v130\n"
"v_mul_f32 v51, v51, v131\n"
"v_cvt_pk_bf16_f32 v48, v48, v49\n"
"v_cvt_pk_bf16_f32 v49, v50, v51\n"
"ds_write_b64 v13, v[48:49] offset:4096\n"
"v_mul_f32 v52, v52, v132\n"
"v_mul_f32 v53, v53, v133\n"
"v_mul_f32 v54, v54, v134\n"
"v_mul_f32 v55, v55, v135\n"
"v_cvt_pk_bf16_f32 v52, v52, v53\n"
"v_cvt_pk_bf16_f32 v53, v54, v55\n"
"ds_write_b64 v13, v[52:53] offset:5120\n"
"v_mul_f32 v56, v56, v136\n"
"v_mul_f32 v57, v57, v137\n"
"v_mul_f32 v58, v58, v138\n"
"v_mul_f32 v59, v59, v139\n"
"v_cvt_pk_bf16_f32 v56, v56, v57\n"
"v_cvt_pk_bf16_f32 v57, v58, v59\n"
"ds_write_b64 v13, v[56:57] offset:6144\n"
"v_mul_f32 v60, v60, v140\n"
"v_mul_f32 v61, v61, v141\n"
"v_mul_f32 v62, v62, v142\n"
"v_mul_f32 v63, v63, v143\n"
"v_cvt_pk_bf16_f32 v60, v60, v61\n"
"v_cvt_pk_bf16_f32 v61, v62, v63\n"
"ds_write_b64 v13, v[60:61] offset:7168\n"
"v_mul_f32 v64, v64, v144\n"
"v_mul_f32 v65, v65, v145\n"
"v_mul_f32 v66, v66, v146\n"
"v_mul_f32 v67, v67, v147\n"
"v_cvt_pk_bf16_f32 v64, v64, v65\n"
"v_cvt_pk_bf16_f32 v65, v66, v67\n"
"ds_write_b64 v14, v[64:65] offset:4096\n"
"v_mul_f32 v68, v68, v148\n"
"v_mul_f32 v69, v69, v149\n"
"v_mul_f32 v70, v70, v150\n"
"v_mul_f32 v71, v71, v151\n"
"v_cvt_pk_bf16_f32 v68, v68, v69\n"
"v_cvt_pk_bf16_f32 v69, v70, v71\n"
"ds_write_b64 v14, v[68:69] offset:5120\n"
"v_mul_f32 v72, v72, v152\n"
"v_mul_f32 v73, v73, v153\n"
"v_mul_f32 v74, v74, v154\n"
"v_mul_f32 v75, v75, v155\n"
"v_cvt_pk_bf16_f32 v72, v72, v73\n"
"v_cvt_pk_bf16_f32 v73, v74, v75\n"
"ds_write_b64 v14, v[72:73] offset:6144\n"
"v_mul_f32 v76, v76, v156\n"
"v_mul_f32 v77, v77, v157\n"
"v_mul_f32 v78, v78, v158\n"
"v_mul_f32 v79, v79, v159\n"
"v_cvt_pk_bf16_f32 v76, v76, v77\n"
"v_cvt_pk_bf16_f32 v77, v78, v79\n"
"ds_write_b64 v14, v[76:77] offset:7168\n"
"s_nop 1\n"
"F2_JOIN:\n"
"v_add_u32 v232, 0xc000, v2\n"
"v_add_u32 v233, 0xc000, v3\n"
"v_add_u32 v234, 0xc000, v4\n"
"v_add_u32 v235, 0x18000, v2\n"
"v_add_u32 v236, 0x18000, v3\n"
"v_add_u32 v237, 0x18000, v4\n"
"v_add_u32 v238, 0x9000, v5\n"
"s_waitcnt vmcnt(6) lgkmcnt(0)\n"
"s_barrier\n"
"ds_read_b128 v[88:91], v5 offset:0\n"
"ds_read_b64_tr_b16 v[52:53], v2 offset:0\n"
"ds_read_b64_tr_b16 v[54:55], v2 offset:512\n"
"ds_read_b128 v[92:95], v5 offset:1024\n"
"ds_read_b64_tr_b16 v[56:57], v3 offset:0\n"
"ds_read_b64_tr_b16 v[58:59], v3 offset:512\n"
"ds_read_b64_tr_b16 v[60:61], v4 offset:0\n"
"ds_read_b64_tr_b16 v[62:63], v4 offset:512\n"
"ds_read_b128 v[96:99], v5 offset:4096\n"
"ds_read_b128 v[100:103], v5 offset:5120\n"
"ds_read_b64_tr_b16 v[64:65], v2 offset:12288\n"
"ds_read_b64_tr_b16 v[66:67], v2 offset:12800\n"
"ds_read_b64_tr_b16 v[68:69], v3 offset:12288\n"
"ds_read_b64_tr_b16 v[70:71], v3 offset:12800\n"
"ds_read_b64_tr_b16 v[72:73], v4 offset:12288\n"
"ds_read_b64_tr_b16 v[74:75], v4 offset:12800\n"
"ds_read_b128 v[104:107], v5 offset:8192\n"
"ds_read_b128 v[108:111], v5 offset:9216\n"
"s_waitcnt lgkmcnt(15)\n"
"v_mfma_f32_16x16x32_bf16 v[16:19], v[52:55], v[88:91], 0\n"
"ds_read_b64_tr_b16 v[76:77], v2 offset:24576\n"
"s_waitcnt lgkmcnt(15)\n"
"v_mfma_f32_16x16x32_bf16 v[20:23], v[52:55], v[92:95], 0\n"
"ds_read_b64_tr_b16 v[78:79], v2 offset:25088\n"
"ds_read_b128 v[112:115], v5 offset:12288\n"
"ds_read_b128 v[116:119], v5 offset:13312\n"
"s_waitcnt lgkmcnt(15)\n"
"v_mfma_f32_16x16x32_bf16 v[24:27], v[56:59], v[88:91], 0\n"
"ds_read_b64_tr_b16 v[80:81], v3 offset:24576\n"
"ds_read_b128 v[120:123], v5 offset:16384\n"
"ds_read_b128 v[124:127], v5 offset:17408\n"
"v_mfma_f32_16x16x32_bf16 v[28:31], v[56:59], v[92:95], 0\n"
"ds_read_b64_tr_b16 v[82:83], v3 offset:25088\n"
"ds_read_b128 v[128:131], v5 offset:20480\n"
"ds_read_b128 v[132:135], v5 offset:21504\n"
"s_waitcnt lgkmcnt(15)\n"
"v_mfma_f32_16x16x32_bf16 v[32:35], v[60:63], v[88:91], 0\n"
"ds_read_b64_tr_b16 v[84:85], v4 offset:24576\n"
"ds_read_b128 v[136:139], v5 offset:24576\n"
"ds_read_b128 v[140:143], v5 offset:25600\n"
"v_mfma_f32_16x16x32_bf16 v[36:39], v[60:63], v[92:95], 0\n"
"ds_read_b64_tr_b16 v[86:87], v4 offset:25088\n"
"v_mfma_f32_16x16x32_bf16 v[16:19], v[64:67], v[96:99], v[16:19]\n"
"ds_read_b64_tr_b16 v[52:53], v2 offset:36864\n"
"v_mfma_f32_16x16x32_bf16 v[20:23], v[64:67], v[100:103], v[20:23]\n"
"ds_read_b64_tr_b16 v[54:55], v2 offset:37376\n"
"ds_read_b128 v[144:147], v5 offset:28672\n"
"ds_read_b128 v[148:151], v5 offset:29696\n"
"s_waitcnt lgkmcnt(15)\n"
"v_mfma_f32_16x16x32_bf16 v[24:27], v[68:71], v[96:99], v[24:27]\n"
"ds_read_b64_tr_b16 v[56:57], v3 offset:36864\n"
"ds_read_b128 v[152:155], v5 offset:32768\n"
"ds_read_b128 v[156:159], v5 offset:33792\n"
"v_mfma_f32_16x16x32_bf16 v[28:31], v[68:71], v[100:103], v[28:31]\n"
"ds_read_b64_tr_b16 v[58:59], v3 offset:37376\n"
"ds_read_b128 v[160:163], v238 offset:0\n"
"ds_read_b128 v[164:167], v238 offset:1024\n"
"v_mfma_f32_16x16x32_bf16 v[32:35], v[72:75], v[96:99], v[32:35]\n"
"ds_read_b64_tr_b16 v[60:61], v4 offset:36864\n"
"ds_read_b128 v[168:171], v238 offset:4096\n"
"ds_read_b128 v[172:175], v238 offset:5120\n"
"v_mfma_f32_16x16x32_bf16 v[36:39], v[72:75], v[100:103], v[36:39]\n"
"ds_read_b64_tr_b16 v[62:63], v4 offset:37376\n"
"v_mfma_f32_16x16x32_bf16 v[16:19], v[76:79], v[104:107], v[16:19]\n"
"s_waitcnt vmcnt(0) lgkmcnt(0)\n"
"s_barrier\n"
"ds_read_b64_tr_b16 v[64:65], v232 offset:0\n"
"v_mfma_f32_16x16x32_bf16 v[20:23], v[76:79], v[108:111], v[20:23]\n"
"ds_read_b64_tr_b16 v[66:67], v232 offset:512\n"
"ds_read_b128 v[176:179], v238 offset:8192\n"
"ds_read_b128 v[180:183], v238 offset:9216\n"
"s_add_u32 s48, s50, 0x3f000\n"
"s_addc_u32 s49, s51, 0\n"
"s_mov_b32 m0, s44\n"
"v_mfma_f32_16x16x32_bf16 v[24:27], v[80:83], v[104:107], v[24:27]\n"
"ds_read_b64_tr_b16 v[68:69], v233 offset:0\n"
"ds_read_b128 v[184:187], v238 offset:12288\n"
"ds_read_b128 v[188:191], v238 offset:13312\n"
"global_load_lds_dwordx4 v250, s[48:49]\n"
"global_load_lds_dwordx4 v251, s[48:49] offset:1024\n"
"v_mfma_f32_16x16x32_bf16 v[28:31], v[80:83], v[108:111], v[28:31]\n"
"ds_read_b64_tr_b16 v[70:71], v233 offset:512\n"
"ds_read_b128 v[192:195], v238 offset:16384\n"
"ds_read_b128 v[196:199], v238 offset:17408\n"
"v_mfma_f32_16x16x32_bf16 v[32:35], v[84:87], v[104:107], v[32:35]\n"
"ds_read_b64_tr_b16 v[72:73], v234 offset:0\n"
"ds_read_b128 v[200:203], v238 offset:20480\n"
"ds_read_b128 v[204:207], v238 offset:21504\n"
"global_load_lds_dwordx4 v252, s[48:49] offset:2048\n"
"v_mfma_f32_16x16x32_bf16 v[36:39], v[84:87], v[108:111], v[36:39]\n"
"ds_read_b64_tr_b16 v[74:75], v234 offset:512\n"
"v_mfma_f32_16x16x32_bf16 v[16:19], v[52:55], v[112:115], v[16:19]\n"
"ds_read_b64_tr_b16 v[76:77], v232 offset:12288\n"
"v_mfma_f32_16x16x32_bf16 v[20:23], v[52:55], v[116:119], v[20:23]\n"
"ds_read_b64_tr_b16 v[78:79], v232 offset:12800\n"
"ds_read_b128 v[208:211], v238 offset:24576\n"
"ds_read_b128 v[212:215], v238 offset:25600\n"
"s_add_u32 m0, s44, 0xc00\n"
"v_mfma_f32_16x16x32_bf16 v[24:27], v[56:59], v[112:115], v[24:27]\n"
"ds_read_b64_tr_b16 v[80:81], v233 offset:12288\n"
"ds_read_b128 v[216:219], v238 offset:28672\n"
"ds_read_b128 v[220:223], v238 offset:29696\n"
"global_load_lds_dwordx4 v253, s[48:49]\n"
"global_load_lds_dwordx4 v254, s[48:49] offset:1024\n"
"v_mfma_f32_16x16x32_bf16 v[28:31], v[56:59], v[116:119], v[28:31]\n"
"ds_read_b64_tr_b16 v[82:83], v233 offset:12800\n"
"v_mfma_f32_16x16x32_bf16 v[32:35], v[60:63], v[112:115], v[32:35]\n"
"ds_read_b64_tr_b16 v[84:85], v234 offset:12288\n"
"ds_read_b128 v[224:227], v238 offset:32768\n"
"ds_read_b128 v[228:231], v238 offset:33792\n"
"global_load_lds_dwordx4 v255, s[48:49] offset:2048\n"
"v_mfma_f32_16x16x32_bf16 v[36:39], v[60:63], v[116:119], v[36:39]\n"
"ds_read_b64_tr_b16 v[86:87], v234 offset:12800\n"
"s_waitcnt lgkmcnt(15)\n"
"v_mfma_f32_16x16x32_bf16 v[16:19], v[64:67], v[120:123], v[16:19]\n"
"s_waitcnt lgkmcnt(0)\n"
"s_barrier\n"
"ds_read_b64_tr_b16 v[52:53], v232 offset:24576\n"
"v_mfma_f32_16x16x32_bf16 v[20:23], v[64:67], v[124:127], v[20:23]\n"
"ds_read_b64_tr_b16 v[54:55], v232 offset:25088\n"
"s_add_u32 s48, s50, 0x63000\n"
"s_addc_u32 s49, s51, 0\n"
"s_add_u32 m0, s44, 0x18000\n"
"v_mfma_f32_16x16x32_bf16 v[24:27], v[68:71], v[120:123], v[24:27]\n"
"ds_read_b64_tr_b16 v[56:57], v233 offset:24576\n"
"global_load_lds_dwordx4 v250, s[48:49]\n"
"global_load_lds_dwordx4 v251, s[48:49] offset:1024\n"
"v_mfma_f32_16x16x32_bf16 v[28:31], v[68:71], v[124:127], v[28:31]\n"
"ds_read_b64_tr_b16 v[58:59], v233 offset:25088\n"
"v_mfma_f32_16x16x32_bf16 v[32:35], v[72:75], v[120:123], v[32:35]\n"
"ds_read_b64_tr_b16 v[60:61], v234 offset:24576\n"
"global_load_lds_dwordx4 v252, s[48:49] offset:2048\n"
"v_mfma_f32_16x16x32_bf16 v[36:39], v[72:75], v[124:127], v[36:39]\n"
"ds_read_b64_tr_b16 v[62:63], v234 offset:25088\n"
"v_mfma_f32_16x16x32_bf16 v[16:19], v[76:79], v[128:131], v[16:19]\n"
"s_waitcnt vmcnt(3) lgkmcnt(0)\n"
"s_barrier\n"
"ds_read_b64_tr_b16 v[64:65], v2 offset:0\n"
"v_mfma_f32_16x16x32_bf16 v[20:23], v[76:79], v[132:135], v[20:23]\n"
"ds_read_b64_tr_b16 v[66:67], v2 offset:512\n"
"s_add_u32 m0, s44, 0x18c00\n"
"v_mfma_f32_16x16x32_bf16 v[24:27], v[80:83], v[128:131], v[24:27]\n"
"ds_read_b64_tr_b16 v[68:69], v3 offset:0\n"
"global_load_lds_dwordx4 v253, s[48:49]\n"
"global_load_lds_dwordx4 v254, s[48:49] offset:1024\n"
"v_mfma_f32_16x16x32_bf16 v[28:31], v[80:83], v[132:135], v[28:31]\n"
"ds_read_b64_tr_b16 v[70:71], v3 offset:512\n"
"v_mfma_f32_16x16x32_bf16 v[32:35], v[84:87], v[128:131], v[32:35]\n"
"ds_read_b64_tr_b16 v[72:73], v4 offset:0\n"
"global_load_lds_dwordx4 v255, s[48:49] offset:2048\n"
"v_mfma_f32_16x16x32_bf16 v[36:39], v[84:87], v[132:135], v[36:39]\n"
"ds_read_b64_tr_b16 v[74:75], v4 offset:512\n"
"v_mfma_f32_16x16x32_bf16 v[16:19], v[52:55], v[136:139], v[16:19]\n"
"ds_read_b64_tr_b16 v[76:77], v2 offset:12288\n"
"v_mfma_f32_16x16x32_bf16 v[20:23], v[52:55], v[140:143], v[20:23]\n"
"ds_read_b64_tr_b16 v[78:79], v2 offset:12800\n"
"s_mov_b32 s46, 0x87000\n"
"s_cmp_ge_u32 s16, 6\n"
"s_cselect_b32 s46, 0xfffe5180, s46\n"
"s_ashr_i32 s47, s46, 31\n"
"s_add_u32 s48, s50, s46\n"
"s_addc_u32 s49, s51, s47\n"
"s_add_u32 m0, s44, 0xc000\n"
"v_mfma_f32_16x16x32_bf16 v[24:27], v[56:59], v[136:139], v[24:27]\n"
"ds_read_b64_tr_b16 v[80:81], v3 offset:12288\n"
"global_load_lds_dwordx4 v250, s[48:49]\n"
"v_mfma_f32_16x16x32_bf16 v[28:31], v[56:59], v[140:143], v[28:31]\n"
"ds_read_b64_tr_b16 v[82:83], v3 offset:12800\n"
"global_load_lds_dwordx4 v251, s[48:49] offset:1024\n"
"v_mfma_f32_16x16x32_bf16 v[32:35], v[60:63], v[136:139], v[32:35]\n"
"ds_read_b64_tr_b16 v[84:85], v4 offset:12288\n"
"v_mfma_f32_16x16x32_bf16 v[36:39], v[60:63], v[140:143], v[36:39]\n"
"ds_read_b64_tr_b16 v[86:87], v4 offset:12800\n"
"s_waitcnt lgkmcnt(10)\n"
"v_mfma_f32_16x16x32_bf16 v[16:19], v[64:67], v[144:147], v[16:19]\n"
"ds_read_b64_tr_b16 v[52:53], v2 offset:24576\n"
"v_mfma_f32_16x16x32_bf16 v[20:23], v[64:67], v[148:151], v[20:23]\n"
"ds_read_b64_tr_b16 v[54:55], v2 offset:25088\n"
"global_load_lds_dwordx4 v252, s[48:49] offset:2048\n"
"s_waitcnt lgkmcnt(10)\n"
"v_mfma_f32_16x16x32_bf16 v[24:27], v[68:71], v[144:147], v[24:27]\n"
"ds_read_b64_tr_b16 v[56:57], v3 offset:24576\n"
"s_add_u32 m0, s44, 0xcc00\n"
"v_mfma_f32_16x16x32_bf16 v[28:31], v[68:71], v[148:151], v[28:31]\n"
"ds_read_b64_tr_b16 v[58:59], v3 offset:25088\n"
"global_load_lds_dwordx4 v253, s[48:49]\n"
"s_waitcnt lgkmcnt(10)\n"
"v_mfma_f32_16x16x32_bf16 v[32:35], v[72:75], v[144:147], v[32:35]\n"
"ds_read_b64_tr_b16 v[60:61], v4 offset:24576\n"
"v_mfma_f32_16x16x32_bf16 v[36:39], v[72:75], v[148:151], v[36:39]\n"
"ds_read_b64_tr_b16 v[62:63], v4 offset:25088\n"
"s_waitcnt lgkmcnt(10)\n"
"v_mfma_f32_16x16x32_bf16 v[16:19], v[76:79], v[152:155], v[16:19]\n"
"ds_read_b64_tr_b16 v[64:65], v2 offset:36864\n"
"v_mfma_f32_16x16x32_bf16 v[20:23], v[76:79], v[156:159], v[20:23]\n"
"ds_read_b64_tr_b16 v[66:67], v2 offset:37376\n"
"global_load_lds_dwordx4 v254, s[48:49] offset:1024\n"
"s_waitcnt lgkmcnt(10)\n"
"v_mfma_f32_16x16x32_bf16 v[24:27], v[80:83], v[152:155], v[24:27]\n"
"ds_read_b64_tr_b16 v[68:69], v3 offset:36864\n"
"v_mfma_f32_16x16x32_bf16 v[28:31], v[80:83], v[156:159], v[28:31]\n"
"ds_read_b64_tr_b16 v[70:71], v3 offset:37376\n"
"global_load_lds_dwordx4 v255, s[48:49] offset:2048\n"
"s_waitcnt lgkmcnt(10)\n"
"v_mfma_f32_16x16x32_bf16 v[32:35], v[84:87], v[152:155], v[32:35]\n"
"ds_read_b64_tr_b16 v[72:73], v4 offset:36864\n"
"v_mfma_f32_16x16x32_bf16 v[36:39], v[84:87], v[156:159], v[36:39]\n"
"ds_read_b64_tr_b16 v[74:75], v4 offset:37376\n"
"s_waitcnt lgkmcnt(10)\n"
"v_mfma_f32_16x16x32_bf16 v[16:19], v[52:55], v[160:163], v[16:19]\n"
"s_waitcnt vmcnt(6) lgkmcnt(0)\n"
"s_barrier\n"
"ds_read_b64_tr_b16 v[76:77], v235 offset:0\n"
"v_mfma_f32_16x16x32_bf16 v[20:23], v[52:55], v[164:167], v[20:23]\n"
"ds_read_b64_tr_b16 v[78:79], v235 offset:512\n"
"s_add_u32 s48, s50, 0x9180\n"
"s_addc_u32 s49, s51, 0\n"
"s_mov_b32 m0, s44\n"
"v_mfma_f32_16x16x32_bf16 v[24:27], v[56:59], v[160:163], v[24:27]\n"
"ds_read_b64_tr_b16 v[80:81], v236 offset:0\n"
"global_load_lds_dwordx4 v250, s[48:49]\n"
"v_mfma_f32_16x16x32_bf16 v[28:31], v[56:59], v[164:167], v[28:31]\n"
"ds_read_b64_tr_b16 v[82:83], v236 offset:512\n"
"global_load_lds_dwordx4 v251, s[48:49] offset:1024\n"
"v_mfma_f32_16x16x32_bf16 v[32:35], v[60:63], v[160:163], v[32:35]\n"
"ds_read_b64_tr_b16 v[84:85], v237 offset:0\n"
"v_mfma_f32_16x16x32_bf16 v[36:39], v[60:63], v[164:167], v[36:39]\n"
"ds_read_b64_tr_b16 v[86:87], v237 offset:512\n"
"v_mfma_f32_16x16x32_bf16 v[16:19], v[64:67], v[168:171], v[16:19]\n"
"ds_read_b64_tr_b16 v[52:53], v235 offset:12288\n"
"v_mfma_f32_16x16x32_bf16 v[20:23], v[64:67], v[172:175], v[20:23]\n"
"ds_read_b64_tr_b16 v[54:55], v235 offset:12800\n"
"global_load_lds_dwordx4 v252, s[48:49] offset:2048\n"
"v_mfma_f32_16x16x32_bf16 v[24:27], v[68:71], v[168:171], v[24:27]\n"
"ds_read_b64_tr_b16 v[56:57], v236 offset:12288\n"
"s_add_u32 m0, s44, 0xc00\n"
"v_mfma_f32_16x16x32_bf16 v[28:31], v[68:71], v[172:175], v[28:31]\n"
"ds_read_b64_tr_b16 v[58:59], v236 offset:12800\n"
"global_load_lds_dwordx4 v253, s[48:49]\n"
"v_mfma_f32_16x16x32_bf16 v[32:35], v[72:75], v[168:171], v[32:35]\n"
"ds_read_b64_tr_b16 v[60:61], v237 offset:12288\n"
"v_mfma_f32_16x16x32_bf16 v[36:39], v[72:75], v[172:175], v[36:39]\n"
"ds_read_b64_tr_b16 v[62:63], v237 offset:12800\n"
"s_waitcnt lgkmcnt(10)\n"
"v_mfma_f32_16x16x32_bf16 v[16:19], v[76:79], v[176:179], v[16:19]\n"
"ds_read_b64_tr_b16 v[64:65], v235 offset:24576\n"
"v_mfma_f32_16x16x32_bf16 v[20:23], v[76:79], v[180:183], v[20:23]\n"
"ds_read_b64_tr_b16 v[66:67], v235 offset:25088\n"
"global_load_lds_dwordx4 v254, s[48:49] offset:1024\n"
"s_waitcnt lgkmcnt(10)\n"
"v_mfma_f32_16x16x32_bf16 v[24:27], v[80:83], v[176:179], v[24:27]\n"
"ds_read_b64_tr_b16 v[68:69], v236 offset:24576\n"
"v_mfma_f32_16x16x32_bf16 v[28:31], v[80:83], v[180:183], v[28:31]\n"
"ds_read_b64_tr_b16 v[70:71], v236 offset:25088\n"
"s_waitcnt lgkmcnt(10)\n"
"v_mfma_f32_16x16x32_bf16 v[32:35], v[84:87], v[176:179], v[32:35]\n"
"ds_read_b64_tr_b16 v[72:73], v237 offset:24576\n"
"v_mfma_f32_16x16x32_bf16 v[36:39], v[84:87], v[180:183], v[36:39]\n"
"ds_read_b64_tr_b16 v[74:75], v237 offset:25088\n"
"s_waitcnt lgkmcnt(10)\n"
"v_mfma_f32_16x16x32_bf16 v[16:19], v[52:55], v[184:187], v[16:19]\n"
"ds_read_b64_tr_b16 v[76:77], v235 offset:36864\n"
"v_mfma_f32_16x16x32_bf16 v[20:23], v[52:55], v[188:191], v[20:23]\n"
"ds_read_b64_tr_b16 v[78:79], v235 offset:37376\n"
"global_load_lds_dwordx4 v255, s[48:49] offset:2048\n"
"s_waitcnt lgkmcnt(10)\n"
"v_mfma_f32_16x16x32_bf16 v[24:27], v[56:59], v[184:187], v[24:27]\n"
"ds_read_b64_tr_b16 v[80:81], v236 offset:36864\n"
"v_mfma_f32_16x16x32_bf16 v[28:31], v[56:59], v[188:191], v[28:31]\n"
"ds_read_b64_tr_b16 v[82:83], v236 offset:37376\n"
"s_waitcnt lgkmcnt(10)\n"
"v_mfma_f32_16x16x32_bf16 v[32:35], v[60:63], v[184:187], v[32:35]\n"
"ds_read_b64_tr_b16 v[84:85], v237 offset:36864\n"
"v_mfma_f32_16x16x32_bf16 v[36:39], v[60:63], v[188:191], v[36:39]\n"
"ds_read_b64_tr_b16 v[86:87], v237 offset:37376\n"
"s_waitcnt lgkmcnt(10)\n"
"v_mfma_f32_16x16x32_bf16 v[16:19], v[64:67], v[192:195], v[16:19]\n"
"s_waitcnt vmcnt(6) lgkmcnt(0)\n"
"s_barrier\n"
"ds_read_b64_tr_b16 v[52:53], v232 offset:0\n"
"v_mfma_f32_16x16x32_bf16 v[20:23], v[64:67], v[196:199], v[20:23]\n"
"ds_read_b64_tr_b16 v[54:55], v232 offset:512\n"
"s_add_u32 s48, s50, 0x2d180\n"
"s_addc_u32 s49, s51, 0\n"
"s_add_u32 m0, s44, 0x18000\n"
"v_mfma_f32_16x16x32_bf16 v[24:27], v[68:71], v[192:195], v[24:27]\n"
"ds_read_b64_tr_b16 v[56:57], v233 offset:0\n"
"global_load_lds_dwordx4 v250, s[48:49]\n"
"v_mfma_f32_16x16x32_bf16 v[28:31], v[68:71], v[196:199], v[28:31]\n"
"ds_read_b64_tr_b16 v[58:59], v233 offset:512\n"
"global_load_lds_dwordx4 v251, s[48:49] offset:1024\n"
"v_mfma_f32_16x16x32_bf16 v[32:35], v[72:75], v[192:195], v[32:35]\n"
"ds_read_b64_tr_b16 v[60:61], v234 offset:0\n"
"v_mfma_f32_16x16x32_bf16 v[36:39], v[72:75], v[196:199], v[36:39]\n"
"ds_read_b64_tr_b16 v[62:63], v234 offset:512\n"
"v_mfma_f32_16x16x32_bf16 v[16:19], v[76:79], v[200:203], v[16:19]\n"
"ds_read_b64_tr_b16 v[64:65], v232 offset:12288\n"
"v_mfma_f32_16x16x32_bf16 v[20:23], v[76:79], v[204:207], v[20:23]\n"
"ds_read_b64_tr_b16 v[66:67], v232 offset:12800\n"
"global_load_lds_dwordx4 v252, s[48:49] offset:2048\n"
"v_mfma_f32_16x16x32_bf16 v[24:27], v[80:83], v[200:203], v[24:27]\n"
"ds_read_b64_tr_b16 v[68:69], v233 offset:12288\n"
"s_add_u32 m0, s44, 0x18c00\n"
"v_mfma_f32_16x16x32_bf16 v[28:31], v[80:83], v[204:207], v[28:31]\n"
"ds_read_b64_tr_b16 v[70:71], v233 offset:12800\n"
"global_load_lds_dwordx4 v253, s[48:49]\n"
"v_mfma_f32_16x16x32_bf16 v[32:35], v[84:87], v[200:203], v[32:35]\n"
"ds_read_b64_tr_b16 v[72:73], v234 offset:12288\n"
"v_mfma_f32_16x16x32_bf16 v[36:39], v[84:87], v[204:207], v[36:39]\n"
"ds_read_b64_tr_b16 v[74:75], v234 offset:12800\n"
"s_waitcnt lgkmcnt(10)\n"
"v_mfma_f32_16x16x32_bf16 v[16:19], v[52:55], v[208:211], v[16:19]\n"
"ds_read_b64_tr_b16 v[76:77], v232 offset:24576\n"
"v_mfma_f32_16x16x32_bf16 v[20:23], v[52:55], v[212:215], v[20:23]\n"
"ds_read_b64_tr_b16 v[78:79], v232 offset:25088\n"
"global_load_lds_dwordx4 v254, s[48:49] offset:1024\n"
"s_waitcnt lgkmcnt(10)\n"
"v_mfma_f32_16x16x32_bf16 v[24:27], v[56:59], v[208:211], v[24:27]\n"
"ds_read_b64_tr_b16 v[80:81], v233 offset:24576\n"
"v_mfma_f32_16x16x32_bf16 v[28:31], v[56:59], v[212:215], v[28:31]\n"
"ds_read_b64_tr_b16 v[82:83], v233 offset:25088\n"
"s_waitcnt lgkmcnt(10)\n"
"v_mfma_f32_16x16x32_bf16 v[32:35], v[60:63], v[208:211], v[32:35]\n"
"ds_read_b64_tr_b16 v[84:85], v234 offset:24576\n"
"v_mfma_f32_16x16x32_bf16 v[36:39], v[60:63], v[212:215], v[36:39]\n"
"ds_read_b64_tr_b16 v[86:87], v234 offset:25088\n"
"s_waitcnt lgkmcnt(10)\n"
"v_mfma_f32_16x16x32_bf16 v[16:19], v[64:67], v[216:219], v[16:19]\n"
"ds_read_b64_tr_b16 v[52:53], v232 offset:36864\n"
"v_mfma_f32_16x16x32_bf16 v[20:23], v[64:67], v[220:223], v[20:23]\n"
"ds_read_b64_tr_b16 v[54:55], v232 offset:37376\n"
"global_load_lds_dwordx4 v255, s[48:49] offset:2048\n"
"s_waitcnt lgkmcnt(10)\n"
"v_mfma_f32_16x16x32_bf16 v[24:27], v[68:71], v[216:219], v[24:27]\n"
"ds_read_b64_tr_b16 v[56:57], v233 offset:36864\n"
"v_mfma_f32_16x16x32_bf16 v[28:31], v[68:71], v[220:223], v[28:31]\n"
"ds_read_b64_tr_b16 v[58:59], v233 offset:37376\n"
"s_waitcnt lgkmcnt(10)\n"
"v_mfma_f32_16x16x32_bf16 v[32:35], v[72:75], v[216:219], v[32:35]\n"
"ds_read_b64_tr_b16 v[60:61], v234 offset:36864\n"
"v_mfma_f32_16x16x32_bf16 v[36:39], v[72:75], v[220:223], v[36:39]\n"
"ds_read_b64_tr_b16 v[62:63], v234 offset:37376\n"
"s_waitcnt lgkmcnt(10)\n"
"v_mfma_f32_16x16x32_bf16 v[16:19], v[76:79], v[224:227], v[16:19]\n"
"s_waitcnt vmcnt(6) lgkmcnt(0)\n"
"s_barrier\n"
"ds_read_b64_tr_b16 v[64:65], v2 offset:0\n"
"v_mfma_f32_16x16x32_bf16 v[20:23], v[76:79], v[228:231], v[20:23]\n"
"ds_read_b64_tr_b16 v[66:67], v2 offset:512\n"
"s_add_u32 s48, s50, 0x51180\n"
"s_addc_u32 s49, s51, 0\n"
"s_add_u32 m0, s44, 0xc000\n"
"v_mfma_f32_16x16x32_bf16 v[24:27], v[80:83], v[224:227], v[24:27]\n"
"ds_read_b64_tr_b16 v[68:69], v3 offset:0\n"
"global_load_lds_dwordx4 v250, s[48:49]\n"
"v_mfma_f32_16x16x32_bf16 v[28:31], v[80:83], v[228:231], v[28:31]\n"
"ds_read_b64_tr_b16 v[70:71], v3 offset:512\n"
"global_load_lds_dwordx4 v251, s[48:49] offset:1024\n"
"v_mfma_f32_16x16x32_bf16 v[32:35], v[84:87], v[224:227], v[32:35]\n"
"ds_read_b64_tr_b16 v[72:73], v4 offset:0\n"
"v_mfma_f32_16x16x32_bf16 v[36:39], v[84:87], v[228:231], v[36:39]\n"
"ds_read_b64_tr_b16 v[74:75], v4 offset:512\n"
"v_cvt_pk_bf16_f32 v40, v16, v17\n"
"v_cvt_pk_bf16_f32 v41, v18, v19\n"
"v_mfma_f32_16x16x32_bf16 v[16:19], v[52:55], v[88:91], 0\n"
"v_cvt_pk_bf16_f32 v44, v20, v21\n"
"v_cvt_pk_bf16_f32 v45, v22, v23\n"
"ds_read_b64_tr_b16 v[76:77], v2 offset:12288\n"
"v_mfma_f32_16x16x32_bf16 v[20:23], v[52:55], v[92:95], 0\n"
"v_cvt_pk_bf16_f32 v42, v24, v25\n"
"v_cvt_pk_bf16_f32 v43, v26, v27\n"
"ds_read_b64_tr_b16 v[78:79], v2 offset:12800\n"
"global_load_lds_dwordx4 v252, s[48:49] offset:2048\n"
"v_mfma_f32_16x16x32_bf16 v[24:27], v[56:59], v[88:91], 0\n"
"v_cvt_pk_bf16_f32 v46, v28, v29\n"
"v_cvt_pk_bf16_f32 v47, v30, v31\n"
"ds_read_b64_tr_b16 v[80:81], v3 offset:12288\n"
"s_add_u32 m0, s44, 0xcc00\n"
"v_mfma_f32_16x16x32_bf16 v[28:31], v[56:59], v[92:95], 0\n"
"v_cvt_pk_bf16_f32 v48, v32, v33\n"
"v_cvt_pk_bf16_f32 v49, v34, v35\n"
"ds_read_b64_tr_b16 v[82:83], v3 offset:12800\n"
"global_load_lds_dwordx4 v253, s[48:49]\n"
"v_mfma_f32_16x16x32_bf16 v[32:35], v[60:63], v[88:91], 0\n"
"v_cvt_pk_bf16_f32 v50, v36, v37\n"
"v_cvt_pk_bf16_f32 v51, v38, v39\n"
"ds_read_b64_tr_b16 v[84:85], v4 offset:12288\n"
"v_mfma_f32_16x16x32_bf16 v[36:39], v[60:63], v[92:95], 0\n"
"ds_read_b64_tr_b16 v[86:87], v4 offset:12800\n"
"s_waitcnt lgkmcnt(10)\n"
"v_mfma_f32_16x16x32_bf16 v[16:19], v[64:67], v[96:99], v[16:19]\n"
"ds_read_b64_tr_b16 v[52:53], v2 offset:24576\n"
"v_permlane32_swap_b32 v40, v42\n"
"v_mfma_f32_16x16x32_bf16 v[20:23], v[64:67], v[100:103], v[20:23]\n"
"ds_read_b64_tr_b16 v[54:55], v2 offset:25088\n"
"global_load_lds_dwordx4 v254, s[48:49] offset:1024\n"
"v_permlane32_swap_b32 v41, v43\n"
"s_waitcnt lgkmcnt(10)\n"
"v_mfma_f32_16x16x32_bf16 v[24:27], v[68:71], v[96:99], v[24:27]\n"
"ds_read_b64_tr_b16 v[56:57], v3 offset:24576\n"
"v_permlane16_swap_b32 v40, v42\n"
"v_mfma_f32_16x16x32_bf16 v[28:31], v[68:71], v[100:103], v[28:31]\n"
"ds_read_b64_tr_b16 v[58:59], v3 offset:25088\n"
"v_permlane16_swap_b32 v41, v43\n"
"s_waitcnt lgkmcnt(10)\n"
"v_mfma_f32_16x16x32_bf16 v[32:35], v[72:75], v[96:99], v[32:35]\n"
"ds_read_b64_tr_b16 v[60:61], v4 offset:24576\n"
"v_mfma_f32_16x16x32_bf16 v[36:39], v[72:75], v[100:103], v[36:39]\n"
"ds_read_b64_tr_b16 v[62:63], v4 offset:25088\n"
"global_store_dwordx4 v6, v[40:43], s[52:53] offset:-3072 sc1\n"
"s_waitcnt lgkmcnt(10)\n"
"v_mfma_f32_16x16x32_bf16 v[16:19], v[76:79], v[104:107], v[16:19]\n"
"ds_read_b64_tr_b16 v[64:65], v2 offset:36864\n"
"v_mfma_f32_16x16x32_bf16 v[20:23], v[76:79], v[108:111], v[20:23]\n"
"ds_read_b64_tr_b16 v[66:67], v2 offset:37376\n"
"global_load_lds_dwordx4 v255, s[48:49] offset:2048\n"
"s_waitcnt lgkmcnt(10)\n"
"v_mfma_f32_16x16x32_bf16 v[24:27], v[80:83], v[104:107], v[24:27]\n"
"ds_read_b64_tr_b16 v[68:69], v3 offset:36864\n"
"v_mfma_f32_16x16x32_bf16 v[28:31], v[80:83], v[108:111], v[28:31]\n"
"ds_read_b64_tr_b16 v[70:71], v3 offset:37376\n"
"s_waitcnt lgkmcnt(10)\n"
"v_mfma_f32_16x16x32_bf16 v[32:35], v[84:87], v[104:107], v[32:35]\n"
"ds_read_b64_tr_b16 v[72:73], v4 offset:36864\n"
"v_mfma_f32_16x16x32_bf16 v[36:39], v[84:87], v[108:111], v[36:39]\n"
"ds_read_b64_tr_b16 v[74:75], v4 offset:37376\n"
"s_waitcnt lgkmcnt(10)\n"
"v_mfma_f32_16x16x32_bf16 v[16:19], v[52:55], v[112:115], v[16:19]\n"
"s_waitcnt vmcnt(7) lgkmcnt(0)\n"
"s_barrier\n"
"ds_read_b64_tr_b16 v[76:77], v235 offset:0\n"
"v_mfma_f32_16x16x32_bf16 v[20:23], v[52:55], v[116:119], v[20:23]\n"
"ds_read_b64_tr_b16 v[78:79], v235 offset:512\n"
"s_add_u32 s48, s50, 0x75180\n"
"s_addc_u32 s49, s51, 0\n"
"s_mov_b32 m0, s44\n"
"v_mfma_f32_16x16x32_bf16 v[24:27], v[56:59], v[112:115], v[24:27]\n"
"ds_read_b64_tr_b16 v[80:81], v236 offset:0\n"
"global_load_lds_dwordx4 v250, s[48:49]\n"
"v_mfma_f32_16x16x32_bf16 v[28:31], v[56:59], v[116:119], v[28:31]\n"
"ds_read_b64_tr_b16 v[82:83], v236 offset:512\n"
"global_load_lds_dwordx4 v251, s[48:49] offset:1024\n"
"v_mfma_f32_16x16x32_bf16 v[32:35], v[60:63], v[112:115], v[32:35]\n"
"ds_read_b64_tr_b16 v[84:85], v237 offset:0\n"
"v_mfma_f32_16x16x32_bf16 v[36:39], v[60:63], v[116:119], v[36:39]\n"
"ds_read_b64_tr_b16 v[86:87], v237 offset:512\n"
"v_mfma_f32_16x16x32_bf16 v[16:19], v[64:67], v[120:123], v[16:19]\n"
"ds_read_b64_tr_b16 v[52:53], v235 offset:12288\n"
"v_mfma_f32_16x16x32_bf16 v[20:23], v[64:67], v[124:127], v[20:23]\n"
"ds_read_b64_tr_b16 v[54:55], v235 offset:12800\n"
"global_load_lds_dwordx4 v252, s[48:49] offset:2048\n"
"v_mfma_f32_16x16x32_bf16 v[24:27], v[68:71], v[120:123], v[24:27]\n"
"ds_read_b64_tr_b16 v[56:57], v236 offset:12288\n"
"s_add_u32 m0, s44, 0xc00\n"
"v_mfma_f32_16x16x32_bf16 v[28:31], v[68:71], v[124:127], v[28:31]\n"
"ds_read_b64_tr_b16 v[58:59], v236 offset:12800\n"
"global_load_lds_dwordx4 v253, s[48:49]\n"
"v_mfma_f32_16x16x32_bf16 v[32:35], v[72:75], v[120:123], v[32:35]\n"
"ds_read_b64_tr_b16 v[60:61], v237 offset:12288\n"
"v_mfma_f32_16x16x32_bf16 v[36:39], v[72:75], v[124:127], v[36:39]\n"
"ds_read_b64_tr_b16 v[62:63], v237 offset:12800\n"
"s_waitcnt lgkmcnt(10)\n"
"v_mfma_f32_16x16x32_bf16 v[16:19], v[76:79], v[128:131], v[16:19]\n"
"ds_read_b64_tr_b16 v[64:65], v235 offset:24576\n"
"v_permlane32_swap_b32 v44, v46\n"
"v_mfma_f32_16x16x32_bf16 v[20:23], v[76:79], v[132:135], v[20:23]\n"
"ds_read_b64_tr_b16 v[66:67], v235 offset:25088\n"
"global_load_lds_dwordx4 v254, s[48:49] offset:1024\n"
"v_permlane32_swap_b32 v45, v47\n"
"s_waitcnt lgkmcnt(10)\n"
"v_mfma_f32_16x16x32_bf16 v[24:27], v[80:83], v[128:131], v[24:27]\n"
"ds_read_b64_tr_b16 v[68:69], v236 offset:24576\n"
"v_permlane16_swap_b32 v44, v46\n"
"v_mfma_f32_16x16x32_bf16 v[28:31], v[80:83], v[132:135], v[28:31]\n"
"ds_read_b64_tr_b16 v[70:71], v236 offset:25088\n"
"v_permlane16_swap_b32 v45, v47\n"
"s_waitcnt lgkmcnt(10)\n"
"v_mfma_f32_16x16x32_bf16 v[32:35], v[84:87], v[128:131], v[32:35]\n"
"ds_read_b64_tr_b16 v[72:73], v237 offset:24576\n"
"v_mfma_f32_16x16x32_bf16 v[36:39], v[84:87], v[132:135], v[36:39]\n"
"ds_read_b64_tr_b16 v[74:75], v237 offset:25088\n"
"global_store_dwordx4 v6, v[44:47], s[54:55] offset:-3072 sc1\n"
"s_waitcnt lgkmcnt(10)\n"
"v_mfma_f32_16x16x32_bf16 v[16:19], v[52:55], v[136:139], v[16:19]\n"
"ds_read_b64_tr_b16 v[76:77], v235 offset:36864\n"
"v_mfma_f32_16x16x32_bf16 v[20:23], v[52:55], v[140:143], v[20:23]\n"
"ds_read_b64_tr_b16 v[78:79], v235 offset:37376\n"
"global_load_lds_dwordx4 v255, s[48:49] offset:2048\n"
"s_waitcnt lgkmcnt(10)\n"
"v_mfma_f32_16x16x32_bf16 v[24:27], v[56:59], v[136:139], v[24:27]\n"
"ds_read_b64_tr_b16 v[80:81], v236 offset:36864\n"
"v_mfma_f32_16x16x32_bf16 v[28:31], v[56:59], v[140:143], v[28:31]\n"
"ds_read_b64_tr_b16 v[82:83], v236 offset:37376\n"
"s_waitcnt lgkmcnt(10)\n"
"v_mfma_f32_16x16x32_bf16 v[32:35], v[60:63], v[136:139], v[32:35]\n"
"ds_read_b64_tr_b16 v[84:85], v237 offset:36864\n"
"v_mfma_f32_16x16x32_bf16 v[36:39], v[60:63], v[140:143], v[36:39]\n"
"ds_read_b64_tr_b16 v[86:87], v237 offset:37376\n"
"s_waitcnt lgkmcnt(10)\n"
"v_mfma_f32_16x16x32_bf16 v[16:19], v[64:67], v[144:147], v[16:19]\n"
"s_waitcnt vmcnt(7) lgkmcnt(0)\n"
"s_barrier\n"
"ds_read_b64_tr_b16 v[52:53], v232 offset:0\n"
"v_mfma_f32_16x16x32_bf16 v[20:23], v[64:67], v[148:151], v[20:23]\n"
"ds_read_b64_tr_b16 v[54:55], v232 offset:512\n"
"s_mov_b32 s46, 0x99180\n"
"s_cmp_ge_u32 s16, 2\n"
"s_cselect_b32 s46, 0xffff7300, s46\n"
"s_ashr_i32 s47, s46, 31\n"
"s_add_u32 s48, s50, s46\n"
"s_addc_u32 s49, s51, s47\n"
"s_add_u32 m0, s44, 0x18000\n"
"v_mfma_f32_16x16x32_bf16 v[24:27], v[68:71], v[144:147], v[24:27]\n"
"ds_read_b64_tr_b16 v[56:57], v233 offset:0\n"
"global_load_lds_dwordx4 v250, s[48:49]\n"
"v_mfma_f32_16x16x32_bf16 v[28:31], v[68:71], v[148:151], v[28:31]\n"
"ds_read_b64_tr_b16 v[58:59], v233 offset:512\n"
"global_load_lds_dwordx4 v251, s[48:49] offset:1024\n"
"v_mfma_f32_16x16x32_bf16 v[32:35], v[72:75], v[144:147], v[32:35]\n"
"ds_read_b64_tr_b16 v[60:61], v234 offset:0\n"
"v_mfma_f32_16x16x32_bf16 v[36:39], v[72:75], v[148:151], v[36:39]\n"
"ds_read_b64_tr_b16 v[62:63], v234 offset:512\n"
"v_mfma_f32_16x16x32_bf16 v[16:19], v[76:79], v[152:155], v[16:19]\n"
"ds_read_b64_tr_b16 v[64:65], v232 offset:12288\n"
"v_mfma_f32_16x16x32_bf16 v[20:23], v[76:79], v[156:159], v[20:23]\n"
"ds_read_b64_tr_b16 v[66:67], v232 offset:12800\n"
"global_load_lds_dwordx4 v252, s[48:49] offset:2048\n"
"v_mfma_f32_16x16x32_bf16 v[24:27], v[80:83], v[152:155], v[24:27]\n"
"ds_read_b64_tr_b16 v[68:69], v233 offset:12288\n"
"s_add_u32 m0, s44, 0x18c00\n"
"v_mfma_f32_16x16x32_bf16 v[28:31], v[80:83], v[156:159], v[28:31]\n"
"ds_read_b64_tr_b16 v[70:71], v233 offset:12800\n"
"global_load_lds_dwordx4 v253, s[48:49]\n"
"v_mfma_f32_16x16x32_bf16 v[32:35], v[84:87], v[152:155], v[32:35]\n"
"ds_read_b64_tr_b16 v[72:73], v234 offset:12288\n"
"v_mfma_f32_16x16x32_bf16 v[36:39], v[84:87], v[156:159], v[36:39]\n"
"ds_read_b64_tr_b16 v[74:75], v234 offset:12800\n"
"s_waitcnt lgkmcnt(10)\n"
"v_mfma_f32_16x16x32_bf16 v[16:19], v[52:55], v[160:163], v[16:19]\n"
"ds_read_b64_tr_b16 v[76:77], v232 offset:24576\n"
"v_permlane16_swap_b32 v48, v50\n"
"v_mfma_f32_16x16x32_bf16 v[20:23], v[52:55], v[164:167], v[20:23]\n"
"ds_read_b64_tr_b16 v[78:79], v232 offset:25088\n"
"global_load_lds_dwordx4 v254, s[48:49] offset:1024\n"
"v_permlane16_swap_b32 v49, v51\n"
"s_waitcnt lgkmcnt(10)\n"
"v_mfma_f32_16x16x32_bf16 v[24:27], v[56:59], v[160:163], v[24:27]\n"
"ds_read_b64_tr_b16 v[80:81], v233 offset:24576\n"
"v_mfma_f32_16x16x32_bf16 v[28:31], v[56:59], v[164:167], v[28:31]\n"
"ds_read_b64_tr_b16 v[82:83], v233 offset:25088\n"
"s_waitcnt lgkmcnt(10)\n"
"v_mfma_f32_16x16x32_bf16 v[32:35], v[60:63], v[160:163], v[32:35]\n"
"ds_read_b64_tr_b16 v[84:85], v234 offset:24576\n"
"v_mfma_f32_16x16x32_bf16 v[36:39], v[60:63], v[164:167], v[36:39]\n"
"ds_read_b64_tr_b16 v[86:87], v234 offset:25088\n"
"global_store_dwordx4 v7, v[48:51], s[52:53] offset:-3072 sc1\n"
"s_waitcnt lgkmcnt(10)\n"
"v_mfma_f32_16x16x32_bf16 v[16:19], v[64:67], v[168:171], v[16:19]\n"
"ds_read_b64_tr_b16 v[52:53], v232 offset:36864\n"
"v_mfma_f32_16x16x32_bf16 v[20:23], v[64:67], v[172:175], v[20:23]\n"
"ds_read_b64_tr_b16 v[54:55], v232 offset:37376\n"
"global_load_lds_dwordx4 v255, s[48:49] offset:2048\n"
"s_waitcnt lgkmcnt(10)\n"
"v_mfma_f32_16x16x32_bf16 v[24:27], v[68:71], v[168:171], v[24:27]\n"
"ds_read_b64_tr_b16 v[56:57], v233 offset:36864\n"
"v_mfma_f32_16x16x32_bf16 v[28:31], v[68:71], v[172:175], v[28:31]\n"
"ds_read_b64_tr_b16 v[58:59], v233 offset:37376\n"
"s_waitcnt lgkmcnt(10)\n"
"v_mfma_f32_16x16x32_bf16 v[32:35], v[72:75], v[168:171], v[32:35]\n"
"ds_read_b64_tr_b16 v[60:61], v234 offset:36864\n"
"v_mfma_f32_16x16x32_bf16 v[36:39], v[72:75], v[172:175], v[36:39]\n"
"ds_read_b64_tr_b16 v[62:63], v234 offset:37376\n"
"s_waitcnt lgkmcnt(10)\n"
"v_mfma_f32_16x16x32_bf16 v[16:19], v[76:79], v[176:179], v[16:19]\n"
"s_waitcnt vmcnt(7) lgkmcnt(0)\n"
"s_barrier\n"
"ds_read_b64_tr_b16 v[64:65], v2 offset:0\n"
"v_mfma_f32_16x16x32_bf16 v[20:23], v[76:79], v[180:183], v[20:23]\n"
"ds_read_b64_tr_b16 v[66:67], v2 offset:512\n"
"s_add_u32 s48, s50, 0x1b300\n"
"s_addc_u32 s49, s51, 0\n"
"s_add_u32 m0, s44, 0xc000\n"
"v_mfma_f32_16x16x32_bf16 v[24:27], v[80:83], v[176:179], v[24:27]\n"
"ds_read_b64_tr_b16 v[68:69], v3 offset:0\n"
"global_load_lds_dwordx4 v250, s[48:49]\n"
"v_mfma_f32_16x16x32_bf16 v[28:31], v[80:83], v[180:183], v[28:31]\n"
"ds_read_b64_tr_b16 v[70:71], v3 offset:512\n"
"global_load_lds_dwordx4 v251, s[48:49] offset:1024\n"
"v_mfma_f32_16x16x32_bf16 v[32:35], v[84:87], v[176:179], v[32:35]\n"
"ds_read_b64_tr_b16 v[72:73], v4 offset:0\n"
"v_mfma_f32_16x16x32_bf16 v[36:39], v[84:87], v[180:183], v[36:39]\n"
"ds_read_b64_tr_b16 v[74:75], v4 offset:512\n"
"v_mfma_f32_16x16x32_bf16 v[16:19], v[52:55], v[184:187], v[16:19]\n"
"ds_read_b64_tr_b16 v[76:77], v2 offset:12288\n"
"v_mfma_f32_16x16x32_bf16 v[20:23], v[52:55], v[188:191], v[20:23]\n"
"ds_read_b64_tr_b16 v[78:79], v2 offset:12800\n"
"global_load_lds_dwordx4 v252, s[48:49] offset:2048\n"
"v_mfma_f32_16x16x32_bf16 v[24:27], v[56:59], v[184:187], v[24:27]\n"
"ds_read_b64_tr_b16 v[80:81], v3 offset:12288\n"
"s_add_u32 m0, s44, 0xcc00\n"
"v_mfma_f32_16x16x32_bf16 v[28:31], v[56:59], v[188:191], v[28:31]\n"
"ds_read_b64_tr_b16 v[82:83], v3 offset:12800\n"
"global_load_lds_dwordx4 v253, s[48:49]\n"
"v_mfma_f32_16x16x32_bf16 v[32:35], v[60:63], v[184:187], v[32:35]\n"
"ds_read_b64_tr_b16 v[84:85], v4 offset:12288\n"
"v_mfma_f32_16x16x32_bf16 v[36:39], v[60:63], v[188:191], v[36:39]\n"
"ds_read_b64_tr_b16 v[86:87], v4 offset:12800\n"
"s_waitcnt lgkmcnt(10)\n"
"v_mfma_f32_16x16x32_bf16 v[16:19], v[64:67], v[192:195], v[16:19]\n"
"ds_read_b64_tr_b16 v[52:53], v2 offset:24576\n"
"v_mfma_f32_16x16x32_bf16 v[20:23], v[64:67], v[196:199], v[20:23]\n"
"ds_read_b64_tr_b16 v[54:55], v2 offset:25088\n"
"global_load_lds_dwordx4 v254, s[48:49] offset:1024\n"
"s_waitcnt lgkmcnt(10)\n"
"v_mfma_f32_16x16x32_bf16 v[24:27], v[68:71], v[192:195], v[24:27]\n"
"ds_read_b64_tr_b16 v[56:57], v3 offset:24576\n"
"v_mfma_f32_16x16x32_bf16 v[28:31], v[68:71], v[196:199], v[28:31]\n"
"ds_read_b64_tr_b16 v[58:59], v3 offset:25088\n"
"s_waitcnt lgkmcnt(10)\n"
"v_mfma_f32_16x16x32_bf16 v[32:35], v[72:75], v[192:195], v[32:35]\n"
"ds_read_b64_tr_b16 v[60:61], v4 offset:24576\n"
"v_mfma_f32_16x16x32_bf16 v[36:39], v[72:75], v[196:199], v[36:39]\n"
"ds_read_b64_tr_b16 v[62:63], v4 offset:25088\n"
"s_waitcnt lgkmcnt(10)\n"
"v_mfma_f32_16x16x32_bf16 v[16:19], v[76:79], v[200:203], v[16:19]\n"
"ds_read_b64_tr_b16 v[64:65], v2 offset:36864\n"
"v_mfma_f32_16x16x32_bf16 v[20:23], v[76:79], v[204:207], v[20:23]\n"
"ds_read_b64_tr_b16 v[66:67], v2 offset:37376\n"
"global_load_lds_dwordx4 v255, s[48:49] offset:2048\n"
"s_waitcnt lgkmcnt(10)\n"
"v_mfma_f32_16x16x32_bf16 v[24:27], v[80:83], v[200:203], v[24:27]\n"
"ds_read_b64_tr_b16 v[68:69], v3 offset:36864\n"
"v_mfma_f32_16x16x32_bf16 v[28:31], v[80:83], v[204:207], v[28:31]\n"
"ds_read_b64_tr_b16 v[70:71], v3 offset:37376\n"
"s_waitcnt lgkmcnt(10)\n"
"v_mfma_f32_16x16x32_bf16 v[32:35], v[84:87], v[200:203], v[32:35]\n"
"ds_read_b64_tr_b16 v[72:73], v4 offset:36864\n"
"v_mfma_f32_16x16x32_bf16 v[36:39], v[84:87], v[204:207], v[36:39]\n"
"ds_read_b64_tr_b16 v[74:75], v4 offset:37376\n"
"s_waitcnt lgkmcnt(10)\n"
"v_mfma_f32_16x16x32_bf16 v[16:19], v[52:55], v[208:211], v[16:19]\n"
"s_waitcnt vmcnt(6) lgkmcnt(0)\n"
"s_barrier\n"
"ds_read_b64_tr_b16 v[76:77], v235 offset:0\n"
"v_mfma_f32_16x16x32_bf16 v[20:23], v[52:55], v[212:215], v[20:23]\n"
"ds_read_b64_tr_b16 v[78:79], v235 offset:512\n"
"s_add_u32 s48, s50, 0x3f300\n"
"s_addc_u32 s49, s51, 0\n"
"s_mov_b32 m0, s44\n"
"v_mfma_f32_16x16x32_bf16 v[24:27], v[56:59], v[208:211], v[24:27]\n"
"ds_read_b64_tr_b16 v[80:81], v236 offset:0\n"
"global_load_lds_dwordx4 v250, s[48:49]\n"
"v_mfma_f32_16x16x32_bf16 v[28:31], v[56:59], v[212:215], v[28:31]\n"
"ds_read_b64_tr_b16 v[82:83], v236 offset:512\n"
"global_load_lds_dwordx4 v251, s[48:49] offset:1024\n"
"v_mfma_f32_16x16x32_bf16 v[32:35], v[60:63], v[208:211], v[32:35]\n"
"ds_read_b64_tr_b16 v[84:85], v237 offset:0\n"
"v_mfma_f32_16x16x32_bf16 v[36:39], v[60:63], v[212:215], v[36:39]\n"
"ds_read_b64_tr_b16 v[86:87], v237 offset:512\n"
"v_mfma_f32_16x16x32_bf16 v[16:19], v[64:67], v[216:219], v[16:19]\n"
"ds_read_b64_tr_b16 v[52:53], v235 offset:12288\n"
"v_mfma_f32_16x16x32_bf16 v[20:23], v[64:67], v[220:223], v[20:23]\n"
"ds_read_b64_tr_b16 v[54:55], v235 offset:12800\n"
"global_load_lds_dwordx4 v252, s[48:49] offset:2048\n"
"v_mfma_f32_16x16x32_bf16 v[24:27], v[68:71], v[216:219], v[24:27]\n"
"ds_read_b64_tr_b16 v[56:57], v236 offset:12288\n"
"s_add_u32 m0, s44, 0xc00\n"
"v_mfma_f32_16x16x32_bf16 v[28:31], v[68:71], v[220:223], v[28:31]\n"
"ds_read_b64_tr_b16 v[58:59], v236 offset:12800\n"
"global_load_lds_dwordx4 v253, s[48:49]\n"
"v_mfma_f32_16x16x32_bf16 v[32:35], v[72:75], v[216:219], v[32:35]\n"
"ds_read_b64_tr_b16 v[60:61], v237 offset:12288\n"
"v_mfma_f32_16x16x32_bf16 v[36:39], v[72:75], v[220:223], v[36:39]\n"
"ds_read_b64_tr_b16 v[62:63], v237 offset:12800\n"
"s_waitcnt lgkmcnt(10)\n"
"v_mfma_f32_16x16x32_bf16 v[16:19], v[76:79], v[224:227], v[16:19]\n"
"ds_read_b64_tr_b16 v[64:65], v235 offset:24576\n"
"v_mfma_f32_16x16x32_bf16 v[20:23], v[76:79], v[228:231], v[20:23]\n"
"ds_read_b64_tr_b16 v[66:67], v235 offset:25088\n"
"global_load_lds_dwordx4 v254, s[48:49] offset:1024\n"
"s_waitcnt lgkmcnt(10)\n"
"v_mfma_f32_16x16x32_bf16 v[24:27], v[80:83], v[224:227], v[24:27]\n"
"ds_read_b64_tr_b16 v[68:69], v236 offset:24576\n"
"v_mfma_f32_16x16x32_bf16 v[28:31], v[80:83], v[228:231], v[28:31]\n"
"ds_read_b64_tr_b16 v[70:71], v236 offset:25088\n"
"s_waitcnt lgkmcnt(10)\n"
"v_mfma_f32_16x16x32_bf16 v[32:35], v[84:87], v[224:227], v[32:35]\n"
"ds_read_b64_tr_b16 v[72:73], v237 offset:24576\n"
"v_mfma_f32_16x16x32_bf16 v[36:39], v[84:87], v[228:231], v[36:39]\n"
"ds_read_b64_tr_b16 v[74:75], v237 offset:25088\n"
"v_cvt_pk_bf16_f32 v40, v16, v17\n"
"v_cvt_pk_bf16_f32 v41, v18, v19\n"
"s_waitcnt lgkmcnt(10)\n"
"v_mfma_f32_16x16x32_bf16 v[16:19], v[52:55], v[88:91], 0\n"
"v_cvt_pk_bf16_f32 v44, v20, v21\n"
"v_cvt_pk_bf16_f32 v45, v22, v23\n"
"ds_read_b64_tr_b16 v[76:77], v235 offset:36864\n"
"v_mfma_f32_16x16x32_bf16 v[20:23], v[52:55], v[92:95], 0\n"
"v_cvt_pk_bf16_f32 v42, v24, v25\n"
"v_cvt_pk_bf16_f32 v43, v26, v27\n"
"ds_read_b64_tr_b16 v[78:79], v235 offset:37376\n"
"global_load_lds_dwordx4 v255, s[48:49] offset:2048\n"
"s_waitcnt lgkmcnt(10)\n"
"v_mfma_f32_16x16x32_bf16 v[24:27], v[56:59], v[88:91], 0\n"
"v_cvt_pk_bf16_f32 v46, v28, v29\n"
"v_cvt_pk_bf16_f32 v47, v30, v31\n"
"ds_read_b64_tr_b16 v[80:81], v236 offset:36864\n"
"v_mfma_f32_16x16x32_bf16 v[28:31], v[56:59], v[92:95], 0\n"
"v_cvt_pk_bf16_f32 v48, v32, v33\n"
"v_cvt_pk_bf16_f32 v49, v34, v35\n"
"ds_read_b64_tr_b16 v[82:83], v236 offset:37376\n"
"s_waitcnt lgkmcnt(10)\n"
"v_mfma_f32_16x16x32_bf16 v[32:35], v[60:63], v[88:91], 0\n"
"v_cvt_pk_bf16_f32 v50, v36, v37\n"
"v_cvt_pk_bf16_f32 v51, v38, v39\n"
"ds_read_b64_tr_b16 v[84:85], v237 offset:36864\n"
"v_mfma_f32_16x16x32_bf16 v[36:39], v[60:63], v[92:95], 0\n"
"ds_read_b64_tr_b16 v[86:87], v237 offset:37376\n"
"s_waitcnt lgkmcnt(10)\n"
"v_mfma_f32_16x16x32_bf16 v[16:19], v[64:67], v[96:99], v[16:19]\n"
"s_waitcnt vmcnt(6) lgkmcnt(0)\n"
"s_barrier\n"
"ds_read_b64_tr_b16 v[52:53], v232 offset:0\n"
"v_permlane32_swap_b32 v40, v42\n"
"v_mfma_f32_16x16x32_bf16 v[20:23], v[64:67], v[100:103], v[20:23]\n"
"ds_read_b64_tr_b16 v[54:55], v232 offset:512\n"
"s_add_u32 s48, s50, 0x63300\n"
"s_addc_u32 s49, s51, 0\n"
"s_add_u32 m0, s44, 0x18000\n"
"v_permlane32_swap_b32 v41, v43\n"
"v_mfma_f32_16x16x32_bf16 v[24:27], v[68:71], v[96:99], v[24:27]\n"
"ds_read_b64_tr_b16 v[56:57], v233 offset:0\n"
"global_load_lds_dwordx4 v250, s[48:49]\n"
"v_permlane16_swap_b32 v40, v42\n"
"v_mfma_f32_16x16x32_bf16 v[28:31], v[68:71], v[100:103], v[28:31]\n"
"ds_read_b64_tr_b16 v[58:59], v233 offset:512\n"
"global_load_lds_dwordx4 v251, s[48:49] offset:1024\n"
"v_permlane16_swap_b32 v41, v43\n"
"v_mfma_f32_16x16x32_bf16 v[32:35], v[72:75], v[96:99], v[32:35]\n"
"ds_read_b64_tr_b16 v[60:61], v234 offset:0\n"
"v_mfma_f32_16x16x32_bf16 v[36:39], v[72:75], v[100:103], v[36:39]\n"
"ds_read_b64_tr_b16 v[62:63], v234 offset:512\n"
"global_store_dwordx4 v6, v[40:43], s[52:53] offset:0 sc1\n"
"v_mfma_f32_16x16x32_bf16 v[16:19], v[76:79], v[104:107], v[16:19]\n"
"ds_read_b64_tr_b16 v[64:65], v232 offset:12288\n"
"v_mfma_f32_16x16x32_bf16 v[20:23], v[76:79], v[108:111], v[20:23]\n"
"ds_read_b64_tr_b16 v[66:67], v232 offset:12800\n"
"global_load_lds_dwordx4 v252, s[48:49] offset:2048\n"
"v_mfma_f32_16x16x32_bf16 v[24:27], v[80:83], v[104:107], v[24:27]\n"
"ds_read_b64_tr_b16 v[68:69], v233 offset:12288\n"
"s_add_u32 m0, s44, 0x18c00\n"
"v_mfma_f32_16x16x32_bf16 v[28:31], v[80:83], v[108:111], v[28:31]\n"
"ds_read_b64_tr_b16 v[70:71], v233 offset:12800\n"
"global_load_lds_dwordx4 v253, s[48:49]\n"
"v_mfma_f32_16x16x32_bf16 v[32:35], v[84:87], v[104:107], v[32:35]\n"
"ds_read_b64_tr_b16 v[72:73], v234 offset:12288\n"
"v_mfma_f32_16x16x32_bf16 v[36:39], v[84:87], v[108:111], v[36:39]\n"
"ds_read_b64_tr_b16 v[74:75], v234 offset:12800\n"
"s_waitcnt lgkmcnt(10)\n"
"v_mfma_f32_16x16x32_bf16 v[16:19], v[52:55], v[112:115], v[16:19]\n"
"ds_read_b64_tr_b16 v[76:77], v232 offset:24576\n"
"v_mfma_f32_16x16x32_bf16 v[20:23], v[52:55], v[116:119], v[20:23]\n"
"ds_read_b64_tr_b16 v[78:79], v232 offset:25088\n"
"global_load_lds_dwordx4 v254, s[48:49] offset:1024\n"
"s_waitcnt lgkmcnt(10)\n"
"v_mfma_f32_16x16x32_bf16 v[24:27], v[56:59], v[112:115], v[24:27]\n"
"ds_read_b64_tr_b16 v[80:81], v233 offset:24576\n"
"v_mfma_f32_16x16x32_bf16 v[28:31], v[56:59], v[116:119], v[28:31]\n"
"ds_read_b64_tr_b16 v[82:83], v233 offset:25088\n"
"s_waitcnt lgkmcnt(10)\n"
"v_mfma_f32_16x16x32_bf16 v[32:35], v[60:63], v[112:115], v[32:35]\n"
"ds_read_b64_tr_b16 v[84:85], v234 offset:24576\n"
"v_mfma_f32_16x16x32_bf16 v[36:39], v[60:63], v[116:119], v[36:39]\n"
"ds_read_b64_tr_b16 v[86:87], v234 offset:25088\n"
"s_waitcnt lgkmcnt(10)\n"
"v_mfma_f32_16x16x32_bf16 v[16:19], v[64:67], v[120:123], v[16:19]\n"
"ds_read_b64_tr_b16 v[52:53], v232 offset:36864\n"
"v_mfma_f32_16x16x32_bf16 v[20:23], v[64:67], v[124:127], v[20:23]\n"
"ds_read_b64_tr_b16 v[54:55], v232 offset:37376\n"
"global_load_lds_dwordx4 v255, s[48:49] offset:2048\n"
"s_waitcnt lgkmcnt(10)\n"
"v_mfma_f32_16x16x32_bf16 v[24:27], v[68:71], v[120:123], v[24:27]\n"
"ds_read_b64_tr_b16 v[56:57], v233 offset:36864\n"
"v_mfma_f32_16x16x32_bf16 v[28:31], v[68:71], v[124:127], v[28:31]\n"
"ds_read_b64_tr_b16 v[58:59], v233 offset:37376\n"
"s_waitcnt lgkmcnt(10)\n"
"v_mfma_f32_16x16x32_bf16 v[32:35], v[72:75], v[120:123], v[32:35]\n"
"ds_read_b64_tr_b16 v[60:61], v234 offset:36864\n"
"v_mfma_f32_16x16x32_bf16 v[36:39], v[72:75], v[124:127], v[36:39]\n"
"ds_read_b64_tr_b16 v[62:63], v234 offset:37376\n"
"s_waitcnt lgkmcnt(10)\n"
"v_mfma_f32_16x16x32_bf16 v[16:19], v[76:79], v[128:131], v[16:19]\n"
"s_waitcnt vmcnt(7) lgkmcnt(0)\n"
"s_barrier\n"
"ds_read_b64_tr_b16 v[64:65], v2 offset:0\n"
"v_permlane32_swap_b32 v44, v46\n"
"v_mfma_f32_16x16x32_bf16 v[20:23], v[76:79], v[132:135], v[20:23]\n"
"ds_read_b64_tr_b16 v[66:67], v2 offset:512\n"
"s_mov_b32 s46, 0x87300\n"
"s_cmp_ge_u32 s16, 6\n"
"s_cselect_b32 s46, 0x7e300, s46\n"
"s_ashr_i32 s47, s46, 31\n"
"s_add_u32 s48, s50, s46\n"
"s_addc_u32 s49, s51, s47\n"
"s_add_u32 m0, s45, 0xc000\n"
"v_permlane32_swap_b32 v45, v47\n"
"v_mfma_f32_16x16x32_bf16 v[24:27], v[80:83], v[128:131], v[24:27]\n"
"ds_read_b64_tr_b16 v[68:69], v3 offset:0\n"
"global_load_lds_dwordx4 v250, s[48:49]\n"
"v_permlane16_swap_b32 v44, v46\n"
"v_mfma_f32_16x16x32_bf16 v[28:31], v[80:83], v[132:135], v[28:31]\n"
"ds_read_b64_tr_b16 v[70:71], v3 offset:512\n"
"global_load_lds_dwordx4 v251, s[48:49] offset:1024\n"
"v_permlane16_swap_b32 v45, v47\n"
"v_mfma_f32_16x16x32_bf16 v[32:35], v[84:87], v[128:131], v[32:35]\n"
"ds_read_b64_tr_b16 v[72:73], v4 offset:0\n"
"v_mfma_f32_16x16x32_bf16 v[36:39], v[84:87], v[132:135], v[36:39]\n"
"ds_read_b64_tr_b16 v[74:75], v4 offset:512\n"
"global_store_dwordx4 v6, v[44:47], s[54:55] offset:0 sc1\n"
"v_mfma_f32_16x16x32_bf16 v[16:19], v[52:55], v[136:139], v[16:19]\n"
"ds_read_b64_tr_b16 v[76:77], v2 offset:12288\n"
"v_mfma_f32_16x16x32_bf16 v[20:23], v[52:55], v[140:143], v[20:23]\n"
"ds_read_b64_tr_b16 v[78:79], v2 offset:12800\n"
"global_load_lds_dwordx4 v252, s[48:49] offset:2048\n"
"v_mfma_f32_16x16x32_bf16 v[24:27], v[56:59], v[136:139], v[24:27]\n"
"ds_read_b64_tr_b16 v[80:81], v3 offset:12288\n"
"s_add_u32 m0, s45, 0xcc00\n"
"v_mfma_f32_16x16x32_bf16 v[28:31], v[56:59], v[140:143], v[28:31]\n"
"ds_read_b64_tr_b16 v[82:83], v3 offset:12800\n"
"global_load_lds_dwordx4 v253, s[48:49]\n"
"v_mfma_f32_16x16x32_bf16 v[32:35], v[60:63], v[136:139], v[32:35]\n"
"ds_read_b64_tr_b16 v[84:85], v4 offset:12288\n"
"v_mfma_f32_16x16x32_bf16 v[36:39], v[60:63], v[140:143], v[36:39]\n"
"ds_read_b64_tr_b16 v[86:87], v4 offset:12800\n"
"s_waitcnt lgkmcnt(10)\n"
"v_mfma_f32_16x16x32_bf16 v[16:19], v[64:67], v[144:147], v[16:19]\n"
"ds_read_b64_tr_b16 v[52:53], v2 offset:24576\n"
"v_mfma_f32_16x16x32_bf16 v[20:23], v[64:67], v[148:151], v[20:23]\n"
"ds_read_b64_tr_b16 v[54:55], v2 offset:25088\n"
"global_load_lds_dwordx4 v254, s[48:49] offset:1024\n"
"s_waitcnt lgkmcnt(10)\n"
"v_mfma_f32_16x16x32_bf16 v[24:27], v[68:71], v[144:147], v[24:27]\n"
"ds_read_b64_tr_b16 v[56:57], v3 offset:24576\n"
"v_mfma_f32_16x16x32_bf16 v[28:31], v[68:71], v[148:151], v[28:31]\n"
"ds_read_b64_tr_b16 v[58:59], v3 offset:25088\n"
"s_waitcnt lgkmcnt(10)\n"
"v_mfma_f32_16x16x32_bf16 v[32:35], v[72:75], v[144:147], v[32:35]\n"
"ds_read_b64_tr_b16 v[60:61], v4 offset:24576\n"
"v_mfma_f32_16x16x32_bf16 v[36:39], v[72:75], v[148:151], v[36:39]\n"
"ds_read_b64_tr_b16 v[62:63], v4 offset:25088\n"
"s_waitcnt lgkmcnt(10)\n"
"v_mfma_f32_16x16x32_bf16 v[16:19], v[76:79], v[152:155], v[16:19]\n"
"ds_read_b64_tr_b16 v[64:65], v2 offset:36864\n"
"v_mfma_f32_16x16x32_bf16 v[20:23], v[76:79], v[156:159], v[20:23]\n"
"ds_read_b64_tr_b16 v[66:67], v2 offset:37376\n"
"global_load_lds_dwordx4 v255, s[48:49] offset:2048\n"
"s_waitcnt lgkmcnt(10)\n"
"v_mfma_f32_16x16x32_bf16 v[24:27], v[80:83], v[152:155], v[24:27]\n"
"ds_read_b64_tr_b16 v[68:69], v3 offset:36864\n"
"v_mfma_f32_16x16x32_bf16 v[28:31], v[80:83], v[156:159], v[28:31]\n"
"ds_read_b64_tr_b16 v[70:71], v3 offset:37376\n"
"s_waitcnt lgkmcnt(10)\n"
"v_mfma_f32_16x16x32_bf16 v[32:35], v[84:87], v[152:155], v[32:35]\n"
"ds_read_b64_tr_b16 v[72:73], v4 offset:36864\n"
"v_mfma_f32_16x16x32_bf16 v[36:39], v[84:87], v[156:159], v[36:39]\n"
"ds_read_b64_tr_b16 v[74:75], v4 offset:37376\n"
"s_waitcnt lgkmcnt(10)\n"
"v_mfma_f32_16x16x32_bf16 v[16:19], v[52:55], v[160:163], v[16:19]\n"
"s_waitcnt vmcnt(7) lgkmcnt(0)\n"
"s_barrier\n"
"ds_read_b64_tr_b16 v[76:77], v235 offset:0\n"
"v_permlane16_swap_b32 v48, v50\n"
"v_mfma_f32_16x16x32_bf16 v[20:23], v[52:55], v[164:167], v[20:23]\n"
"ds_read_b64_tr_b16 v[78:79], v235 offset:512\n"
"v_permlane16_swap_b32 v49, v51\n"
"v_mfma_f32_16x16x32_bf16 v[24:27], v[56:59], v[160:163], v[24:27]\n"
"ds_read_b64_tr_b16 v[80:81], v236 offset:0\n"
"v_mfma_f32_16x16x32_bf16 v[28:31], v[56:59], v[164:167], v[28:31]\n"
"ds_read_b64_tr_b16 v[82:83], v236 offset:512\n"
"v_mfma_f32_16x16x32_bf16 v[32:35], v[60:63], v[160:163], v[32:35]\n"
"ds_read_b64_tr_b16 v[84:85], v237 offset:0\n"
"v_mfma_f32_16x16x32_bf16 v[36:39], v[60:63], v[164:167], v[36:39]\n"
"ds_read_b64_tr_b16 v[86:87], v237 offset:512\n"
"global_store_dwordx4 v7, v[48:51], s[52:53] offset:0 sc1\n"
"v_mfma_f32_16x16x32_bf16 v[16:19], v[64:67], v[168:171], v[16:19]\n"
"ds_read_b64_tr_b16 v[52:53], v235 offset:12288\n"
"v_lshrrev_b32 v130, 4, v1\n"
"v_and_b32 v131, 15, v1\n"
"v_mfma_f32_16x16x32_bf16 v[20:23], v[64:67], v[172:175], v[20:23]\n"
"ds_read_b64_tr_b16 v[54:55], v235 offset:12800\n"
"v_bfe_u32 v132, v1, 3, 1\n"
"s_mul_i32 s40, s16, 0x180\n"
"v_mfma_f32_16x16x32_bf16 v[24:27], v[68:71], v[168:171], v[24:27]\n"
"ds_read_b64_tr_b16 v[56:57], v236 offset:12288\n"
"s_add_u32 s40, s40, 0xc00\n"
"s_mov_b32 s41, 0x2400\n"
"v_mfma_f32_16x16x32_bf16 v[28:31], v[68:71], v[172:175], v[28:31]\n"
"ds_read_b64_tr_b16 v[58:59], v236 offset:12800\n"
"v_mul_u32_u24 v133, s41, v130\n"
"v_add_u32 v133, s40, v133\n"
"v_mfma_f32_16x16x32_bf16 v[32:35], v[72:75], v[168:171], v[32:35]\n"
"ds_read_b64_tr_b16 v[60:61], v237 offset:12288\n"
"v_lshl_add_u32 v144, v131, 4, v133\n"
"v_mfma_f32_16x16x32_bf16 v[36:39], v[72:75], v[172:175], v[36:39]\n"
"ds_read_b64_tr_b16 v[62:63], v237 offset:12800\n"
"s_waitcnt lgkmcnt(10)\n"
"v_mfma_f32_16x16x32_bf16 v[16:19], v[76:79], v[176:179], v[16:19]\n"
"ds_read_b64_tr_b16 v[64:65], v235 offset:24576\n"
"s_mov_b32 s41, 0x8f80\n"
"v_mul_u32_u24 v133, s41, v132\n"
"v_mfma_f32_16x16x32_bf16 v[20:23], v[76:79], v[180:183], v[20:23]\n"
"ds_read_b64_tr_b16 v[66:67], v235 offset:25088\n"
"v_add_u32 v133, 0x100, v133\n"
"v_add_u32 v145, v133, v144\n"
"s_waitcnt lgkmcnt(10)\n"
"v_mfma_f32_16x16x32_bf16 v[24:27], v[80:83], v[176:179], v[24:27]\n"
"ds_read_b64_tr_b16 v[68:69], v236 offset:24576\n"
"s_mul_i32 s40, s18, 0x90000\n"
"s_mul_i32 s41, s19, 0x12000\n"
"v_mfma_f32_16x16x32_bf16 v[28:31], v[80:83], v[180:183], v[28:31]\n"
"ds_read_b64_tr_b16 v[70:71], v236 offset:25088\n"
"s_add_u32 s40, s40, s41\n"
"s_add_u32 s40, s40, 0xa8e000\n"
"s_waitcnt lgkmcnt(10)\n"
"v_mfma_f32_16x16x32_bf16 v[32:35], v[84:87], v[176:179], v[32:35]\n"
"ds_read_b64_tr_b16 v[72:73], v237 offset:24576\n"
"s_add_u32 s20, s4, s40\n"
"s_addc_u32 s21, s5, 0\n"
"v_mfma_f32_16x16x32_bf16 v[36:39], v[84:87], v[180:183], v[36:39]\n"
"ds_read_b64_tr_b16 v[74:75], v237 offset:25088\n"
"s_waitcnt lgkmcnt(10)\n"
"v_mfma_f32_16x16x32_bf16 v[16:19], v[52:55], v[184:187], v[16:19]\n"
"ds_read_b64_tr_b16 v[76:77], v235 offset:36864\n"
"s_add_u32 s22, s20, 0x9000\n"
"s_addc_u32 s23, s21, 0\n"
"v_mfma_f32_16x16x32_bf16 v[20:23], v[52:55], v[188:191], v[20:23]\n"
"ds_read_b64_tr_b16 v[78:79], v235 offset:37376\n"
"v_and_b32 v133, 1, v131\n"
"v_lshlrev_b32 v133, 4, v133\n"
"s_waitcnt lgkmcnt(10)\n"
"v_mfma_f32_16x16x32_bf16 v[24:27], v[56:59], v[184:187], v[24:27]\n"
"ds_read_b64_tr_b16 v[80:81], v236 offset:36864\n"
"v_mov_b32 v155, 0x3f80\n"
"v_lshlrev_b32 v133, v133, v155\n"
"v_mfma_f32_16x16x32_bf16 v[28:31], v[56:59], v[188:191], v[28:31]\n"
"ds_read_b64_tr_b16 v[82:83], v236 offset:37376\n"
"v_lshrrev_b32 v155, 1, v131\n"
"v_cmp_eq_u32 vcc, 0, v155\ns_nop 1\nv_cndmask_b32 v136, 0, v133, vcc\n"
"s_waitcnt lgkmcnt(10)\n"
"v_mfma_f32_16x16x32_bf16 v[32:35], v[60:63], v[184:187], v[32:35]\n"
"ds_read_b64_tr_b16 v[84:85], v237 offset:36864\n"
"v_cmp_eq_u32 vcc, 1, v155\ns_nop 1\nv_cndmask_b32 v137, 0, v133, vcc\n"
"v_mfma_f32_16x16x32_bf16 v[36:39], v[60:63], v[188:191], v[36:39]\n"
"ds_read_b64_tr_b16 v[86:87], v237 offset:37376\n"
"s_waitcnt lgkmcnt(10)\n"
"v_mfma_f32_16x16x32_bf16 v[16:19], v[64:67], v[192:195], v[16:19]\n"
"s_waitcnt vmcnt(1) lgkmcnt(0)\n"
"s_barrier\n"
"ds_read_b64_tr_b16 v[52:53], v232 offset:0\n"
"v_cmp_eq_u32 vcc, 2, v155\ns_nop 1\nv_cndmask_b32 v138, 0, v133, vcc\n"
"v_cmp_eq_u32 vcc, 3, v155\ns_nop 1\nv_cndmask_b32 v139, 0, v133, vcc\n"
"v_mfma_f32_16x16x32_bf16 v[20:23], v[64:67], v[196:199], v[20:23]\n"
"ds_read_b64_tr_b16 v[54:55], v232 offset:512\n"
"v_cmp_eq_u32 vcc, 4, v155\ns_nop 1\nv_cndmask_b32 v140, 0, v133, vcc\n"
"v_cmp_eq_u32 vcc, 5, v155\ns_nop 1\nv_cndmask_b32 v141, 0, v133, vcc\n"
"v_mfma_f32_16x16x32_bf16 v[24:27], v[68:71], v[192:195], v[24:27]\n"
"ds_read_b64_tr_b16 v[56:57], v233 offset:0\n"
"v_cmp_eq_u32 vcc, 6, v155\ns_nop 1\nv_cndmask_b32 v142, 0, v133, vcc\n"
"v_cmp_eq_u32 vcc, 7, v155\ns_nop 1\nv_cndmask_b32 v143, 0, v133, vcc\n"
"v_mfma_f32_16x16x32_bf16 v[28:31], v[68:71], v[196:199], v[28:31]\n"
"ds_read_b64_tr_b16 v[58:59], v233 offset:512\n"
"v_lshrrev_b32 v133, 1, v130\n"
"v_and_b32 v148, v133, v132\n"
"v_mfma_f32_16x16x32_bf16 v[32:35], v[72:75], v[192:195], v[32:35]\n"
"ds_read_b64_tr_b16 v[60:61], v234 offset:0\n"
"v_lshlrev_b32 v133, 1, v133\n"
"v_mfma_f32_16x16x32_bf16 v[36:39], v[72:75], v[196:199], v[36:39]\n"
"ds_read_b64_tr_b16 v[62:63], v234 offset:512\n"
"v_mfma_f32_16x16x32_bf16 v[16:19], v[76:79], v[200:203], v[16:19]\n"
"ds_read_b64_tr_b16 v[64:65], v232 offset:12288\n"
"v_max_u32 v133, v133, v132\n"
"s_mul_i32 s40, s16, 3\n"
"v_mfma_f32_16x16x32_bf16 v[20:23], v[76:79], v[204:207], v[20:23]\n"
"ds_read_b64_tr_b16 v[66:67], v232 offset:12800\n"
"v_add_u32 v146, s40, v133\n"
"v_and_b32 v147, 1, v130\n"
"v_mfma_f32_16x16x32_bf16 v[24:27], v[80:83], v[200:203], v[24:27]\n"
"ds_read_b64_tr_b16 v[68:69], v233 offset:12288\n"
"v_xor_b32 v148, 1, v148\n"
"v_cvt_f32_u32 v148, v148\n"
"v_mfma_f32_16x16x32_bf16 v[28:31], v[80:83], v[204:207], v[28:31]\n"
"ds_read_b64_tr_b16 v[70:71], v233 offset:12800\n"
"v_and_b32 v132, 7, v1\n"
"s_lshl_b32 s40, s18, 9\n"
"v_mfma_f32_16x16x32_bf16 v[32:35], v[84:87], v[200:203], v[32:35]\n"
"ds_read_b64_tr_b16 v[72:73], v234 offset:12288\n"
"s_add_u32 s28, s4, s40\n"
"s_addc_u32 s29, s5, 0\n"
"v_mfma_f32_16x16x32_bf16 v[36:39], v[84:87], v[204:207], v[36:39]\n"
"ds_read_b64_tr_b16 v[74:75], v234 offset:12800\n"
"s_waitcnt lgkmcnt(10)\n"
"v_mfma_f32_16x16x32_bf16 v[16:19], v[52:55], v[208:211], v[16:19]\n"
"ds_read_b64_tr_b16 v[76:77], v232 offset:24576\n"
"v_mov_b32 v149, 0\n"
"v_mov_b32 v150, 1\n"
"v_mfma_f32_16x16x32_bf16 v[20:23], v[52:55], v[212:215], v[20:23]\n"
"ds_read_b64_tr_b16 v[78:79], v232 offset:25088\n"
"s_mul_i32 s40, s14, 0x6000\n"
"s_add_u32 s40, s40, 0x48e000\n"
"s_waitcnt lgkmcnt(10)\n"
"v_mfma_f32_16x16x32_bf16 v[24:27], v[56:59], v[208:211], v[24:27]\n"
"ds_read_b64_tr_b16 v[80:81], v233 offset:24576\n"
"s_add_u32 s34, s4, s40\n"
"s_addc_u32 s35, s5, 0\n"
"v_mfma_f32_16x16x32_bf16 v[28:31], v[56:59], v[212:215], v[28:31]\n"
"ds_read_b64_tr_b16 v[82:83], v233 offset:25088\n"
"s_mul_i32 s40, s16, 0xc00\n"
"v_lshl_add_u32 v151, v1, 4, s40\n"
"s_waitcnt lgkmcnt(10)\n"
"v_mfma_f32_16x16x32_bf16 v[32:35], v[60:63], v[208:211], v[32:35]\n"
"ds_read_b64_tr_b16 v[84:85], v234 offset:24576\n"
"s_lshl_b32 s40, s14, 3\n"
"v_mfma_f32_16x16x32_bf16 v[36:39], v[60:63], v[212:215], v[36:39]\n"
"ds_read_b64_tr_b16 v[86:87], v234 offset:25088\n"
"s_waitcnt lgkmcnt(10)\n"
"v_mfma_f32_16x16x32_bf16 v[16:19], v[64:67], v[216:219], v[16:19]\n"
"v_add_u32 v130, s40, v132\n"
"v_mul_u32_u24 v130, 0x480, v130\n"
"v_mfma_f32_16x16x32_bf16 v[20:23], v[64:67], v[220:223], v[20:23]\n"
"v_lshl_add_u32 v130, v146, 4, v130\n"
"v_lshlrev_b32 v131, 5, v146\n"
"s_waitcnt lgkmcnt(8)\n"
"v_mfma_f32_16x16x32_bf16 v[24:27], v[68:71], v[216:219], v[24:27]\n"
"v_lshl_add_u32 v131, v147, 4, v131\n"
"global_load_dwordx4 v[100:103], v151, s[34:35] offset:0 sc1\n"
"v_mfma_f32_16x16x32_bf16 v[28:31], v[68:71], v[220:223], v[28:31]\n"
"global_load_dwordx4 v[104:107], v151, s[34:35] offset:1024 sc1\n"
"global_load_dwordx4 v[108:111], v151, s[34:35] offset:2048 sc1\n"
"s_waitcnt lgkmcnt(6)\n"
"v_mfma_f32_16x16x32_bf16 v[32:35], v[72:75], v[216:219], v[32:35]\n"
"s_add_u32 s36, s4, 0x9580\n"
"s_addc_u32 s37, s5, 0\n"
"v_mfma_f32_16x16x32_bf16 v[36:39], v[72:75], v[220:223], v[36:39]\n"
"s_waitcnt lgkmcnt(4)\n"
"v_mfma_f32_16x16x32_bf16 v[16:19], v[76:79], v[224:227], v[16:19]\n"
"global_load_dwordx4 v[88:91], v131, s[36:37] offset:0 sc1\n"
"v_mfma_f32_16x16x32_bf16 v[20:23], v[76:79], v[228:231], v[20:23]\n"
"global_load_dwordx4 v[92:95], v131, s[36:37] offset:768 sc1\n"
"s_waitcnt lgkmcnt(2)\n"
"v_mfma_f32_16x16x32_bf16 v[24:27], v[80:83], v[224:227], v[24:27]\n"
"global_load_dwordx4 v[96:99], v131, s[36:37] offset:1536 sc1\n"
"v_mfma_f32_16x16x32_bf16 v[28:31], v[80:83], v[228:231], v[28:31]\n"
"v_add_u32 v153, 0x300, v130\n"
"s_waitcnt lgkmcnt(0)\n"
"v_mfma_f32_16x16x32_bf16 v[32:35], v[84:87], v[224:227], v[32:35]\n"
"v_mul_u32_u24 v131, 0x180, v147\n"
"v_mfma_f32_16x16x32_bf16 v[36:39], v[84:87], v[228:231], v[36:39]\n"
"v_add_u32 v152, v130, v131\n"
"v_cvt_pk_bf16_f32 v40, v16, v17\n"
"v_cvt_pk_bf16_f32 v41, v18, v19\n"
"v_cvt_pk_bf16_f32 v42, v24, v25\n"
"v_cvt_pk_bf16_f32 v43, v26, v27\n"
"v_cvt_pk_bf16_f32 v44, v20, v21\n"
"v_cvt_pk_bf16_f32 v45, v22, v23\n"
"v_cvt_pk_bf16_f32 v46, v28, v29\n"
"v_cvt_pk_bf16_f32 v47, v30, v31\n"
"v_cvt_pk_bf16_f32 v48, v32, v33\n"
"v_cvt_pk_bf16_f32 v49, v34, v35\n"
"v_cvt_pk_bf16_f32 v50, v36, v37\n"
"v_cvt_pk_bf16_f32 v51, v38, v39\n"
"v_permlane32_swap_b32 v40, v42\n"
"v_permlane32_swap_b32 v41, v43\n"
"v_permlane32_swap_b32 v44, v46\n"
"v_permlane32_swap_b32 v45, v47\n"
"v_permlane16_swap_b32 v48, v50\n"
"v_permlane16_swap_b32 v49, v51\n"
"global_store_dwordx4 v7, v[48:51], s[52:53] offset:3072 sc1\n"
"v_permlane16_swap_b32 v40, v42\n"
"v_permlane16_swap_b32 v41, v43\n"
"global_store_dwordx4 v6, v[40:43], s[52:53] offset:3072 sc1\n"
"v_permlane16_swap_b32 v44, v46\n"
"v_permlane16_swap_b32 v45, v47\n"
"global_store_dwordx4 v6, v[44:47], s[54:55] offset:3072 sc1\n"
"s_waitcnt vmcnt(0) lgkmcnt(0)\n"
"s_barrier\n"
"s_cmp_eq_u32 s16, 0\n"
"s_cselect_b64 exec, 1, 0\n"
"global_atomic_add v155, v149, v150, s[28:29] offset:128 sc0\n"
"s_mov_b64 exec, -1\n"
"s_waitcnt vmcnt(0)\n"
"v_readfirstlane_b32 s40, v155\n"
"s_nop 1\n"
"s_cmp_lg_u32 s16, 0\ns_cbranch_scc1 CW_OK8\ns_add_u32 s40, s40, 1\ns_cmp_eq_u32 s40, s96\ns_cbranch_scc1 CW_OK8\ns_mov_b32 s42, 0x40000\ns_mov_b64 exec, 1\nCW8:\ns_sleep 1\nglobal_atomic_add v155, v149, v149, s[28:29] offset:128 sc0\ns_waitcnt vmcnt(0)\nv_readfirstlane_b32 s40, v155\ns_sub_u32 s42, s42, 1\ns_cselect_b32 s40, s96, s40\ns_cmp_lg_u32 s40, s96\ns_cbranch_scc1 CW8\ns_mov_b64 exec, -1\nCW_OK8:\n"
"s_barrier\n"
"global_load_dwordx4 v[52:55], v144, s[20:21] offset:-3072 sc1\n"
"global_load_dwordx4 v[56:59], v144, s[22:23] offset:-3072 sc1\n"
"global_load_dwordx4 v[60:63], v145, s[20:21] offset:-3072 sc1\n"
"global_load_dwordx4 v[64:67], v144, s[20:21] offset:0 sc1\n"
"global_load_dwordx4 v[68:71], v144, s[22:23] offset:0 sc1\n"
"global_load_dwordx4 v[72:75], v145, s[20:21] offset:0 sc1\n"
"global_load_dwordx4 v[76:79], v144, s[20:21] offset:3072 sc1\n"
"global_load_dwordx4 v[80:83], v144, s[22:23] offset:3072 sc1\n"
"global_load_dwordx4 v[84:87], v145, s[20:21] offset:3072 sc1\n"
"s_waitcnt vmcnt(8)\n"
"v_mfma_f32_16x16x32_bf16 v[16:19], v[136:139], v[52:55], 0\n"
"s_waitcnt vmcnt(7)\n"
"v_mfma_f32_16x16x32_bf16 v[16:19], v[136:139], v[56:59], v[16:19]\n"
"s_waitcnt vmcnt(6)\n"
"v_mfma_f32_16x16x32_bf16 v[16:19], v[140:143], v[60:63], v[16:19]\n"
"s_waitcnt vmcnt(5)\n"
"v_mfma_f32_16x16x32_bf16 v[20:23], v[136:139], v[64:67], 0\n"
"s_waitcnt vmcnt(4)\n"
"v_mfma_f32_16x16x32_bf16 v[20:23], v[136:139], v[68:71], v[20:23]\n"
"s_waitcnt vmcnt(3)\n"
"v_mfma_f32_16x16x32_bf16 v[20:23], v[140:143], v[72:75], v[20:23]\n"
"s_waitcnt vmcnt(2)\n"
"v_mfma_f32_16x16x32_bf16 v[24:27], v[136:139], v[76:79], 0\n"
"s_waitcnt vmcnt(1)\n"
"v_mfma_f32_16x16x32_bf16 v[24:27], v[136:139], v[80:83], v[24:27]\n"
"s_waitcnt vmcnt(0)\n"
"v_mfma_f32_16x16x32_bf16 v[24:27], v[140:143], v[84:87], v[24:27]\n"
"v_add_f32_dpp v16, v16, v16 row_ror:8 row_mask:0xc bank_mask:0xf\n"
"v_add_f32_dpp v17, v17, v17 row_ror:8 row_mask:0xc bank_mask:0xf\n"
"v_add_f32_dpp v18, v18, v18 row_ror:8 row_mask:0xc bank_mask:0xf\n"
"v_add_f32_dpp v19, v19, v19 row_ror:8 row_mask:0xc bank_mask:0xf\n"
"v_add_f32_dpp v20, v20, v20 row_ror:8 row_mask:0xc bank_mask:0xf\n"
"v_add_f32_dpp v21, v21, v21 row_ror:8 row_mask:0xc bank_mask:0xf\n"
"v_add_f32_dpp v22, v22, v22 row_ror:8 row_mask:0xc bank_mask:0xf\n"
"v_add_f32_dpp v23, v23, v23 row_ror:8 row_mask:0xc bank_mask:0xf\n"
"v_add_f32_dpp v24, v24, v24 row_ror:8 row_mask:0xc bank_mask:0xf\n"
"v_add_f32_dpp v25, v25, v25 row_ror:8 row_mask:0xc bank_mask:0xf\n"
"v_add_f32_dpp v26, v26, v26 row_ror:8 row_mask:0xc bank_mask:0xf\n"
"v_add_f32_dpp v27, v27, v27 row_ror:8 row_mask:0xc bank_mask:0xf\n"
"v_fma_f32 v16, v88, v16, v100\n"
"v_fma_f32 v17, v89, v17, v101\n"
"v_fma_f32 v18, v90, v18, v102\n"
"v_fma_f32 v19, v91, v19, v103\n"
"v_fma_f32 v20, v92, v20, v104\n"
"v_fma_f32 v21, v93, v21, v105\n"
"v_fma_f32 v22, v94, v22, v106\n"
"v_fma_f32 v23, v95, v23, v107\n"
"v_fma_f32 v24, v96, v24, v108\n"
"v_fma_f32 v25, v97, v25, v109\n"
"v_fma_f32 v26, v98, v26, v110\n"
"v_fma_f32 v27, v99, v27, v111\n"
"v_cvt_pk_bf16_f32 v28, v16, v17\n"
"v_cvt_pk_bf16_f32 v29, v18, v19\n"
"v_cvt_pk_bf16_f32 v30, v20, v21\n"
"v_cvt_pk_bf16_f32 v31, v22, v23\n"
"v_cvt_pk_bf16_f32 v32, v24, v25\n"
"v_cvt_pk_bf16_f32 v33, v26, v27\n"
"v_mov_b32 v34, v32\n"
"v_mov_b32 v35, v33\n"
"v_permlane16_swap_b32 v28, v30\n"
"v_permlane16_swap_b32 v29, v31\n"
"global_store_dwordx4 v152, v[28:31], s[70:71] offset:0\n"
"v_permlane16_swap_b32 v32, v34\n"
"v_permlane16_swap_b32 v33, v35\n"
"global_store_dwordx4 v153, v[32:35], s[70:71] offset:0\n"
"s_branch K_END\n"
"F1_FALLBACK:\n"
"s_waitcnt vmcnt(0) lgkmcnt(0)\n"
"s_barrier\n"
"s_mov_b32 s27, 1\n"
"s_mov_b32 s26, 16\n"
"s_sub_u32 s30, s30, 0x20000\n"
"s_subb_u32 s31, s31, 0\n"
"s_sub_u32 s34, s34, 0x20000\n"
"s_subb_u32 s35, s35, 0\n"
"F1_FB_LOOP:\n"
"s_mov_b32 m0, s29\n"
"s_nop 0\n"
"global_load_lds_dwordx4 v240, s[30:31]\n"
"global_load_lds_dwordx4 v241, s[30:31] offset:1024\n"
"s_add_u32 s30, s30, 0x2000\n"
"s_addc_u32 s31, s31, 0\n"
"s_waitcnt vmcnt(0)\n"
"s_barrier\n"
"ds_read_b128 v[80:83], v228 offset:0\n"
"s_waitcnt lgkmcnt(0)\n"
"v_mfma_f32_16x16x32_bf16 v[112:115], v[80:83], v[176:179], 0\n"
"v_mfma_f32_16x16x32_bf16 v[116:119], v[80:83], v[192:195], 0\n"
"ds_read_b128 v[84:87], v229 offset:0\n"
"s_waitcnt lgkmcnt(0)\n"
"v_mfma_f32_16x16x32_bf16 v[112:115], v[84:87], v[180:183], v[112:115]\n"
"v_mfma_f32_16x16x32_bf16 v[116:119], v[84:87], v[196:199], v[116:119]\n"
"ds_read_b128 v[88:91], v230 offset:0\n"
"s_waitcnt lgkmcnt(0)\n"
"v_mfma_f32_16x16x32_bf16 v[112:115], v[88:91], v[184:187], v[112:115]\n"
"v_mfma_f32_16x16x32_bf16 v[116:119], v[88:91], v[200:203], v[116:119]\n"
"ds_read_b128 v[92:95], v231 offset:0\n"
"s_waitcnt lgkmcnt(0)\n"
"v_mfma_f32_16x16x32_bf16 v[112:115], v[92:95], v[188:191], v[112:115]\n"
"v_mfma_f32_16x16x32_bf16 v[116:119], v[92:95], v[204:207], v[116:119]\n"
"ds_read_b128 v[80:83], v228 offset:4096\n"
"s_waitcnt lgkmcnt(0)\n"
"v_mfma_f32_16x16x32_bf16 v[120:123], v[80:83], v[176:179], 0\n"
"v_mfma_f32_16x16x32_bf16 v[124:127], v[80:83], v[192:195], 0\n"
"ds_read_b128 v[84:87], v229 offset:4096\n"
"s_waitcnt lgkmcnt(0)\n"
"v_mfma_f32_16x16x32_bf16 v[120:123], v[84:87], v[180:183], v[120:123]\n"
"v_mfma_f32_16x16x32_bf16 v[124:127], v[84:87], v[196:199], v[124:127]\n"
"ds_read_b128 v[88:91], v230 offset:4096\n"
"s_waitcnt lgkmcnt(0)\n"
"v_mfma_f32_16x16x32_bf16 v[120:123], v[88:91], v[184:187], v[120:123]\n"
"v_mfma_f32_16x16x32_bf16 v[124:127], v[88:91], v[200:203], v[124:127]\n"
"ds_read_b128 v[92:95], v231 offset:4096\n"
"s_waitcnt lgkmcnt(0)\n"
"v_mfma_f32_16x16x32_bf16 v[120:123], v[92:95], v[188:191], v[120:123]\n"
"v_mfma_f32_16x16x32_bf16 v[124:127], v[92:95], v[204:207], v[124:127]\n"
"v_max3_f32 v172, v172, v112, v113\n"
"v_max3_f32 v172, v172, v114, v115\n"
"s_nop 4\n"
"v_max3_f32 v172, v172, v120, v121\n"
"v_max3_f32 v172, v172, v122, v123\n"
"v_max3_f32 v173, v173, v116, v117\n"
"v_max3_f32 v173, v173, v118, v119\n"
"v_max3_f32 v173, v173, v124, v125\n"
"v_max3_f32 v173, v173, v126, v127\n"
"s_barrier\n"
"s_sub_u32 s26, s26, 1\n"
"s_cmp_lg_u32 s26, 0\n"
"s_cbranch_scc1 F1_FB_LOOP\n"
"s_sub_u32 s30, s30, 0x20000\n"
"s_subb_u32 s31, s31, 0\n"
"s_mov_b32 m0, s29\n"
"s_nop 0\n"
"global_load_lds_dwordx4 v240, s[30:31]\n"
"global_load_lds_dwordx4 v241, s[30:31] offset:1024\n"
"s_add_u32 s30, s30, 0x2000\n"
"s_addc_u32 s31, s31, 0\n"
"s_add_u32 m0, s29, 0x8000\n"
"s_nop 0\n"
"global_load_lds_dwordx4 v240, s[30:31]\n"
"global_load_lds_dwordx4 v241, s[30:31] offset:1024\n"
"s_add_u32 s30, s30, 0x2000\n"
"s_addc_u32 s31, s31, 0\n"
"s_waitcnt vmcnt(0)\n"
"s_barrier\n"
"ds_read_b128 v[80:83], v228 offset:0\n"
"ds_read_b128 v[84:87], v229 offset:0\n"
"ds_read_b128 v[88:91], v230 offset:0\n"
"s_nop 7\n"
"s_branch F1_ATT\n"
"K_END:\n"
   : "+{v0}"(tid), "+{s[88:89]}"(c), "+{s[90:91]}"(wm)
   : "{s[4:5]}"(ws), "{s[6:7]}"(x), "{s[8:9]}"(wq), "{s[10:11]}"(kc), "{s[12:13]}"(vc), "{s14}"(bid), "{s3}"(lb),
     "{s[62:63]}"(wo), "{s[64:65]}"(wg), "{s[66:67]}"(wu), "{s[68:69]}"(wd), "{s[70:71]}"(out)
   : "v1","v2","v3","v4","v5","v6","v7","v8","v9","v10","v11","v12","v13","v14","v15","v16","v17","v18","v19","v20","v21","v22","v23","v24","v25","v26","v27","v28","v29","v30","v31","v32","v33","v34","v35","v36","v37","v38","v39","v40","v41","v42","v43","v44","v45","v46","v47","v48","v49","v50","v51","v52","v53","v54","v55","v56","v57","v58","v59","v60","v61","v62","v63","v64","v65","v66","v67","v68","v69","v70","v71","v72","v73","v74","v75","v76","v77","v78","v79","v80","v81","v82","v83","v84","v85","v86","v87","v88","v89","v90","v91","v92","v93","v94","v95","v96","v97","v98","v99","v100","v101","v102","v103","v104","v105","v106","v107","v108","v109","v110","v111","v112","v113","v114","v115","v116","v117","v118","v119","v120","v121","v122","v123","v124","v125","v126","v127","v128","v129","v130","v131","v132","v133","v134","v135","v136","v137","v138","v139","v140","v141","v142","v143","v144","v145","v146","v147","v148","v149","v150","v151","v152","v153","v154","v155","v156","v157","v158","v159","v160","v161","v162","v163","v164","v165","v166","v167","v168","v169","v170","v171","v172","v173","v174","v175","v176","v177","v178","v179","v180","v181","v182","v183","v184","v185","v186","v187","v188","v189","v190","v191","v192","v193","v194","v195","v196","v197","v198","v199","v200","v201","v202","v203","v204","v205","v206","v207","v208","v209","v210","v211","v212","v213","v214","v215","v216","v217","v218","v219","v220","v221","v222","v223","v224","v225","v226","v227","v228","v229","v230","v231","v232","v233","v234","v235","v236","v237","v238","v239","v240","v241","v242","v243","v244","v245","v246","v247","v248","v249","v250","v251","v252","v253","v254","v255","s15","s16","s17","s18","s19","s20","s21","s22","s23","s24","s25","s26","s27","s28","s29","s30","s31","s34","s35","s36","s37","s38","s39","s40","s41","s42","s43","s44","s45","s46","s47","s48","s49","s50","s51","s52","s53","s54","s55","s56","s57","s58","s59","s72","s73","s74","s75","s76","s77","s78","s79","s80","s81","s82","s83","s84","s85","s86","s87","s92","s93","s94","s95","s96","s97","s98","vcc","scc","memory");
}

extern "C" void kernel_launch(void* const* d_in,const int* in_sizes,int n_in,void* d_out,int out_size,void* d_ws,size_t ws_size,hipStream_t stream){
  k_main<<<256,512,0,stream>>>((char*)d_ws,(const u16*)d_in[0],d_in[3],d_in[1],d_in[2],d_in[4],d_in[6],d_in[7],d_in[8],d_out,(const u16*)d_in[9],(const u16*)d_in[10]);
}
```
